```python
import torch

def _softmax(x, axis=-1):
    return torch.softmax(x, dim=axis)
def _gelu(x):
    return torch.nn.functional.gelu(x, approximate="tanh")
def _silu(x):
    return torch.nn.functional.silu(x)
def _sigmoid(x):
    return torch.sigmoid(x)
def _layernorm(x, axis=-1, eps=1e-5):
    m = x.mean(dim=axis, keepdim=True)
    v = x.var(dim=axis, keepdim=True, unbiased=False)
    return (x - m) / torch.sqrt(v + eps)
def _scatter_add(tgt, idx, src, axis=0):
    return tgt.clone().index_add_(axis, idx, src)
def _segment_sum(src, seg, n):
    out = torch.zeros((n,) + src.shape[1:], dtype=src.dtype)
    return out.index_add_(0, seg, src)
def _pad(x, pw):
    flat = tuple(v for pair in reversed(pw) for v in pair)
    return torch.nn.functional.pad(x, flat)

kernel_name = 'parallel_sum__ssm_conv_s5__glu_gated_bilinear__prompt'

def setup_inputs():

    _w = (lambda a: (lambda t: t.to(torch.bfloat16) if t.is_floating_point() else t)(torch.as_tensor(a)))
    def _A_setup():
        B, S, D = 1, 2048, 640
        P = D
        Lambda_re = -0.5 * (lambda _x: torch.exp(torch.as_tensor(_x)))((lambda *s: torch.randn(*s, dtype=torch.bfloat16) * 0.2)(P))
        Lambda_im = (lambda *s: torch.randn(*s, dtype=torch.bfloat16) * 0.8)(P) * 1.0
        B_re = (lambda *s: torch.randn(*s, dtype=torch.bfloat16) * 0.2)(P, D) * (D ** -0.5)
        B_im = (lambda *s: torch.randn(*s, dtype=torch.bfloat16) * 0.2)(P, D) * (D ** -0.5)
        C_re = (lambda *s: torch.randn(*s, dtype=torch.bfloat16) * 0.2)(D, P) * (P ** -0.5)
        C_im = (lambda *s: torch.randn(*s, dtype=torch.bfloat16) * 0.2)(D, P) * (P ** -0.5)
        log_dt = (lambda *s: torch.randn(*s, dtype=torch.bfloat16) * 0.2)(P) * 0.3 - 2.0
        Dskip = (lambda *s: torch.randn(*s, dtype=torch.bfloat16) * 0.05)(D)
        return {
            "x": _w((lambda *s: torch.randn(*s, dtype=torch.bfloat16) * 0.05)(B, S, D)),
            "Lambda_re": _w(Lambda_re),
            "Lambda_im": _w(Lambda_im),
            "B_re": _w(B_re),
            "B_im": _w(B_im),
            "C_re": _w(C_re),
            "C_im": _w(C_im),
            "log_dt": _w(log_dt),
            "Dskip": _w(Dskip),
        }
    _A_in = _A_setup()

    def _B_setup():
        B, S, D, F = 1, 2048, 640, 1728
        return {
            "x": _w((lambda *s: torch.randn(*s, dtype=torch.bfloat16) * 0.05)(B, S, D)),
            "w_gate": _w((lambda *s: torch.randn(*s, dtype=torch.bfloat16) * 0.4)(D, F) * (D ** -0.5)),
            "w_up": _w((lambda *s: torch.randn(*s, dtype=torch.bfloat16) * 0.4)(D, F) * (D ** -0.5)),
            "w_down": _w((lambda *s: torch.randn(*s, dtype=torch.bfloat16) * 0.4)(F, D) * (F ** -0.5)),
        }
    _B_in = _B_setup()

    return {
        "A_x": _A_in["x"],
        "A_Lambda_re": _A_in["Lambda_re"],
        "A_Lambda_im": _A_in["Lambda_im"],
        "A_B_re": _A_in["B_re"],
        "A_B_im": _A_in["B_im"],
        "A_C_re": _A_in["C_re"],
        "A_C_im": _A_in["C_im"],
        "A_log_dt": _A_in["log_dt"],
        "A_Dskip": _A_in["Dskip"],
        "B_x": _B_in["x"],
        "B_w_gate": _B_in["w_gate"],
        "B_w_up": _B_in["w_up"],
        "B_w_down": _B_in["w_down"],
    }

def reference(A_x, A_Lambda_re, A_Lambda_im, A_B_re, A_B_im, A_C_re, A_C_im, A_log_dt, A_Dskip, B_x, B_w_gate, B_w_up, B_w_down):
    def _A_ref(x, Lambda_re, Lambda_im, B_re, B_im, C_re, C_im, log_dt, Dskip):
        Bsz, L, H = x.shape
        Lam = Lambda_re + 1j * Lambda_im
        Bmat = B_re + 1j * B_im
        Cmat = C_re + 1j * C_im
        dt = torch.exp(log_dt)
        Lam_bar = torch.exp(dt * Lam)
        B_bar = ((Lam_bar - 1.0) / Lam)[:, None] * Bmat
        Bu = torch.einsum("bld,pd->blp", x + 0j, B_bar)
        h = torch.zeros_like(Bu[:, 0, :])
        hs = []
        for t in range(L):
            h = Lam_bar[None, :] * h + Bu[:, t, :]
            hs.append(h)
        hseq = torch.stack(hs, 1)
        y = 2.0 * torch.einsum("blp,dp->bld", hseq, Cmat).real + x * Dskip[None, None, :]
        return y

    def _B_ref(x, w_gate, w_up, w_down):
        g = torch.einsum("bsd,df->bsf", x, w_gate)
        u = torch.einsum("bsd,df->bsf", x, w_up)
        h = g * u
        return torch.einsum("bsf,fd->bsd", h, w_down)

    x = A_x
    xn = _layernorm(x, axis=-1)
    return x + _A_ref(xn, A_Lambda_re, A_Lambda_im, A_B_re, A_B_im, A_C_re, A_C_im, A_log_dt, A_Dskip) + _B_ref(xn, B_w_gate, B_w_up, B_w_down)
```

```cpp
#include <hip/hip_runtime.h>
#include <hip/hip_bf16.h>
#include <stdint.h>

typedef unsigned short u16;
__device__ __forceinline__ float bf2f(u16 b) { return __uint_as_float(((unsigned)b) << 16); }
__device__ __forceinline__ u16 f2bf(float f) { unsigned u = __float_as_uint(f); u += 0x7FFFu + ((u >> 16) & 1u); return (u16)(u >> 16); }
__device__ __forceinline__ float rbf(float f) { return bf2f(f2bf(f)); }

constexpr int L = 2048, D = 640, P = 640, F = 1728;

__global__ void k_ln(const u16* x, float* xn) {
  int t = blockIdx.x; __shared__ float red[256];
  float s = 0; for (int d = threadIdx.x; d < D; d += 256) s += bf2f(x[t * D + d]);
  red[threadIdx.x] = s; __syncthreads();
  for (int o = 128; o > 0; o >>= 1) { if (threadIdx.x < o) red[threadIdx.x] += red[threadIdx.x + o]; __syncthreads(); }
  float mean = red[0] / D; __syncthreads();
  s = 0; for (int d = threadIdx.x; d < D; d += 256) { float v = bf2f(x[t * D + d]) - mean; s += v * v; }
  red[threadIdx.x] = s; __syncthreads();
  for (int o = 128; o > 0; o >>= 1) { if (threadIdx.x < o) red[threadIdx.x] += red[threadIdx.x + o]; __syncthreads(); }
  float var = rbf(red[0] / D); float m = rbf(mean);
  float sd = rbf(sqrtf(rbf(var + 1e-5f)));
  for (int d = threadIdx.x; d < D; d += 256) xn[t * D + d] = rbf(rbf(bf2f(x[t * D + d]) - m) / sd);
}
__global__ void k_bu(const float* xn, const u16* Bre, const u16* Bim, const u16* lre, const u16* lim, const u16* ldt, float2* Bu) {
  int t = blockIdx.x; __shared__ float xs[D];
  for (int d = threadIdx.x; d < D; d += blockDim.x) xs[d] = xn[t * D + d];
  __syncthreads();
  for (int p = threadIdx.x; p < P; p += blockDim.x) {
    float ar = 0, ai = 0;
    for (int d = 0; d < D; ++d) { ar += xs[d] * bf2f(Bre[p * D + d]); ai += xs[d] * bf2f(Bim[p * D + d]); }
    float dt = rbf(expf(bf2f(ldt[p]))); float lr = bf2f(lre[p]), li = bf2f(lim[p]);
    float e = expf(dt * lr); float br = e * cosf(dt * li), bi = e * sinf(dt * li);
    float nr = br - 1.f, ni = bi; float den = lr * lr + li * li;
    float cr = (nr * lr + ni * li) / den, ci = (ni * lr - nr * li) / den;
    Bu[t * P + p] = make_float2(cr * ar - ci * ai, cr * ai + ci * ar);
  }
}
__global__ void k_scan(float2* Bu, const u16* lre, const u16* lim, const u16* ldt) {
  int p = blockIdx.x * blockDim.x + threadIdx.x; if (p >= P) return;
  float dt = rbf(expf(bf2f(ldt[p]))); float lr = bf2f(lre[p]), li = bf2f(lim[p]);
  float e = expf(dt * lr); float br = e * cosf(dt * li), bi = e * sinf(dt * li);
  float hr = 0, hi = 0;
  for (int t = 0; t < L; ++t) { float2 u = Bu[t * P + p]; float nr = br * hr - bi * hi + u.x, ni = br * hi + bi * hr + u.y; hr = nr; hi = ni; Bu[t * P + p] = make_float2(hr, hi); }
}
__global__ void k_glu(const float* xn, const u16* wg, const u16* wu, float* H) {
  int t = blockIdx.x; __shared__ float xs[D];
  for (int d = threadIdx.x; d < D; d += blockDim.x) xs[d] = xn[t * D + d];
  __syncthreads();
  for (int f = threadIdx.x; f < F; f += blockDim.x) {
    float g = 0, u = 0;
    for (int d = 0; d < D; ++d) { g += xs[d] * bf2f(wg[d * F + f]); u += xs[d] * bf2f(wu[d * F + f]); }
    H[t * F + f] = rbf(rbf(g) * rbf(u));
  }
}
__global__ void k_out(const u16* x, const float* xn, const float2* h, const float* H, const u16* Cre, const u16* Cim, const u16* Dsk, const u16* wd, float* out) {
  int t = blockIdx.x; __shared__ float2 hs[P]; __shared__ float Hs[F];
  for (int p = threadIdx.x; p < P; p += blockDim.x) hs[p] = h[t * P + p];
  for (int f = threadIdx.x; f < F; f += blockDim.x) Hs[f] = H[t * F + f];
  __syncthreads();
  for (int d = threadIdx.x; d < D; d += blockDim.x) {
    float y = 0; for (int p = 0; p < P; ++p) y += hs[p].x * bf2f(Cre[d * P + p]) - hs[p].y * bf2f(Cim[d * P + p]);
    float z = 0; for (int f = 0; f < F; ++f) z += Hs[f] * bf2f(wd[f * D + d]);
    float a = 2.f * y + rbf(xn[t * D + d] * bf2f(Dsk[d]));
    out[t * D + d] = bf2f(x[t * D + d]) + a + rbf(z);
  }
}

extern "C" void kernel_launch(void* const* d_in, const int* in_sizes, int n_in, void* d_out, int out_size, void* d_ws, size_t ws_size, hipStream_t stream) {
  const u16* x = (const u16*)d_in[0]; const u16* lre = (const u16*)d_in[1]; const u16* lim = (const u16*)d_in[2];
  const u16* Bre = (const u16*)d_in[3]; const u16* Bim = (const u16*)d_in[4]; const u16* Cre = (const u16*)d_in[5]; const u16* Cim = (const u16*)d_in[6];
  const u16* ldt = (const u16*)d_in[7]; const u16* Dsk = (const u16*)d_in[8];
  const u16* wg = (const u16*)d_in[10]; const u16* wu = (const u16*)d_in[11]; const u16* wd = (const u16*)d_in[12];
  float* xn = (float*)d_ws; float2* Bu = (float2*)(xn + L * D); float* H = (float*)(Bu + L * P);
  k_ln<<<L, 256, 0, stream>>>(x, xn);
  k_bu<<<L, 256, 0, stream>>>(xn, Bre, Bim, lre, lim, ldt, Bu);
  k_scan<<<P / 64, 64, 0, stream>>>(Bu, lre, lim, ldt);
  k_glu<<<L, 256, 0, stream>>>(xn, wg, wu, H);
  k_out<<<L, 256, 0, stream>>>(x, xn, Bu, H, Cre, Cim, Dsk, wd, (float*)d_out);
}
```

```cpp
#include <hip/hip_runtime.h>
#include <stdint.h>

typedef unsigned short u16;
typedef unsigned v4u __attribute__((ext_vector_type(4)));
typedef unsigned v2u __attribute__((ext_vector_type(2)));
typedef float f32x4 __attribute__((ext_vector_type(4)));
typedef float f32x2 __attribute__((ext_vector_type(2)));
typedef short bf16x8 __attribute__((ext_vector_type(8)));
typedef short s16x4 __attribute__((ext_vector_type(4)));
typedef __bf16 bf2 __attribute__((ext_vector_type(2)));
#define LAS __attribute__((address_space(3)))
#define DI __device__ __forceinline__

constexpr int L_ = 2048, D_ = 640, P_ = 640, F_ = 1728;
constexpr int NKB = 94;
constexpr unsigned WS_EPOCH = 0;
constexpr unsigned WS_STATS = 4096;
constexpr unsigned WS_AGG = WS_STATS + 32768;
constexpr unsigned WS_CTL_BYTES = WS_AGG + 8 * 640 * 16;
constexpr unsigned WS_W2 = 1u << 20;
constexpr unsigned WS_W2_BYTES = 8 * NKB * 5120;
constexpr unsigned WS_H = 8u << 20;
constexpr unsigned WS_H_BYTES = 32 * NKB * 4096;

constexpr int STG = 53248;
constexpr int LDS_SPARE = 3 * STG;
constexpr int LDS1 = 163840;
constexpr int L_STATS = LDS_SPARE;
constexpr int L_CS = LDS_SPARE + 2048;
constexpr int L_CST = 49152;
constexpr int L_AGGW = 0;
constexpr int L_CWG = 2048;

DI float bf2f(unsigned b) { return __uint_as_float(b << 16); }
DI float bflo(unsigned w) { return __uint_as_float(w << 16); }
DI float bfhi(unsigned w) { return __uint_as_float(w & 0xffff0000u); }
DI unsigned pk2(float a, float b) { f32x2 v = {a, b}; return __builtin_bit_cast(unsigned, __builtin_convertvector(v, bf2)); }
DI float rbf(float a) { return bflo(pk2(a, a) & 0xffffu); }
DI f32x4 mfma(v4u a, v4u b, f32x4 c) {
  return __builtin_amdgcn_mfma_f32_16x16x32_bf16(__builtin_bit_cast(bf16x8, a), __builtin_bit_cast(bf16x8, b), c, 0, 0, 0);
}
DI unsigned rfl(unsigned v) { return __builtin_amdgcn_readfirstlane(v); }
DI __amdgpu_buffer_rsrc_t mkrs(const void* p, unsigned bytes) { return __builtin_amdgcn_make_buffer_rsrc((void*)p, 0, bytes, 0x00020000); }
DI void glds(__amdgpu_buffer_rsrc_t rs, unsigned ldsaddr, unsigned voff, unsigned soff) {
  asm volatile("s_mov_b32 m0, %0\n\ts_nop 0\n\tbuffer_load_dwordx4 %1, %2, %3 offen lds" ::"s"(ldsaddr), "v"(voff), "s"(rs), "s"(soff) : "memory");
}
DI v4u lds128(unsigned a) { return *(LAS v4u*)(uintptr_t)a; }
DI f32x4 ldsf4(unsigned a) { return *(LAS f32x4*)(uintptr_t)a; }
DI void stsf4(unsigned a, f32x4 v) { *(LAS f32x4*)(uintptr_t)a = v; }
DI v4u ldstr(unsigned a0, unsigned a1) {
  s16x4 lo = __builtin_amdgcn_ds_read_tr16_b64_v4i16((LAS s16x4*)(uintptr_t)a0);
  s16x4 hi = __builtin_amdgcn_ds_read_tr16_b64_v4i16((LAS s16x4*)(uintptr_t)a1);
  return __builtin_bit_cast(v4u, __builtin_shufflevector(lo, hi, 0, 1, 2, 3, 4, 5, 6, 7));
}
#define BAR() asm volatile("s_waitcnt lgkmcnt(0)\n\ts_barrier" ::: "memory")
template <int N> DI float dpp_shr(float v) {
  return __int_as_float(__builtin_amdgcn_update_dpp(0, __float_as_int(v), 0x110 + N, 0xf, 0xf, true));
}
template <int N> DI f32x4 dpp_shr4(f32x4 v) { return f32x4{dpp_shr<N>(v.x), dpp_shr<N>(v.y), dpp_shr<N>(v.z), dpp_shr<N>(v.w)}; }
DI float wave_sum(float v) {
#pragma unroll
  for (int o = 32; o > 0; o >>= 1) v += __shfl_xor(v, o);
  return v;
}
DI void ssm_par(const u16* lre, const u16* lim, const u16* ldt, int p, float& br, float& bi, float& cr, float& ci) {
  const float dt = rbf(expf(bf2f(ldt[p]))), lr = bf2f(lre[p]), li = bf2f(lim[p]);
  const float e = expf(dt * lr);
  float sn, cs;
  sincosf(dt * li, &sn, &cs);
  br = e * cs; bi = e * sn;
  const float nr = br - 1.f, ni = bi, den = lr * lr + li * li;
  cr = (nr * lr + ni * li) / den; ci = (ni * lr - nr * li) / den;
}

#define STAMP(ws, k, i)
struct P1 {
  const u16 *x, *lre, *lim, *bre, *bim, *cre, *cim, *ldt, *wg, *wu, *wd;
  unsigned char* ws;
};

template <int MODE>
DI void k1_body(const P1& p, const int R, const int g, const int wgi, const int wm, const int lane) {
  constexpr bool SSM = MODE >= 2, LOADER = (MODE & 1) != 0;
  constexpr int NB = MODE == 0 ? 3 : 2;
  constexpr int NCS = SSM ? 4 : 5;
  constexpr int NW = SSM ? 4 : 5;
  const int c = lane & 15, q = lane >> 4;
  const int wn = LOADER ? 1 : 0;
  const int tid = (wn * 4 + wm) * 64 + lane;
  const __amdgpu_buffer_rsrc_t rctl = mkrs(p.ws, WS_CTL_BYTES);
  const unsigned tag = *(const unsigned*)(p.ws + WS_EPOCH) * 0x9E3779B1u + 0x7F4A7C15u;

  STAMP(p.ws, 0, 0);
  __amdgpu_buffer_rsrc_t rx = mkrs(p.x, L_ * D_ * 2), rw = rx;
  unsigned voffX[2] = {0, 0}, voffW[5] = {0, 0, 0, 0, 0}, soX = 0, soW = 0, ldW = 0;
  if constexpr (LOADER) {
    voffX[0] = (4 * (lane >> 3)) * 1280 + (((lane & 7) ^ (lane >> 4)) * 16);
    voffX[1] = (4 * (lane >> 3)) * 1280 + (((lane & 7) ^ (4 + (lane >> 4))) * 16);
    soX = (256 * R + 64 * wm) * 1280;
    if constexpr (SSM) {
      rw = mkrs((wm >> 1) ? p.bim : p.bre, P_ * D_ * 2);
      voffW[0] = (lane >> 3) * 1280 + (((lane & 7) ^ (lane >> 4)) * 16);
      voffW[1] = (lane >> 3) * 1280 + (((lane & 7) ^ (4 + (lane >> 4))) * 16);
      soW = (64 * g + 32 * (wm & 1)) * 1280;
      ldW = 32768 + (wm >> 1) * 8192 + (wm & 1) * 4096;
    } else {
      rw = mkrs((wm >> 1) ? p.wu : p.wg, D_ * F_ * 2);
#pragma unroll
      for (int i = 0; i < 5; ++i) {
        const int lc = 64 * (5 * (wm & 1) + i) + lane, pos = lc / 10, ci = lc - pos * 10, k = pos ^ (4 * ((pos >> 3) & 1));
        voffW[i] = k * 3456 + ci * 16;
      }
      soW = 160 * g;
      ldW = 32768 + (wm >> 1) * 10240 + (wm & 1) * 5120;
    }
  }
  auto issue = [&](int st, unsigned sb) {
#pragma unroll
    for (int pp = 0; pp < 8; ++pp)
      glds(rx, sb + wm * 8192 + pp * 1024, voffX[pp & 1], soX + (32 * (pp & 1) + (pp >> 1)) * 1280 + st * 128);
#pragma unroll
    for (int i = 0; i < NW; ++i) {
      if constexpr (SSM) glds(rw, sb + ldW + i * 1024, voffW[i & 1], soW + i * 8 * 1280 + st * 128);
      else glds(rw, sb + ldW + i * 1024, voffW[i], soW + st * (64 * 3456));
    }
  };
  if constexpr (LOADER) { asm volatile("s_nop 4" ::: "memory"); issue(0, 0); issue(1, STG); }

  v4u sx4[2] = {}; unsigned sx1[2] = {0, 0};
  v4u jb[8] = {};
  v4u stv = {};
  const int srow = 256 * R + 8 * wgi + 2 * wm;
  const int gi = g * 8 + R;
  if constexpr (!LOADER) {
#pragma unroll
    for (int i = 0; i < 2; ++i) {
      sx4[i] = *(const v4u*)(p.x + (srow + i) * D_ + lane * 8);
      sx1[i] = *(const unsigned*)(p.x + (srow + i) * D_ + 512 + lane * 2);
    }
  }

  const unsigned xa0 = (64 * wm + c) * 128 + ((q ^ (c >> 1)) * 16), xa1 = xa0 ^ 64;
  unsigned wa0, wa1, wb0 = 0, wb1 = 0, ca0 = 0, ca1 = 0;
  const bool ev = (g & 1) == 0;
  if constexpr (SSM) {
    wa0 = 32768 + (32 * wn + c) * 128 + ((q ^ (c >> 1)) * 16); wa1 = wa0 ^ 64;
    ca0 = 32768 + (wm >> 1) * 8192 + c * 128 + (((4 * (wm & 1) + q) ^ (c >> 1)) * 16);
  } else {
    const unsigned h0 = (8 * q + 4 * (q & 1) + (c >> 2)) * 160 + (c & 3) * 8, h1 = (8 * q + 4 * ((q & 1) ^ 1) + (c >> 2)) * 160 + (c & 3) * 8;
    const unsigned tp = LOADER ? (ev ? 0 : 3) : (ev ? 2 : 1), ts = ev ? 4 : 0;
    wa0 = 32768 + h0 + tp * 32; wa1 = 32768 + h1 + tp * 32;
    wb0 = 32768 + h0 + ts * 32; wb1 = 32768 + h1 + ts * 32;
    ca0 = 32768 + (wm >> 1) * 10240 + (wm & 1) * 5120 + h0; ca1 = 32768 + (wm >> 1) * 10240 + (wm & 1) * 5120 + h1;
  }

  f32x4 acc[NB][2][4];
  f32x4 acs[NCS];
#pragma unroll
  for (int t = 0; t < NB; ++t)
#pragma unroll
    for (int m = 0; m < 2; ++m)
#pragma unroll
      for (int j = 0; j < 4; ++j) acc[t][m][j] = f32x4{0.f, 0.f, 0.f, 0.f};
#pragma unroll
  for (int t = 0; t < NCS; ++t) acs[t] = f32x4{0.f, 0.f, 0.f, 0.f};
  const v4u ones = {0x3F803F80u, 0x3F803F80u, 0x3F803F80u, 0x3F803F80u};

  unsigned sb = 0;
  for (int i = 0; i < 10; ++i) {
    if constexpr (LOADER) {
      if (i < 9) { if constexpr (SSM) asm volatile("s_waitcnt vmcnt(12)" ::: "memory"); else asm volatile("s_waitcnt vmcnt(13)" ::: "memory"); }
      else asm volatile("s_waitcnt vmcnt(0)" ::: "memory");
    }
    BAR();
    if constexpr (!LOADER) STAMP(p.ws, 0, 1 + i);
    if constexpr (LOADER) {
      if (i < 8) issue(i + 2, sb == 0 ? 2 * STG : sb - STG);
    }
#pragma unroll
    for (int kk = 0; kk < 2; ++kk) {
      v4u xf[4];
#pragma unroll
      for (int j = 0; j < 4; ++j) xf[j] = lds128(sb + (kk ? xa1 : xa0) + j * 2048);
#pragma unroll
      for (int t = 0; t < NB; ++t)
#pragma unroll
        for (int m = 0; m < 2; ++m) {
          v4u wf;
          if constexpr (SSM) wf = lds128(sb + (kk ? wa1 : wa0) + m * 8192 + t * 2048);
          else if (t < 2) wf = ldstr(sb + wa0 + kk * 5120 + m * 10240 + t * 32, sb + wa1 + kk * 5120 + m * 10240 + t * 32);
          else wf = ldstr(sb + wb0 + kk * 5120 + m * 10240, sb + wb1 + kk * 5120 + m * 10240);
#pragma unroll
          for (int j = 0; j < 4; ++j) acc[t][m][j] = mfma(wf, xf[j], acc[t][m][j]);
        }
    }
    if constexpr (LOADER) {
#pragma unroll
      for (int t = 0; t < NCS; ++t) {
        v4u wf;
        if constexpr (SSM) wf = lds128(sb + ca0 + t * 2048);
        else wf = ldstr(sb + ca0 + t * 32, sb + ca1 + t * 32);
        acs[t] = mfma(wf, ones, acs[t]);
      }
    } else {
      if (i == 1) {
#pragma unroll
        for (int r = 0; r < 2; ++r) {
          float v[10] = {bflo(sx4[r].x), bfhi(sx4[r].x), bflo(sx4[r].y), bfhi(sx4[r].y), bflo(sx4[r].z), bfhi(sx4[r].z), bflo(sx4[r].w), bfhi(sx4[r].w), bflo(sx1[r]), bfhi(sx1[r])};
          float s = 0.f;
#pragma unroll
          for (int e = 0; e < 10; ++e) s += v[e];
          const float mean = wave_sum(s) * (1.f / D_);
          float s2 = 0.f;
#pragma unroll
          for (int e = 0; e < 10; ++e) s2 += (v[e] - mean) * (v[e] - mean);
          const float var = rbf(wave_sum(s2) * (1.f / D_));
          const float sd = rbf(sqrtf(rbf(var + 1e-5f)));
          if (lane == 0) {
            v4u o = {__float_as_uint(rbf(mean)), tag, __float_as_uint(1.f / sd), tag};
            __builtin_amdgcn_raw_buffer_store_b128(o, rctl, WS_STATS + (srow + r) * 16, 0, 16);
          }
        }
        if constexpr (SSM) {
          if (wm == 0) {
            float br, bi, cr, ci;
            ssm_par(p.lre, p.lim, p.ldt, 64 * g + lane, br, bi, cr, ci);
            LAS float* cst = (LAS float*)(uintptr_t)L_CST;
            cst[lane] = br; cst[64 + lane] = bi;
            int n = 1;
#pragma unroll
            for (int s = 1; s <= 8; ++s) {
              const float nr = br * br - bi * bi, ni = 2.f * br * bi;
              br = nr; bi = ni;
              if (s >= 2 && s != 7) { cst[n * 128 + lane] = br; cst[n * 128 + 64 + lane] = bi; ++n; }
            }
          }
        } else {
          const int t = wm * 64 + lane;
          if (gi < 100) {
            const int off = (64 * (gi % 10) + (t >> 3)) * P_ + 64 * (gi / 10) + 8 * (t & 7);
            jb[0] = *(const v4u*)(p.cre + off); jb[1] = *(const v4u*)(p.cim + off);
            jb[2] = *(const v4u*)(p.cre + off + 32 * P_); jb[3] = *(const v4u*)(p.cim + off + 32 * P_);
          } else if (gi < 168) {
            const int id = min((gi - 100) * 256 + t, 17279), bq = id / 80, o = id - bq * 80;
            const u16* src = p.wd + (32 * (bq >> 2) + 4 * (bq & 3)) * D_ + 8 * o;
#pragma unroll
            for (int j = 0; j < 8; ++j) jb[j] = *(const v4u*)(src + (16 * (j >> 2) + (j & 3)) * D_);
          }
        }
      }
      if (i == 8) stv = __builtin_amdgcn_raw_buffer_load_b128(rctl, WS_STATS + (256 * R + wm * 64 + lane) * 16, 0, 16);
    }
    sb = (sb == 2 * STG) ? 0 : sb + STG;
  }

  STAMP(p.ws, 0, 11);
  if constexpr (LOADER) {
    if (c == 0) {
#pragma unroll
      for (int t = 0; t < NCS; ++t) stsf4(L_CS + ((wm & 1) * 160 + (wm >> 1) * 80 + t * 16 + 4 * q) * 4, acs[t]);
    }
  } else {
    for (int spin = 0; spin < (1 << 20); ++spin) {
      if (__all(stv.y == tag && stv.w == tag)) break;
      __builtin_amdgcn_s_sleep(1);
      stv = __builtin_amdgcn_raw_buffer_load_b128(rctl, WS_STATS + (256 * R + wm * 64 + lane) * 16, 0, 16);
    }
    *(LAS v2u*)(uintptr_t)(L_STATS + (wm * 64 + lane) * 8) = v2u{stv.x, stv.z};
  }
  BAR();
  STAMP(p.ws, 0, 12);
  float mu[4], rs[4];
  {
    const f32x4 s0 = ldsf4(L_STATS + (64 * wm + 4 * c) * 8), s1 = ldsf4(L_STATS + (64 * wm + 4 * c) * 8 + 16);
    mu[0] = s0.x; rs[0] = s0.y; mu[1] = s0.z; rs[1] = s0.w; mu[2] = s1.x; rs[2] = s1.y; mu[3] = s1.z; rs[3] = s1.w;
  }
  const __amdgpu_buffer_rsrc_t rh = mkrs(p.ws + WS_H + (unsigned)(4 * R + wm) * (NKB * 4096), NKB * 4096);

  if constexpr (!SSM) {
    const int tp = LOADER ? (ev ? 0 : 3) : (ev ? 2 : 1), ts = ev ? 4 : 0;
    unsigned hp[NB][4][2];
#pragma unroll
    for (int t = 0; t < NB; ++t) {
      const int tl = t < 2 ? tp + t : ts;
      const f32x4 cg = ldsf4(L_CS + (tl * 16 + 4 * q) * 4) + ldsf4(L_CS + (160 + tl * 16 + 4 * q) * 4);
      const f32x4 cu = ldsf4(L_CS + (80 + tl * 16 + 4 * q) * 4) + ldsf4(L_CS + (240 + tl * 16 + 4 * q) * 4);
#pragma unroll
      for (int j = 0; j < 4; ++j) {
        const f32x4 h = (acc[t][0][j] - mu[j] * cg) * (acc[t][1][j] - mu[j] * cu) * (rs[j] * rs[j]);
        hp[t][j][0] = pk2(h.x, h.y); hp[t][j][1] = pk2(h.z, h.w);
      }
    }
    const int Tp = 5 * g + tp;
    if (Tp < 108) {
#pragma unroll
      for (int j = 0; j < 4; ++j)
        __builtin_amdgcn_raw_buffer_store_b128(v4u{hp[0][j][0], hp[0][j][1], hp[1][j][0], hp[1][j][1]}, rh, lane * 16 + j * 1024 + (Tp >> 1) * 4096, 0, 0);
    }
    if constexpr (NB == 3) {
      const int Ts = 5 * g + ts;
#pragma unroll
      for (int j = 0; j < 4; ++j)
        __builtin_amdgcn_raw_buffer_store_b64(v2u{hp[2][j][0], hp[2][j][1]}, rh, lane * 16 + j * 1024 + (Ts >> 1) * 4096 + (Ts & 1) * 8, 0, 0);
      const int t = wm * 64 + lane;
      const __amdgpu_buffer_rsrc_t r2 = mkrs(p.ws + WS_W2, WS_W2_BYTES);
      if (gi < 100) {
        float br, bi, cr, ci;
        ssm_par(p.lre, p.lim, p.ldt, 64 * (gi / 10) + lane, br, bi, cr, ci);
        const int o = t & 7;
        float kr[8], ki[8];
#pragma unroll
        for (int e = 0; e < 8; ++e) { kr[e] = 2.f * __shfl(cr, 8 * o + e); ki[e] = 2.f * __shfl(ci, 8 * o + e); }
#pragma unroll
        for (int hf = 0; hf < 2; ++hf) {
          const v4u a = jb[2 * hf], b = jb[2 * hf + 1];
          const float ar[8] = {bflo(a.x), bfhi(a.x), bflo(a.y), bfhi(a.y), bflo(a.z), bfhi(a.z), bflo(a.w), bfhi(a.w)};
          const float ai[8] = {bflo(b.x), bfhi(b.x), bflo(b.y), bfhi(b.y), bflo(b.z), bfhi(b.z), bflo(b.w), bfhi(b.w)};
          float zr[8], zi[8];
#pragma unroll
          for (int e = 0; e < 8; ++e) { zr[e] = ar[e] * kr[e] - ai[e] * ki[e]; zi[e] = -(ar[e] * ki[e] + ai[e] * kr[e]); }
          const int d = 64 * (gi % 10) + (t >> 3) + 32 * hf, ct = d / 80, it = (d - ct * 80) >> 4;
          const unsigned off = ((ct * NKB + 54 + 4 * (gi / 10) + (o >> 1)) * 5 + it) * 1024 + (2 * (o & 1) * 16 + (d & 15)) * 16;
          __builtin_amdgcn_raw_buffer_store_b128(v4u{pk2(zr[0], zr[1]), pk2(zr[2], zr[3]), pk2(zi[0], zi[1]), pk2(zi[2], zi[3])}, r2, off, 0, 0);
          __builtin_amdgcn_raw_buffer_store_b128(v4u{pk2(zr[4], zr[5]), pk2(zr[6], zr[7]), pk2(zi[4], zi[5]), pk2(zi[6], zi[7])}, r2, off + 256, 0, 0);
        }
      } else if (gi < 168) {
        const int id = (gi - 100) * 256 + t;
        if (id < 17280) {
          const int bq = id / 80, o = id - bq * 80, d0 = 8 * o, ct = d0 / 80, it = (d0 - ct * 80) >> 4;
          const unsigned off = ((ct * NKB + (bq >> 2)) * 5 + it) * 1024 + ((bq & 3) * 16 + (d0 & 15)) * 16;
#pragma unroll
          for (int e = 0; e < 8; ++e) {
            v4u o4;
#pragma unroll
            for (int jj = 0; jj < 4; ++jj) {
              const unsigned lo = jb[2 * jj][e >> 1], hi = jb[2 * jj + 1][e >> 1];
              o4[jj] = (e & 1) ? __builtin_amdgcn_perm(hi, lo, 0x07060302u) : __builtin_amdgcn_perm(hi, lo, 0x05040100u);
            }
            __builtin_amdgcn_raw_buffer_store_b128(o4, r2, off + e * 16, 0, 0);
          }
        }
      } else if (gi == 175) {
        for (int spin = 0; spin < (1 << 20); ++spin) {
          const v4u s = __builtin_amdgcn_raw_buffer_load_b128(rctl, WS_STATS + (8 * t + 7) * 16, 0, 16);
          if (__all(s.y == tag)) break;
          __builtin_amdgcn_s_sleep(1);
        }
        LAS unsigned* cnt = (LAS unsigned*)(uintptr_t)(L_CS + 1280);
        (void)cnt;
      }
    }
    if (gi == 175) {
      BAR();
      if (tid == 0) __builtin_amdgcn_raw_buffer_store_b32(tag, rctl, WS_EPOCH, 0, 16);
    }
  } else {
    const unsigned cb = L_CST + (32 * wn + 4 * q) * 4;
    f32x4 er[2], ei[2];
#pragma unroll
    for (int t = 0; t < 2; ++t) {
      const int tl = 2 * wn + t;
      const f32x4 cr4 = ldsf4(L_CS + (tl * 16 + 4 * q) * 4) + ldsf4(L_CS + (160 + tl * 16 + 4 * q) * 4);
      const f32x4 ci4 = ldsf4(L_CS + (80 + tl * 16 + 4 * q) * 4) + ldsf4(L_CS + (240 + tl * 16 + 4 * q) * 4);
#pragma unroll
      for (int j = 0; j < 4; ++j) {
        acc[t][0][j] = (acc[t][0][j] - mu[j] * cr4) * rs[j];
        acc[t][1][j] = (acc[t][1][j] - mu[j] * ci4) * rs[j];
      }
      const f32x4 lr = ldsf4(cb + t * 64), li = ldsf4(cb + t * 64 + 256);
      f32x4 a = acc[t][0][0], b = acc[t][1][0];
#pragma unroll
      for (int j = 1; j < 4; ++j) {
        const f32x4 na = lr * a - li * b + acc[t][0][j], nb = lr * b + li * a + acc[t][1][j];
        a = na; b = nb;
      }
      { const f32x4 mr = ldsf4(cb + t * 64 + 512), mi = ldsf4(cb + t * 64 + 768), sa = dpp_shr4<1>(a), sbb = dpp_shr4<1>(b);
        a += mr * sa - mi * sbb; b += mr * sbb + mi * sa; }
      { const f32x4 mr = ldsf4(cb + t * 64 + 1024), mi = ldsf4(cb + t * 64 + 1280), sa = dpp_shr4<2>(a), sbb = dpp_shr4<2>(b);
        a += mr * sa - mi * sbb; b += mr * sbb + mi * sa; }
      { const f32x4 mr = ldsf4(cb + t * 64 + 1536), mi = ldsf4(cb + t * 64 + 1792), sa = dpp_shr4<4>(a), sbb = dpp_shr4<4>(b);
        a += mr * sa - mi * sbb; b += mr * sbb + mi * sa; }
      { const f32x4 mr = ldsf4(cb + t * 64 + 2048), mi = ldsf4(cb + t * 64 + 2304), sa = dpp_shr4<8>(a), sbb = dpp_shr4<8>(b);
        a += mr * sa - mi * sbb; b += mr * sbb + mi * sa; }
      er[t] = a; ei[t] = b;
      if (c == 15) { stsf4(L_AGGW + (wm * 128 + tl * 16 + 4 * q) * 4, a); stsf4(L_AGGW + (wm * 128 + 64 + tl * 16 + 4 * q) * 4, b); }
    }
    BAR();
    STAMP(p.ws, 0, 13);
    v4u pv[7] = {};
    const bool poller = !LOADER && wm == 0;
    if (poller) {
      LAS float* ag = (LAS float*)(uintptr_t)L_AGGW;
      LAS float* cst = (LAS float*)(uintptr_t)L_CST;
      const float lr = cst[5 * 128 + lane], li = cst[5 * 128 + 64 + lane];
      float a = ag[lane], b = ag[64 + lane];
#pragma unroll
      for (int w = 1; w < 4; ++w) { const float na = lr * a - li * b + ag[w * 128 + lane], nb = lr * b + li * a + ag[w * 128 + 64 + lane]; a = na; b = nb; }
      if (R < 7) __builtin_amdgcn_raw_buffer_store_b128(v4u{__float_as_uint(a), tag, __float_as_uint(b), tag}, rctl, WS_AGG + (R * 640 + 64 * g + lane) * 16, 0, 16);
#pragma unroll
      for (int r = 0; r < 7; ++r)
        if (r < R) pv[r] = __builtin_amdgcn_raw_buffer_load_b128(rctl, WS_AGG + (r * 640 + 64 * g + lane) * 16, 0, 16);
    }
    f32x4 pr[2], pi[2];
#pragma unroll
    for (int t = 0; t < 2; ++t) {
      const int tl = 2 * wn + t;
      f32x4 a = {1.f, 1.f, 1.f, 1.f}, b = {0.f, 0.f, 0.f, 0.f};
#pragma unroll
      for (int s = 0; s < 4; ++s) {
        f32x4 mr = ldsf4(cb + t * 64 + 512 * (s + 1)), mi = ldsf4(cb + t * 64 + 512 * (s + 1) + 256);
        if (!((c >> s) & 1)) { mr = f32x4{1.f, 1.f, 1.f, 1.f}; mi = f32x4{0.f, 0.f, 0.f, 0.f}; }
        const f32x4 na = a * mr - b * mi, nb = a * mi + b * mr;
        a = na; b = nb;
      }
      const f32x4 lr = ldsf4(cb + t * 64 + 512 * 5), li = ldsf4(cb + t * 64 + 512 * 5 + 256);
      f32x4 ia = {0.f, 0.f, 0.f, 0.f}, ib = ia, qa = a, qb = b;
      for (int w = 0; w < wm; ++w) {
        const f32x4 ga = ldsf4(L_AGGW + (w * 128 + tl * 16 + 4 * q) * 4), gb = ldsf4(L_AGGW + (w * 128 + 64 + tl * 16 + 4 * q) * 4);
        const f32x4 na = lr * ia - li * ib + ga, nb = lr * ib + li * ia + gb;
        ia = na; ib = nb;
        const f32x4 ra = lr * qa - li * qb, rb = lr * qb + li * qa;
        qa = ra; qb = rb;
      }
      er[t] = dpp_shr4<1>(er[t]) + a * ia - b * ib;
      ei[t] = dpp_shr4<1>(ei[t]) + a * ib + b * ia;
      pr[t] = qa; pi[t] = qb;
    }
    if (poller) {
      for (int spin = 0; spin < (1 << 20); ++spin) {
        bool ok = true;
#pragma unroll
        for (int r = 0; r < 7; ++r) if (r < R) ok = ok && pv[r].y == tag && pv[r].w == tag;
        if (__all(ok)) break;
        __builtin_amdgcn_s_sleep(1);
#pragma unroll
        for (int r = 0; r < 7; ++r)
          if (r < R) pv[r] = __builtin_amdgcn_raw_buffer_load_b128(rctl, WS_AGG + (r * 640 + 64 * g + lane) * 16, 0, 16);
      }
      LAS float* cst = (LAS float*)(uintptr_t)L_CST;
      const float lr = cst[6 * 128 + lane], li = cst[6 * 128 + 64 + lane];
      float a = 0.f, b = 0.f;
#pragma unroll
      for (int r = 0; r < 7; ++r)
        if (r < R) { const float na = lr * a - li * b + __uint_as_float(pv[r].x), nb = lr * b + li * a + __uint_as_float(pv[r].z); a = na; b = nb; }
      LAS float* cw = (LAS float*)(uintptr_t)L_CWG;
      cw[lane] = a; cw[64 + lane] = b;
    }
    BAR();
    STAMP(p.ws, 0, 14);
#pragma unroll
    for (int t = 0; t < 2; ++t) {
      const int tl = 2 * wn + t;
      const f32x4 ca = ldsf4(L_CWG + (tl * 16 + 4 * q) * 4), cbb = ldsf4(L_CWG + (64 + tl * 16 + 4 * q) * 4);
      const f32x4 lr = ldsf4(cb + t * 64), li = ldsf4(cb + t * 64 + 256);
      f32x4 a = er[t] + pr[t] * ca - pi[t] * cbb, b = ei[t] + pr[t] * cbb + pi[t] * ca;
#pragma unroll
      for (int j = 0; j < 4; ++j) {
        const f32x4 na = lr * a - li * b + acc[t][0][j], nb = lr * b + li * a + acc[t][1][j];
        a = na; b = nb;
        __builtin_amdgcn_raw_buffer_store_b128(v4u{pk2(a.x, a.y), pk2(a.z, a.w), pk2(b.x, b.y), pk2(b.z, b.w)}, rh, lane * 16 + j * 1024 + (54 + 4 * g + tl) * 4096, 0, 0);
      }
    }
  }
  STAMP(p.ws, 0, 15);
}

__global__ __launch_bounds__(512, 2) void k1(P1 p) {
  const int bid = blockIdx.x, xcd = bid & 7, idx = bid >> 3;
  const int R = 4 * (xcd >> 2) + (idx & 3), s = idx >> 2, slot = (xcd & 3) + 4 * (s >> 1);
  const int wgi = (xcd & 3) * 8 + s;
  const int w = rfl(threadIdx.x >> 6), lane = threadIdx.x & 63;
  if (slot < 5) {
    const int g = 2 * slot + (s & 1);
    if (w < 4) k1_body<2>(p, R, g, wgi, w, lane); else k1_body<3>(p, R, g, wgi, w - 4, lane);
  } else {
    const int g = 2 * (slot - 5) + (s & 1);
    if (w < 4) k1_body<0>(p, R, g, wgi, w, lane); else k1_body<1>(p, R, g, wgi, w - 4, lane);
  }
}

constexpr int DEPTH = 4;
__global__ __launch_bounds__(256, 1) void k2(const u16* x, const u16* dsk, const unsigned char* ws, float* out) {
  extern __shared__ __attribute__((aligned(16))) unsigned char smem[];
  const int bid = blockIdx.x, xcd = bid & 7, idx = bid >> 3;
  const int rt = (xcd >> 1) * 8 + (idx >> 2), ct = (xcd & 1) * 4 + (idx & 3);
  const int lane = threadIdx.x & 63, w = rfl(threadIdx.x >> 6);
  const __amdgpu_buffer_rsrc_t rh = mkrs(ws + WS_H + (unsigned)rt * (NKB * 4096), NKB * 4096);
  const __amdgpu_buffer_rsrc_t rw = mkrs(ws + WS_W2 + (unsigned)ct * (NKB * 5120), NKB * 5120);
  STAMP((unsigned char*)ws, 1, 0);
  constexpr int NIT = (NKB + 3) / 4;
  f32x4 acc[4][5];
#pragma unroll
  for (int j = 0; j < 4; ++j)
#pragma unroll
    for (int i = 0; i < 5; ++i) acc[j][i] = f32x4{0.f, 0.f, 0.f, 0.f};
  v4u hb[DEPTH][4], wb[DEPTH][5];
  const int vo = lane * 16;
  auto load = [&](int it, int s) {
    const int kb = it * 4 + w;
#pragma unroll
    for (int j = 0; j < 4; ++j) hb[s][j] = __builtin_amdgcn_raw_buffer_load_b128(rh, vo + j * 1024, kb * 4096, 0);
#pragma unroll
    for (int i = 0; i < 5; ++i) wb[s][i] = __builtin_amdgcn_raw_buffer_load_b128(rw, vo + i * 1024, kb * 5120, 0);
  };
#pragma unroll
  for (int d = 0; d < DEPTH; ++d) load(d, d);
  const int tok = rt * 64 + 4 * (lane & 15) + w, d0 = ct * 80 + (lane >> 4) * 4;
  v2u xv[5], dv[5];
#pragma unroll
  for (int i = 0; i < 5; ++i) { xv[i] = *(const v2u*)(x + tok * D_ + d0 + i * 16); dv[i] = *(const v2u*)(dsk + d0 + i * 16); }
  const v4u st = *(const v4u*)(ws + WS_STATS + tok * 16);
  __builtin_amdgcn_sched_barrier(0);
#pragma unroll
  for (int it = 0; it < NIT; ++it) {
    const int s = it % DEPTH;
#pragma unroll
    for (int j = 0; j < 4; ++j)
#pragma unroll
      for (int i = 0; i < 5; ++i) acc[j][i] = mfma(wb[s][i], hb[s][j], acc[j][i]);
    __builtin_amdgcn_sched_barrier(0);
    if (it + DEPTH < NIT) load(it + DEPTH, s);
    __builtin_amdgcn_sched_barrier(0);
  }
  STAMP((unsigned char*)ws, 1, 1);
  f32x4* red = (f32x4*)smem;
#pragma unroll
  for (int j = 0; j < 4; ++j)
#pragma unroll
    for (int i = 0; i < 5; ++i) red[(w * 20 + j * 5 + i) * 64 + lane] = acc[j][i];
  __syncthreads();
  const float mu = __uint_as_float(st.x), rs = __uint_as_float(st.z);
#pragma unroll
  for (int i = 0; i < 5; ++i) {
    f32x4 s = (red[(0 * 20 + w * 5 + i) * 64 + lane] + red[(1 * 20 + w * 5 + i) * 64 + lane]) + (red[(2 * 20 + w * 5 + i) * 64 + lane] + red[(3 * 20 + w * 5 + i) * 64 + lane]);
    const f32x4 xf = {bflo(xv[i].x), bfhi(xv[i].x), bflo(xv[i].y), bfhi(xv[i].y)};
    const f32x4 df = {bflo(dv[i].x), bfhi(dv[i].x), bflo(dv[i].y), bfhi(dv[i].y)};
    s += xf + (xf - mu) * rs * df;
    *(f32x4*)(out + tok * D_ + d0 + i * 16) = s;
  }
  STAMP((unsigned char*)ws, 1, 2);
}

extern "C" void kernel_launch(void* const* d_in, const int* in_sizes, int n_in, void* d_out, int out_size, void* d_ws, size_t ws_size, hipStream_t stream) {
  P1 p;
  p.x = (const u16*)d_in[0]; p.lre = (const u16*)d_in[1]; p.lim = (const u16*)d_in[2];
  p.bre = (const u16*)d_in[3]; p.bim = (const u16*)d_in[4]; p.cre = (const u16*)d_in[5]; p.cim = (const u16*)d_in[6];
  p.ldt = (const u16*)d_in[7]; p.wg = (const u16*)d_in[10]; p.wu = (const u16*)d_in[11]; p.wd = (const u16*)d_in[12];
  p.ws = (unsigned char*)d_ws;
  (void)hipFuncSetAttribute((const void*)k1, hipFuncAttributeMaxDynamicSharedMemorySize, LDS1);
  (void)hipFuncSetAttribute((const void*)k2, hipFuncAttributeMaxDynamicSharedMemorySize, 81920);
  k1<<<256, 512, LDS1, stream>>>(p);
  k2<<<256, 256, 81920, stream>>>((const u16*)d_in[0], (const u16*)d_in[8], (const unsigned char*)d_ws, (float*)d_out);
}
```

```cpp
#include <hip/hip_runtime.h>
#include <stdint.h>

typedef unsigned short u16;
typedef unsigned v4u __attribute__((ext_vector_type(4)));
typedef unsigned v2u __attribute__((ext_vector_type(2)));
typedef float f32x4 __attribute__((ext_vector_type(4)));
typedef float f32x2 __attribute__((ext_vector_type(2)));
typedef float f32x16 __attribute__((ext_vector_type(16)));
typedef short bf16x8 __attribute__((ext_vector_type(8)));
typedef short s16x4 __attribute__((ext_vector_type(4)));
typedef __bf16 bf2 __attribute__((ext_vector_type(2)));
#define LAS __attribute__((address_space(3)))
#define DI __device__ __forceinline__
typedef LAS unsigned char* lp;

constexpr int L_ = 2048, D_ = 640, P_ = 640, F_ = 1728;
constexpr int NKB = 94;
constexpr unsigned WS_EPOCH = 0;
constexpr unsigned WS_FLH = 1024;
constexpr unsigned WS_FLW = 2048;
constexpr unsigned WS_FLC = WS_FLW + 320;
constexpr unsigned WS_STATS = 4096;
constexpr unsigned WS_AGG = WS_STATS + 32768;
constexpr unsigned WS_CSG = WS_AGG + 8 * 640 * 16;
constexpr unsigned WS_CSS = WS_CSG + 3 * 3456 * 8;
constexpr unsigned WS_CTL_BYTES = WS_CSS + 1280 * 8;
constexpr unsigned WS_W2 = 1u << 20;
constexpr unsigned WS_W2_BYTES = 8 * NKB * 5120;
constexpr unsigned WS_H = 8u << 20;
constexpr unsigned WS_H_BYTES = 32 * NKB * 4096;

constexpr int UPO = 10368;
constexpr int STG = 32768 + 20480 + 128;
constexpr int LDS1 = 163840;
constexpr int L_STATS = 3 * STG;
constexpr int L_CS = L_STATS + 2048;
constexpr int L_TAG = L_CS + 640;
constexpr int L_CNT = L_TAG + 4;
constexpr int L_CST = 49152;
constexpr int L_W = STG;
constexpr int L_GO = L_CNT + 4, L_RDY = L_CNT + 8;
constexpr int L_FULL = L_TAG + 16, L_FREE = L_TAG + 32;
constexpr int L_E = 136000, ESTR = 528;
constexpr int SPIN = 1 << 18;
constexpr unsigned OOB = 0x40000000u;

DI float bf2f(unsigned b) { return __uint_as_float(b << 16); }
DI float bflo(unsigned w) { return __uint_as_float(w << 16); }
DI float bfhi(unsigned w) { return __uint_as_float(w & 0xffff0000u); }
DI unsigned pk2(float a, float b) { f32x2 v = {a, b}; return __builtin_bit_cast(unsigned, __builtin_convertvector(v, bf2)); }
DI float rbf(float a) { return bflo(pk2(a, a) & 0xffffu); }
DI f32x4 mfma16(v4u a, v4u b, f32x4 c) { return __builtin_amdgcn_mfma_f32_16x16x32_bf16(__builtin_bit_cast(bf16x8, a), __builtin_bit_cast(bf16x8, b), c, 0, 0, 0); }
DI f32x16 mfma32(v4u a, v4u b, f32x16 c) { return __builtin_amdgcn_mfma_f32_32x32x16_bf16(__builtin_bit_cast(bf16x8, a), __builtin_bit_cast(bf16x8, b), c, 0, 0, 0); }
DI unsigned rfl(unsigned v) { return __builtin_amdgcn_readfirstlane(v); }
DI __amdgpu_buffer_rsrc_t mkrs(const void* p, unsigned bytes) { return __builtin_amdgcn_make_buffer_rsrc((void*)p, 0, bytes, 0x00020000); }
DI void glds(__amdgpu_buffer_rsrc_t rs, unsigned ldsaddr, unsigned voff, unsigned soff) {
  asm volatile("s_mov_b32 m0, %0\n\ts_nop 0\n\tbuffer_load_dwordx4 %1, %2, %3 offen lds" ::"s"(ldsaddr), "v"(voff), "s"(rs), "s"(soff));
}
DI void gldsc(__amdgpu_buffer_rsrc_t rs, unsigned ldsaddr, unsigned voff, unsigned soff) {
  asm volatile("s_mov_b32 m0, %0\n\ts_nop 0\n\tbuffer_load_dwordx4 %1, %2, %3 offen sc1 lds" ::"s"(ldsaddr), "v"(voff), "s"(rs), "s"(soff));
}
DI lp LP(unsigned a) { return (lp)(uintptr_t)a; }
DI v4u l128(lp a) { return *(LAS v4u*)a; }
DI f32x4 lf4(lp a) { return *(LAS f32x4*)a; }
DI void sf4(lp a, f32x4 v) { *(LAS f32x4*)a = v; }
DI s16x4 ltr(lp a) { return __builtin_amdgcn_ds_read_tr16_b64_v4i16((LAS s16x4*)a); }
DI v4u cat(s16x4 lo, s16x4 hi) { return __builtin_bit_cast(v4u, __builtin_shufflevector(lo, hi, 0, 1, 2, 3, 4, 5, 6, 7)); }
DI v4u ld16(__amdgpu_buffer_rsrc_t r, unsigned vo, unsigned so) { return __builtin_amdgcn_raw_buffer_load_b128(r, vo, so, 0); }
DI v4u ld16c(__amdgpu_buffer_rsrc_t r, unsigned vo, unsigned so) { return __builtin_amdgcn_raw_buffer_load_b128(r, vo, so, 16); }
DI unsigned ld4c(__amdgpu_buffer_rsrc_t r, unsigned vo) { return __builtin_amdgcn_raw_buffer_load_b32(r, vo, 0, 16); }
DI void st16c(v4u v, __amdgpu_buffer_rsrc_t r, unsigned vo) { __builtin_amdgcn_raw_buffer_store_b128(v, r, vo, 0, 16); }
DI void st4c(unsigned v, __amdgpu_buffer_rsrc_t r, unsigned vo) { __builtin_amdgcn_raw_buffer_store_b32(v, r, vo, 0, 16); }
#define BAR() asm volatile("s_waitcnt lgkmcnt(0)\n\ts_barrier" ::: "memory")
#define SB0() __builtin_amdgcn_sched_barrier(0)
#define WAITV(n) asm volatile("s_waitcnt vmcnt(%0)" ::"i"(n) : "memory")
#define FENCE() asm volatile("" ::: "memory")
template <int C> DI float dppf(float v) { return __int_as_float(__builtin_amdgcn_update_dpp(0, __float_as_int(v), C, 0xf, 0xf, true)); }
template <int N> DI f32x4 shr4(f32x4 v) { return f32x4{dppf<0x110 + N>(v.x), dppf<0x110 + N>(v.y), dppf<0x110 + N>(v.z), dppf<0x110 + N>(v.w)}; }
DI float sum8(float v) {
  v += dppf<0xB1>(v); v += dppf<0x4E>(v); v += dppf<0x141>(v);
  return v;
}
DI float dot2(unsigned a, unsigned b, float c) { return __builtin_amdgcn_fdot2_f32_bf16(__builtin_bit_cast(bf2, a), __builtin_bit_cast(bf2, b), c, false); }
DI void gather4(float x, float (&t)[4]) {
  const auto a = __builtin_amdgcn_permlane16_swap(__float_as_uint(x), __float_as_uint(x), false, false);
  const auto e = __builtin_amdgcn_permlane32_swap(a[0], a[0], false, false), o = __builtin_amdgcn_permlane32_swap(a[1], a[1], false, false);
  t[0] = __uint_as_float(e[0]); t[2] = __uint_as_float(e[1]); t[1] = __uint_as_float(o[0]); t[3] = __uint_as_float(o[1]);
}
DI void cm1(float& a, float& b, float lr, float li, float ur, float ui) {
  const float na = fmaf(lr, a, fmaf(-li, b, ur)), nb = fmaf(lr, b, fmaf(li, a, ui));
  a = na; b = nb;
}
DI f32x4 fma4(f32x4 a, f32x4 b, f32x4 c) { return __builtin_elementwise_fma(a, b, c); }
DI f32x4 sp4(float a) { return f32x4{a, a, a, a}; }
DI void cmac(f32x4& a, f32x4& b, f32x4 lr, f32x4 li, f32x4 ur, f32x4 ui) {
  const f32x4 na = fma4(lr, a, fma4(-li, b, ur)), nb = fma4(lr, b, fma4(li, a, ui));
  a = na; b = nb;
}

#define STAMP(i)
#define STAMPC(i)

struct Par {
  const u16 *x, *lre, *lim, *bre, *bim, *cre, *cim, *ldt, *dsk, *wg, *wu, *wd;
  unsigned char* ws;
  float* out;
};
struct Ids { int R, g, cg, rt, ct; };

constexpr int DW = 4, DH = 3, NIT = 12, NL = 3;
#define PIN4(x) asm volatile("" : "+v"(x))
DI void sig(unsigned addr, unsigned v) {
  asm volatile("s_mov_b64 exec, 1\n\tds_write_b32 %0, %1\n\ts_mov_b64 exec, -1" ::"v"(addr), "v"(v) : "memory");
}
DI unsigned lrd(unsigned addr) { return *(volatile LAS unsigned*)LP(addr); }
#define LSLEEP 1
template <int SL = 1> DI void lwait(unsigned addr, unsigned v, unsigned need) {
  if (__builtin_expect(!__all(v >= need), 0)) {
    _Pragma("unroll 1") for (int spin = 0; spin < 64 * SPIN; ++spin) { FENCE();
      if (__all(lrd(addr) >= need)) break;
      __builtin_amdgcn_s_sleep(SL);
    }
  }
  FENCE();
}

template <bool SSM>
DI void compute_wave(const Par& p, const Ids& id, const int w, const int lane) {
  STAMP(0);
  const unsigned afull = L_FULL + (lane & 3) * 4, afree = L_FREE + w * 4;
  unsigned fv = 0;
  if constexpr (!SSM) {
    const int r = lane & 31, hh = lane >> 5;
    const unsigned xa = (64 * w + r) * 128 + ((hh ^ ((r >> 1) & 7)) * 16);
    const unsigned wa = 32768 + ((lane >> 4) & 1) * UPO + (8 * hh + ((lane & 15) >> 2)) * 160 + (lane & 3) * 8;
    f32x16 acc[5][2] = {};
#pragma unroll
    for (int e = 0; e < 5; ++e) { PIN4(acc[e][0]); PIN4(acc[e][1]); }
    v4u fx[2][2]; s16x4 fl[4], fh[4];
    lp px[4], pw;
    auto setp = [&](unsigned sb) {
#pragma unroll
      for (int s = 0; s < 4; ++s) px[s] = LP(sb + (xa ^ (32 * s)));
      pw = LP(sb + wa);
    };
    auto woff = [&](int n) { return (n / 5) * 2560 + (n % 5) * 32; };
    setp(0);
    SB0();
    BAR();
    lwait(afull, 0, 1);
    STAMP(1);
    fx[0][0] = l128(px[0]); fx[0][1] = l128(px[0] + 4096);
#pragma unroll
    for (int n = 0; n < 3; ++n) { fl[n] = ltr(pw + woff(n)); fh[n] = ltr(pw + woff(n) + 640); }
    unsigned sb = 0;
#pragma unroll 1
    for (int i = 0; i < 10; ++i) {
#pragma unroll
      for (int n = 0; n < 20; ++n) {
        const int ks = n / 5, e = n % 5, nn = (n + 3) % 20;
        if (n == 14) fv = lrd(afull);
        if (n == 17) { sig(afree, i + 1); lwait(afull, fv, i + 2); STAMP(2 + i); if (i == 3 || i == 7) STAMPC(24 + i); sb = sb == 2 * STG ? 0 : sb + STG; setp(sb); SB0(); }
        const v4u wf = cat(fl[n & 3], fh[n & 3]);
        acc[e][0] = mfma32(wf, fx[ks & 1][0], acc[e][0]); SB0();
        fl[(n + 3) & 3] = ltr(pw + woff(nn));
        fh[(n + 3) & 3] = ltr(pw + woff(nn) + 640); SB0();
        acc[e][1] = mfma32(wf, fx[ks & 1][1], acc[e][1]); SB0();
        if (ks < 3 && e == 1) { fx[(ks + 1) & 1][0] = l128(px[ks + 1]); fx[(ks + 1) & 1][1] = l128(px[ks + 1] + 4096); }
        if (n == 17) { fx[0][0] = l128(px[0]); fx[0][1] = l128(px[0] + 4096); }
        SB0();
      }
    }
    STAMP(12);
    const __amdgpu_buffer_rsrc_t rh = mkrs(p.ws + WS_H + (unsigned)(4 * id.R + w) * (NKB * 4096), 54 * 4096);
    float mu[2], r2[2];
#pragma unroll
    for (int u = 0; u < 2; ++u) {
      const int F = 4 * w + 2 * u + (r >> 4);
      const f32x2 s = *(LAS f32x2*)LP(L_STATS + (128 * (F >> 3) + 8 * (r & 15) + (F & 7)) * 8);
      mu[u] = s.x; r2[u] = s.y * s.y;
    }
    const lp pc = LP(L_CS + hh * 16);
    const unsigned so0 = (r >> 4) * 1024 + (hh * 16 + (r & 15)) * 16;
#pragma unroll
    for (int e = 0; e < 5; ++e) {
      const f32x4 g0 = lf4(pc + e * 64), g1 = lf4(pc + e * 64 + 32), u0 = lf4(pc + e * 64 + 320), u1 = lf4(pc + e * 64 + 352);
      const int T = 5 * id.g + e;
      const unsigned so = so0 + (T >> 1) * 4096 + (T & 1) * 512;
#pragma unroll
      for (int u = 0; u < 2; ++u) {
        const f32x16 a = acc[e][u];
        const f32x4 nm = sp4(-mu[u]);
        const f32x4 h0 = fma4(nm, g0, f32x4{a[0], a[1], a[2], a[3]}) * fma4(nm, u0, f32x4{a[8], a[9], a[10], a[11]}) * r2[u];
        const f32x4 h1 = fma4(nm, g1, f32x4{a[4], a[5], a[6], a[7]}) * fma4(nm, u1, f32x4{a[12], a[13], a[14], a[15]}) * r2[u];
        st16c(v4u{pk2(h0.x, h0.y), pk2(h0.z, h0.w), pk2(h1.x, h1.y), pk2(h1.z, h1.w)}, rh, so + u * 2048);
      }
    }
  } else {
    const int c = lane & 15, q = lane >> 4, G = w >> 1, blk = w & 1;
    const unsigned xa = (128 * G + c) * 128 + ((q ^ (c >> 1)) * 16);
    const unsigned wa = 32768 + (32 * blk + c) * 128 + ((q ^ (c >> 1)) * 16);
    f32x4 acc[8][4] = {};
#pragma unroll
    for (int j = 0; j < 8; ++j)
#pragma unroll
      for (int t = 0; t < 4; ++t) PIN4(acc[j][t]);
    v4u fw[2][4], fx[4];
    lp px[2], pw[2];
    auto setp = [&](unsigned sb) { px[0] = LP(sb + xa); px[1] = LP(sb + (xa ^ 64)); pw[0] = LP(sb + wa); pw[1] = LP(sb + (wa ^ 64)); };
    auto woff = [&](int t) { return (t >> 1) * 8192 + (t & 1) * 2048; };
    setp(0);
    SB0();
    BAR();
    lwait(afull, 0, 1);
    STAMP(1);
#pragma unroll
    for (int t = 0; t < 4; ++t) fw[0][t] = l128(pw[0] + woff(t));
#pragma unroll
    for (int n = 0; n < 3; ++n) fx[n] = l128(px[0] + n * 2048);
    unsigned sb = 0;
#pragma unroll 1
    for (int i = 0; i < 10; ++i) {
#pragma unroll
      for (int n = 0; n < 16; ++n) {
        const int kk = n >> 3, j = n & 7, nn = (n + 3) & 15;
        if (n == 10) fv = lrd(afull);
        if (n == 13) { sig(afree, i + 1); lwait(afull, fv, i + 2); STAMP(2 + i); if (i == 3 || i == 7) STAMPC(24 + i); sb = sb == 2 * STG ? 0 : sb + STG; setp(sb); SB0(); }
        const v4u xf = fx[n & 3];
        acc[j][0] = mfma16(fw[kk][0], xf, acc[j][0]); SB0();
        fx[(n + 3) & 3] = l128(px[nn >> 3] + (nn & 7) * 2048); SB0();
        acc[j][1] = mfma16(fw[kk][1], xf, acc[j][1]); SB0();
        if (n >= 2 && n < 6) fw[1][n - 2] = l128(pw[1] + woff(n - 2));
        if (n == 13) fw[0][0] = l128(pw[0] + woff(0));
        if (n == 14) fw[0][2] = l128(pw[0] + woff(2));
        SB0();
        acc[j][2] = mfma16(fw[kk][2], xf, acc[j][2]); SB0();
        if (n == 13) fw[0][1] = l128(pw[0] + woff(1));
        if (n == 14) fw[0][3] = l128(pw[0] + woff(3));
        SB0();
        acc[j][3] = mfma16(fw[kk][3], xf, acc[j][3]); SB0();
      }
    }
    STAMP(12);
    const lp ps = LP(L_STATS + (128 * G + 8 * c) * 8);
    const lp pc = LP(L_CS + (32 * blk + 8 * q) * 4);
    const lp pk = LP(L_CST + (32 * blk + 8 * q) * 4);
    const lp pe = LP(L_E + (16 * G + c) * ESTR + (32 * blk + 8 * q) * 4);
    f32x4 lr[2], li[2], er[2], ei[2];
    const unsigned frv = lrd(L_FREE + (lane & 3) * 4);
    {
      f32x4 cs[4], st[4];
#pragma unroll
      for (int t = 0; t < 4; ++t) { cs[t] = lf4(pc + (t >> 1) * 320 + (t & 1) * 16); st[t] = lf4(ps + t * 16); }
#pragma unroll
      for (int ab = 0; ab < 2; ++ab) { lr[ab] = lf4(pk + ab * 16); li[ab] = lf4(pk + 256 + ab * 16); }
#pragma unroll
      for (int jj = 0; jj < 4; ++jj) {
#pragma unroll
        for (int t = 0; t < 4; ++t) {
          acc[2 * jj][t] = fma4(sp4(-st[jj].x), cs[t], acc[2 * jj][t]) * st[jj].y;
          acc[2 * jj + 1][t] = fma4(sp4(-st[jj].z), cs[t], acc[2 * jj + 1][t]) * st[jj].w;
        }
      }
#pragma unroll
      for (int ab = 0; ab < 2; ++ab) { er[ab] = acc[0][ab]; ei[ab] = acc[0][2 + ab]; }
#pragma unroll
      for (int j = 1; j < 8; ++j)
#pragma unroll
        for (int ab = 0; ab < 2; ++ab) cmac(er[ab], ei[ab], lr[ab], li[ab], acc[j][ab], acc[j][2 + ab]);
      lwait(L_FREE + (lane & 3) * 4, frv, 9);
#pragma unroll
      for (int ab = 0; ab < 2; ++ab) { sf4(pe + ab * 16, er[ab]); sf4(pe + 256 + ab * 16, ei[ab]); }
    }
    SB0();
    BAR();
    STAMP(13);
#pragma unroll
    for (int ab = 0; ab < 2; ++ab) {
      const f32x4 kr = lf4(pk + 512 * 4 + ab * 16), ki = lf4(pk + 512 * 4 + 256 + ab * 16);
#pragma unroll
      for (int j = 0; j < 8; ++j) {
        const f32x4 ur = acc[j][ab], ui = acc[j][2 + ab];
        acc[j][ab] = fma4(-ki, ui, kr * ur); acc[j][2 + ab] = fma4(-ki, ur, -kr * ui);
        PIN4(acc[j][ab]); PIN4(acc[j][2 + ab]);
      }
      li[ab] = -li[ab];
    }
    const __amdgpu_buffer_rsrc_t rh = mkrs(p.ws + WS_H + (unsigned)(4 * id.R + 2 * G) * (NKB * 4096), 2 * NKB * 4096);
    unsigned vo[4];
#pragma unroll
    for (int z = 0; z < 4; ++z) { vo[z] = (54 + 2 * id.g + blk + 20 * (z & 1) + NKB * (z >> 1)) * 4096 + lane * 16; PIN4(vo[z]); }
    SB0();
    BAR();
    STAMP(14);
#pragma unroll
    for (int ab = 0; ab < 2; ++ab) { er[ab] = lf4(pe + ab * 16); ei[ab] = lf4(pe + 256 + ab * 16); }
#pragma unroll
    for (int j = 0; j < 8; ++j) {
#pragma unroll
      for (int ab = 0; ab < 2; ++ab) cmac(er[ab], ei[ab], lr[ab], li[ab], acc[j][ab], acc[j][2 + ab]);
      st16c(v4u{pk2(er[0].x, er[0].y), pk2(er[0].z, er[0].w), pk2(er[1].x, er[1].y), pk2(er[1].z, er[1].w)}, rh, vo[2 * (j >> 2)] + (j & 3) * 1024);
      st16c(v4u{pk2(ei[0].x, ei[0].y), pk2(ei[0].z, ei[0].w), pk2(ei[1].x, ei[1].y), pk2(ei[1].z, ei[1].w)}, rh, vo[2 * (j >> 2) + 1] + (j & 3) * 1024);
    }
  }
  SB0();
  STAMP(15);
}

template <bool SSM>
DI void helper_wave(const Par& p, const Ids& id, const int h, const int lane) {
  const __amdgpu_buffer_rsrc_t rctl = mkrs(p.ws, WS_CTL_BYTES);
  const int c = lane & 15, q = lane >> 4;
  const unsigned afree = L_FREE + (lane & 3) * 4, afull = L_FULL + h * 4;
  STAMP(0);
#define HBAR() do { SB0(); BAR(); SB0(); } while (0)
  if (h < 3) {
    if (h < 2) {
      const __amdgpu_buffer_rsrc_t rx = mkrs(p.x, L_ * D_ * 2);
      const unsigned v0 = (8 * (lane >> 3)) * 1280 + (((lane & 7) ^ (lane >> 4)) * 16), v1 = v0 ^ 64;
      unsigned so = (256 * id.R + 128 * h) * 1280;
      auto issue = [&](unsigned s, unsigned sb) {
#pragma unroll
        for (int n = 0; n < 16; ++n) glds(rx, sb + h * 16384 + n * 1024, (n & 1) ? v1 : v0, s + (64 * (n & 1) + (n >> 1)) * 1280);
      };
      asm volatile("s_nop 4");
      issue(so, 0);
      BAR();
      issue(so + 128, STG);
      so += 256;
      unsigned sb = 2 * STG;
#pragma unroll 1
      for (int j = 0; j < 8; ++j) {
        WAITV(16); sig(afull, j + 1);
        lwait<LSLEEP>(afree, lrd(afree), j);
        issue(so, sb);
        so += 128; sb = sb == 2 * STG ? 0 : sb + STG;
      }
      WAITV(16); sig(afull, 9);
    } else {
      constexpr int NV = SSM ? 2 : 10;
      const __amdgpu_buffer_rsrc_t ra = mkrs(SSM ? p.bre : p.wg, SSM ? P_ * D_ * 2 : D_ * F_ * 2), rb = mkrs(SSM ? p.bim : p.wu, SSM ? P_ * D_ * 2 : D_ * F_ * 2);
      unsigned vw[NV];
      if constexpr (SSM) {
        vw[0] = (8 * (lane >> 5) + ((lane >> 3) & 3)) * 1280 + (((lane & 7) ^ (lane >> 4)) * 16); vw[1] = vw[0] ^ 64;
      } else {
#pragma unroll
        for (int i = 0; i < 10; ++i) { const int lc = 64 * i + lane, pos = lc / 10; vw[i] = pos * 3456 + (lc - pos * 10) * 16; }
      }
      constexpr unsigned WSTEP = SSM ? 128u : 64u * 3456u;
      constexpr int NPW = SSM ? 16 : 20;
      unsigned so = SSM ? 64 * id.g * 1280 : 160 * id.g;
      auto issue = [&](unsigned s, unsigned sb) {
#pragma unroll
        for (int n = 0; n < NPW / 2; ++n) {
          if constexpr (SSM) {
            const unsigned o = s + (32 * (n >> 2) + 16 * (n & 1) + 4 * ((n >> 1) & 1)) * 1280;
            glds(ra, sb + 32768 + n * 1024, vw[n & 1], o); glds(rb, sb + 32768 + 8192 + n * 1024, vw[n & 1], o);
          } else {
            glds(ra, sb + 32768 + n * 1024, vw[n], s); glds(rb, sb + 32768 + UPO + n * 1024, vw[n], s);
          }
        }
      };
      asm volatile("s_nop 4");
      issue(so, 0);
      BAR();
      issue(so + WSTEP, STG);
      so += 2 * WSTEP;
      unsigned sb = 2 * STG;
#pragma unroll 1
      for (int j = 0; j < 8; ++j) {
        WAITV(NPW); sig(afull, j + 1);
        lwait<LSLEEP>(afree, lrd(afree), j);
        issue(so, sb);
        so += WSTEP; sb = sb == 2 * STG ? 0 : sb + STG;
      }
      WAITV(NPW); sig(afull, 9);
    }
    WAITV(0); sig(afull, 11);
  } else {
    if (lane < 12) *(LAS unsigned*)LP(L_TAG + lane * 4) = lane == 7 ? 10u : 0u;
    const int gi = id.g * 8 + id.R;
    const unsigned epoch = ld4c(rctl, WS_EPOCH);
    const int srow = 256 * id.R + 8 * id.cg + (lane >> 3);
    v4u sx[10];
#pragma unroll
    for (int i = 0; i < 10; ++i) sx[i] = *(const v4u*)(p.x + srow * D_ + (lane & 7) * 8 + i * 64);
    HBAR();
    const __amdgpu_buffer_rsrc_t r2 = mkrs(p.ws + WS_W2, WS_W2_BYTES);
    unsigned tag;
    auto stats = [&]() {
      tag = rfl(epoch) * 0x9E3779B1u + 0x7F4A7C15u;
      f32x4 s1 = {}, s2 = {};
#pragma unroll
      for (int i = 0; i < 10; ++i)
#pragma unroll
        for (int e = 0; e < 4; ++e) { s1[e] = dot2(sx[i][e], 0x3F803F80u, s1[e]); s2[e] = dot2(sx[i][e], sx[i][e], s2[e]); }
      const float mean = sum8((s1.x + s1.y) + (s1.z + s1.w)) * (1.f / D_);
      const float var = rbf(fmaxf(sum8((s2.x + s2.y) + (s2.z + s2.w)) * (1.f / D_) - mean * mean, 0.f));
      const float sd = rbf(sqrtf(rbf(var + 1e-5f)));
      st16c(v4u{__float_as_uint(rbf(mean)), tag, __float_as_uint(1.f / sd), tag}, rctl, (lane & 7) == 0 ? WS_STATS + srow * 16 : OOB);
      if (lane == 0) *(LAS unsigned*)LP(L_TAG) = tag;
      SB0();
    };
    if constexpr (SSM) {
      const __amdgpu_buffer_rsrc_t rd = mkrs(p.wd, F_ * D_ * 2);
      const unsigned rvo = (lane >> 3) * 1280 + (((lane & 7) ^ (2 * ((lane >> 4) & 1) + 4 * ((lane >> 5) & 1))) * 16);
      unsigned rta[4], rto[4];
      const int f0 = 16 * (q >> 1) + 4 * (q & 1) + (c >> 2);
#pragma unroll
      for (int i = 0; i < 4; ++i) {
        rta[i] = STG + 49152 + f0 * 128 + (((2 * i + ((c >> 1) & 1)) ^ (f0 & 6)) * 16) + (c & 1) * 8;
        const unsigned dt = 4 * id.g + i, ct = dt / 5;
        rto[i] = ((ct * NKB + id.R) * 5 + dt - 5 * ct) * 1024 + lane * 16;
      }
      auto rt_load = [&](int k) {
#pragma unroll
        for (int z = 0; z < 2; ++z) {
          const unsigned so = (id.R + 8 * (2 * k + z)) * (32 * 1280) + id.g * 128;
#pragma unroll
          for (int n = 0; n < 4; ++n) glds(rd, (z + 1) * STG + 49152 + n * 1024, rvo, so + n * (8 * 1280));
        }
        SB0();
      };
      v4u tf[8];
      auto rt_read = [&]() {
#pragma unroll
        for (int i = 0; i < 8; ++i) { const lp a = LP(rta[i & 3]) + (i >> 2) * STG; tf[i] = cat(ltr(a), ltr(a + 1024)); }
        asm volatile("s_waitcnt lgkmcnt(0)" : "+v"(tf[0]), "+v"(tf[1]), "+v"(tf[2]), "+v"(tf[3]), "+v"(tf[4]), "+v"(tf[5]), "+v"(tf[6]), "+v"(tf[7])::"memory");
      };
      auto rt_store = [&](int k) {
#pragma unroll
        for (int i = 0; i < 8; ++i) {
          const int n = 2 * k + (i >> 2);
          if (n < 7) st16c(tf[i], r2, id.R + 8 * n < 54 ? rto[i & 3] + n * (8 * 5120) : OOB);
        }
        SB0();
      };
      const unsigned pl = p.ldt[64 * id.g + lane], pr = p.lre[64 * id.g + lane], pi = p.lim[64 * id.g + lane];
      asm volatile("s_nop 4");
      rt_load(0);
      stats();
      v4u cw[20];
#pragma unroll
      for (int i = 0; i < 20; ++i) cw[i] = *(const v4u*)((i < 10 ? p.bre : p.bim) + (64 * id.g + 8 * id.R + (lane >> 3)) * D_ + (lane & 7) * 8 + (i % 10) * 64);
      SB0();
      WAITV(21); rt_read(); rt_load(1); rt_store(0);
      {
        const float dt = rbf(expf(bf2f(pl))), lr = bf2f(pr), li = bf2f(pi);
        const float ex = expf(dt * lr);
        float sn, cn;
        sincosf(dt * li, &sn, &cn);
        float br = ex * cn, bi = ex * sn;
        const float nr = br - 1.f, ni = bi, den = lr * lr + li * li;
        LAS float* cst = (LAS float*)LP(L_CST);
        cst[4 * 128 + lane] = 2.f * (nr * lr + ni * li) / den; cst[4 * 128 + 64 + lane] = 2.f * (ni * lr - nr * li) / den;
        cst[lane] = br; cst[64 + lane] = bi;
#pragma unroll
        for (int s = 1; s <= 8; ++s) {
          const float a = br * br - bi * bi, b = 2.f * br * bi;
          br = a; bi = b;
          const int n = s == 3 ? 1 : s == 6 ? 2 : s == 8 ? 3 : 0;
          if (n) { cst[n * 128 + lane] = br; cst[n * 128 + 64 + lane] = bi; }
        }
        SB0();
      }
      WAITV(8); rt_read(); rt_load(2); rt_store(1);
      {
        f32x4 a = {};
#pragma unroll
        for (int i = 0; i < 20; ++i)
#pragma unroll
          for (int e = 0; e < 4; ++e) a[2 * (i / 10) + (e & 1)] = dot2(cw[i][e], 0x3F803F80u, a[2 * (i / 10) + (e & 1)]);
        const float s0 = sum8(a.x + a.y), s1 = sum8(a.z + a.w);
        const unsigned row = 64 * id.g + 8 * id.R + (lane >> 3);
        __builtin_amdgcn_raw_buffer_store_b64(v2u{__float_as_uint((lane & 1) ? s1 : s0), tag}, rctl, (lane & 6) == 0 ? WS_CSS + ((lane & 1) * 640 + row) * 8 : OOB, 0, 16);
        SB0();
      }
      WAITV(9); rt_read(); rt_load(3); rt_store(2);
      WAITV(8); rt_read(); rt_store(3);
      WAITV(0);
      st4c(tag, rctl, lane == 0 ? WS_FLW + gi * 4 : OOB);
    } else {
      stats();
      v4u cf[10];
      const __amdgpu_buffer_rsrc_t rcc = mkrs((gi >> 2) < 20 ? p.cre : p.cim, gi < 160 ? D_ * P_ * 2 : 0);
#pragma unroll
      for (int i = 0; i < 10; ++i) cf[i] = ld16(rcc, (160 * (gi & 3) + c) * 1280 + ((gi >> 2) % 20) * 64 + q * 16, i * (16 * 1280));
      const int cu = gi % 54, ckt = gi / 54;
      const __amdgpu_buffer_rsrc_t rc = mkrs(cu < 27 ? p.wg : p.wu, gi < 162 ? D_ * F_ * 2 : 0);
      v4u cw[27], sel;
#pragma unroll
      for (int i = 0; i < 27; ++i) cw[i] = ld16(rc, (216 * ckt + (lane >> 3)) * 3456 + (cu < 27 ? cu : cu - 27) * 128 + (lane & 7) * 16, i * (8 * 3456));
      SB0();
      {
        const unsigned o = gi < 160 ? ((2 * (gi & 3) * NKB + 54 + (gi >> 2)) * 5) * 1024 + lane * 16 : OOB;
#pragma unroll
        for (int i = 0; i < 10; ++i) st16c(cf[i], r2, o + (i / 5) * (NKB * 5120) + (i % 5) * 1024);
      }
      SB0();
#pragma unroll
      for (int e = 0; e < 4; ++e) sel[e] = (c == 2 * e ? 0x3F80u : 0u) | (c == 2 * e + 1 ? 0x3F800000u : 0u);
      f32x4 cacc = {};
#pragma unroll
      for (int i = 0; i < 27; ++i) cacc = mfma16(sel, cw[i], cacc);
      f32x4 v;
#pragma unroll
      for (int e = 0; e < 4; ++e) v[e] = cacc[e] + dppf<0x128>(cacc[e]);
      const unsigned o = (q < 2 && gi < 162) ? WS_CSG + (ckt * 3456 + cu * 64 + (c & 7) * 8 + 4 * q + 2 * (c >> 3)) * 8 : OOB;
      st16c(v4u{__float_as_uint(c < 8 ? v.x : v.z), tag, __float_as_uint(c < 8 ? v.y : v.w), tag}, rctl, o);
      SB0();
      WAITV(1);
      st4c(tag, rctl, (lane == 0 && gi < 160) ? WS_FLC + gi * 4 : OOB);
      if (gi == 175) {
        _Pragma("unroll 1") for (int spin = 0; spin < SPIN; ++spin) { FENCE();
          bool ok = true;
#pragma unroll
          for (int i = 0; i < 4; ++i) ok = ok & (ld4c(rctl, WS_STATS + (8 * (64 * i + lane)) * 16 + 4) == tag);
          if (__all(ok)) break;
          __builtin_amdgcn_s_sleep(8);
        }
        st4c(tag, rctl, lane == 0 ? WS_EPOCH : OOB);
      }
    }
    STAMP(1);
    lwait<8>(afree, lrd(afree), 6);
    v4u stv[4], cv[SSM ? 1 : 6];
    const __amdgpu_buffer_rsrc_t rcs = mkrs(p.ws + WS_CSG, 3 * 3456 * 8);
    _Pragma("unroll 1") for (int spin = 0; spin < SPIN; ++spin) { FENCE();
#pragma unroll
      for (int i = 0; i < 4; ++i) stv[i] = ld16c(rctl, WS_STATS + (256 * id.R + 64 * i + lane) * 16, 0);
      if constexpr (SSM) cv[0] = ld16c(rctl, WS_CSS + ((lane >> 5) * 640 + 64 * id.g + 2 * (lane & 31)) * 8, 0);
      else {
#pragma unroll
        for (int m = 0; m < 2; ++m) {
          const int col = 80 * id.g + 2 * lane;
          const unsigned vo = (lane < 40 && col < F_) ? (m * F_ + col) * 8 : OOB;
#pragma unroll
          for (int k = 0; k < 3; ++k) cv[3 * m + k] = ld16c(rcs, vo, k * (3456 * 8));
        }
      }
      bool ok = true;
#pragma unroll
      for (int i = 0; i < 4; ++i) ok = ok & (stv[i].y == tag) & (stv[i].w == tag);
      bool ok2 = true;
#pragma unroll
      for (int i = 0; i < (SSM ? 1 : 6); ++i) ok2 = ok2 & (cv[i].y == tag) & (cv[i].w == tag);
      if (!SSM) ok2 = ok2 | (lane >= 40) | (80 * id.g + 2 * lane >= F_);
      if (__all(ok & ok2)) break;
      __builtin_amdgcn_s_sleep(16);
    }
#pragma unroll
    for (int i = 0; i < 4; ++i) *(LAS v2u*)LP(L_STATS + (64 * i + lane) * 8) = v2u{stv[i].x, stv[i].z};
    if constexpr (SSM) *(LAS v2u*)LP(L_CS + (lane >> 5) * 320 + (lane & 31) * 8) = v2u{cv[0].x, cv[0].z};
    else if (lane < 40) {
#pragma unroll
      for (int m = 0; m < 2; ++m)
        *(LAS f32x2*)LP(L_CS + m * 320 + lane * 8) = f32x2{__uint_as_float(cv[3 * m].x) + __uint_as_float(cv[3 * m + 1].x) + __uint_as_float(cv[3 * m + 2].x),
                                                             __uint_as_float(cv[3 * m].z) + __uint_as_float(cv[3 * m + 1].z) + __uint_as_float(cv[3 * m + 2].z)};
    }
    sig(afull, 11);
    STAMP(2);
    lwait<8>(afree, lrd(afree), 8);
    _Pragma("unroll 1") for (int spin = 0; spin < SPIN; ++spin) { FENCE();
      const v4u fl = ld16c(rctl, WS_FLW + min(lane, 59) * 16, 0);
      if (__all((fl.x == tag) & (fl.y == tag) & (fl.z == tag) & (fl.w == tag))) break;
      __builtin_amdgcn_s_sleep(16);
    }
    sig(L_RDY, 1);
    STAMP(3);
  }
}

DI void carries(const Par& p, const Ids& id, const int h, const int lane) {
  const __amdgpu_buffer_rsrc_t rctl = mkrs(p.ws, WS_CTL_BYTES);
  const int c = lane & 15, q = lane >> 4;
  {
    const unsigned tag = *(LAS unsigned*)LP(L_TAG);
    const int ch = 16 * h + c;
    LAS float* cst = (LAS float*)LP(L_CST);
    const float l8r = cst[128 + ch], l8i = cst[192 + ch], l64r = cst[256 + ch], l64i = cst[320 + ch], lwr = cst[384 + ch], lwi = cst[448 + ch];
    const float kr = cst[512 + ch], ki = cst[576 + ch];
    float w0r = 1.f, w0i = 0.f, w1r = 1.f, w1i = 0.f, qr = kr, qi = ki;
#pragma unroll
    for (int s = 0; s < 6; ++s) {
      float a = w0r, b = w0i; cm1(a, b, lwr, lwi, 0.f, 0.f);
      if (s < id.R - 1 - q) { w0r = a; w0i = b; }
      if (s < id.R - 5 - q) { w1r = a; w1i = b; }
    }
#pragma unroll
    for (int s = 0; s < 3; ++s) { float a = qr, b = qi; cm1(a, b, l64r, l64i, 0.f, 0.f); if (s < q) { qr = a; qi = b; } }
    const __amdgpu_buffer_rsrc_t rag = mkrs(p.ws + WS_AGG, id.R * 640 * 16);
    LAS float* pe = (LAS float*)LP(L_E + 8 * q * ESTR + ch * 4);
    HBAR();
    float er[8], ei[8];
#pragma unroll
    for (int s = 0; s < 8; ++s) { er[s] = pe[s * (ESTR / 4)]; ei[s] = pe[s * (ESTR / 4) + 64]; }
    float a = er[0], b = ei[0];
#pragma unroll
    for (int s = 1; s < 8; ++s) cm1(a, b, l8r, l8i, er[s], ei[s]);
    float ta[4], tb[4];
    gather4(a, ta); gather4(b, tb);
    float pa = 0.f, pb = 0.f;
    a = ta[0]; b = tb[0];
#pragma unroll
    for (int s = 1; s < 4; ++s) {
      if (q == s) { pa = a; pb = b; }
      cm1(a, b, l64r, l64i, ta[s], tb[s]);
    }
    st16c(v4u{__float_as_uint(a), tag, __float_as_uint(b), tag}, rctl, (q == 0 && id.R < 7) ? WS_AGG + (id.R * 640 + 64 * id.g + ch) * 16 : OOB);
    v4u p0 = ld16c(rag, (q * 640 + 64 * id.g + ch) * 16, 0), p1 = ld16c(rag, ((q + 4) * 640 + 64 * id.g + ch) * 16, 0);
    SB0();
    cm1(pa, pb, kr, ki, 0.f, 0.f);
#pragma unroll
    for (int s = 0; s < 7; ++s) cm1(er[s], ei[s], kr, ki, 0.f, 0.f);
    _Pragma("unroll 1") for (int spin = 0; spin < SPIN; ++spin) { FENCE();
      const bool ok = ((q >= id.R) | ((p0.y == tag) & (p0.w == tag))) & ((q + 4 >= id.R) | ((p1.y == tag) & (p1.w == tag)));
      if (__all(ok)) break;
      p0 = ld16c(rag, (q * 640 + 64 * id.g + ch) * 16, 0); p1 = ld16c(rag, ((q + 4) * 640 + 64 * id.g + ch) * 16, 0);
    }
    float ca = __uint_as_float(p1.x), cb = __uint_as_float(p1.z);
    cm1(ca, cb, w1r, w1i, 0.f, 0.f);
    { float x = __uint_as_float(p0.x), y = __uint_as_float(p0.z); cm1(x, y, w0r, w0i, ca, cb); ca = x; cb = y; }
    gather4(ca, ta); gather4(cb, tb);
    ca = (ta[0] + ta[1]) + (ta[2] + ta[3]); cb = (tb[0] + tb[1]) + (tb[2] + tb[3]);
    cm1(ca, cb, qr, qi, pa, pb);
#pragma unroll
    for (int s = 0; s < 8; ++s) {
      pe[s * (ESTR / 4)] = ca; pe[s * (ESTR / 4) + 64] = -cb;
      if (s < 7) cm1(ca, cb, l8r, l8i, er[s], ei[s]);
    }
    HBAR();
  }
}

__global__ __launch_bounds__(512, 2) void fused(Par p) {
  const int bid = blockIdx.x, xcd = bid & 7, idx = bid >> 3, a = xcd >> 2, b = xcd & 3, s = idx >> 2;
  Ids id;
  id.R = 4 * a + (idx & 3);
  const int ns = b < 2 ? 3 : 2, s0 = b < 2 ? 3 * b : 2 * b + 2, g0 = b < 2 ? 5 * b : 6 * b - 2;
  const bool ssm = s < ns;
  id.g = ssm ? s0 + s : g0 + s - ns;
  id.cg = ssm ? id.g : 10 + id.g;
  id.rt = 16 * a + 8 * (b >> 1) + (idx >> 2);
  id.ct = 4 * (b & 1) + (idx & 3);
  const int wv = rfl(threadIdx.x >> 6), lane = threadIdx.x & 63;
  if (ssm) { if (wv < 4) compute_wave<true>(p, id, wv, lane); else helper_wave<true>(p, id, wv - 4, lane); }
  else { if (wv < 4) compute_wave<false>(p, id, wv, lane); else helper_wave<false>(p, id, wv - 4, lane); }

  const __amdgpu_buffer_rsrc_t rctl = mkrs(p.ws, WS_CTL_BYTES);
  if (wv >= 4) lwait<8>(L_RDY, lrd(L_RDY), 1);
  const unsigned tag = rfl(*(LAS unsigned*)LP(L_TAG));
  if (wv < 4) {
    WAITV(0);
    unsigned old = 0;
    if (lane == 0) old = __hip_atomic_fetch_add((LAS unsigned*)LP(L_CNT), 1u, __ATOMIC_RELAXED, __HIP_MEMORY_SCOPE_WORKGROUP);
    st4c(tag, rctl, (lane == 0 && old == 3) ? WS_FLH + (id.R * 32 + id.cg) * 4 : OOB);
    lwait(L_RDY, lrd(L_RDY), 1);
  }
  STAMP(16);
  const __amdgpu_buffer_rsrc_t rw = mkrs(p.ws + WS_W2 + (unsigned)id.ct * (NKB * 5120), NKB * 5120);
  const __amdgpu_buffer_rsrc_t rh = mkrs(p.ws + WS_H + (unsigned)id.rt * (NKB * 4096), NKB * 4096);
  const unsigned vl = lane * 16;
  v4u wb[DW][5], hb[DH][4];
  auto loadw = [&](int it) {
#pragma unroll
    for (int i = 0; i < 5; ++i) wb[it % DW][i] = ld16c(rw, vl + (i & 3) * 1024, (8 * it + wv) * 5120 + (i >> 2) * 4096);
  };
  auto loadh = [&](int it) {
#pragma unroll
    for (int j = 0; j < 4; ++j) hb[it % DH][j] = ld16c(rh, vl + j * 1024, (8 * it + wv) * 4096);
  };
  SB0();
#pragma unroll
  for (int it = NL; it < NL + DW; ++it) { loadw(it); SB0(); }
  SB0();
  if (wv >= 4) {
    lwait<4>(L_FREE + (lane & 3) * 4, 0, 9);
    asm volatile("s_nop 4");
#pragma unroll
    for (int n = 0; n < 20; ++n) gldsc(rw, L_W + ((wv - 4) * 20 + n) * 1024, vl, ((wv - 4) * 20 + n) * 1024);
    lwait<4>(L_FREE + (lane & 3) * 4, 0, 10);
#pragma unroll
    for (int n = 0; n < 10; ++n) gldsc(rw, ((wv - 4) * 10 + n) * 1024, vl, (80 + (wv - 4) * 10 + n) * 1024);
  }
  SB0();
  if (ssm && wv >= 4) carries(p, id, wv - 4, lane);
  const unsigned fo = WS_FLH + ((id.rt >> 2) * 32 + (lane & 31)) * 4;
  if (wv >= 4) {
    WAITV(0);
    if (wv == 4) {
      _Pragma("unroll 1") for (int spin = 0; spin < SPIN; ++spin) { FENCE();
        if (__all((ld4c(rctl, fo) == tag) | ((lane & 31) < 10))) break;
        __builtin_amdgcn_s_sleep(1);
      }
    }
    FENCE();
    if (lane == 0) __hip_atomic_fetch_add((LAS unsigned*)LP(L_GO), 1u, __ATOMIC_RELAXED, __HIP_MEMORY_SCOPE_WORKGROUP);
  }
  _Pragma("unroll 1") for (int spin = 0; spin < 64 * SPIN; ++spin) { FENCE();
    if (rfl(__hip_atomic_load((LAS unsigned*)LP(L_GO), __ATOMIC_RELAXED, __HIP_MEMORY_SCOPE_WORKGROUP)) >= 4) break;
    __builtin_amdgcn_s_sleep(1);
  }
  FENCE();
  SB0();
  STAMP(20);
#pragma unroll
  for (int it = 0; it < DH; ++it) { loadh(it); SB0(); }
  f32x4 acc[4][5];
#pragma unroll
  for (int j = 0; j < 4; ++j)
#pragma unroll
    for (int i = 0; i < 5; ++i) acc[j][i] = f32x4{0.f, 0.f, 0.f, 0.f};
  const int tid = wv * 64 + lane;
  v2u xv[3], dv[3]; v4u sv[3]; int oo[3];
  unsigned sfl = 0;
  const lp pw = LP(L_W + wv * 5120 + vl), pw2 = LP(wv * 5120 + vl);
  v4u wl = l128(pw);
  SB0();
#pragma unroll
  for (int it = 0; it < NIT; ++it) {
    if (it < NL) {
#pragma unroll
      for (int i = 0; i < 5; ++i) {
        const v4u wf = wl;
        if (5 * it + i + 1 < 5 * NL) wl = l128((5 * it + i + 1) / 5 == 2 ? pw2 + ((5 * it + i + 1) % 5) * 1024 : pw + ((5 * it + i + 1) / 5) * 40960 + ((5 * it + i + 1) % 5) * 1024);
#pragma unroll
        for (int j = 0; j < 4; ++j) acc[j][i] = mfma16(wf, hb[it % DH][j], acc[j][i]);
        SB0();
      }
    } else {
#pragma unroll
      for (int j = 0; j < 4; ++j)
#pragma unroll
        for (int i = 0; i < 5; ++i) acc[j][i] = mfma16(wb[it % DW][i], hb[it % DH][j], acc[j][i]);
    }
    SB0();
    if (it + DH == 6) {
      _Pragma("unroll 1") for (int spin = 0; spin < SPIN; ++spin) { FENCE();
        if (__all(sfl == tag)) break;
        __builtin_amdgcn_s_sleep(1);
        sfl = ld4c(rctl, fo);
      }
      FENCE();
      STAMP(18);
    }
    if (it == 0) sfl = ld4c(rctl, fo);
    if (it + DH < NIT) loadh(it + DH);
    if (it >= NL && it + DW < NIT) loadw(it + DW);
    if (it == NIT - DH) {
#pragma unroll
      for (int r = 0; r < 3; ++r) {
        const int tl = min(wv + 8 * r, 19), j = tl / 5, i = tl - 5 * j;
        const int tok = 128 * (id.rt >> 1) + 8 * (lane & 15) + 4 * (id.rt & 1) + j, d = 80 * id.ct + 16 * i + 4 * (lane >> 4);
        oo[r] = tok * D_ + d;
        xv[r] = *(const v2u*)(p.x + oo[r]); dv[r] = *(const v2u*)(p.dsk + d);
        sv[r] = ld16c(rctl, WS_STATS + tok * 16, 0);
      }
      BAR();
    }
    SB0();
  }
  STAMP(21);
  const lp rb = LP(wv * 20480 + lane * 16);
#pragma unroll
  for (int j = 0; j < 4; ++j)
#pragma unroll
    for (int i = 0; i < 5; ++i) sf4(rb + (j * 5 + i) * 1024, acc[j][i]);
  BAR();
  STAMP(19);
#pragma unroll
  for (int r = 0; r < 3; ++r) {
    if (r < 2 || wv < 4) {
      const lp q0 = LP(tid * 16 + r * 8192), q1 = LP(tid * 16 + r * 8192 + 4 * 20480);
      f32x4 sum = ((lf4(q0) + lf4(q0 + 20480)) + (lf4(q0 + 40960) + lf4(q0 + 61440))) + ((lf4(q1) + lf4(q1 + 20480)) + (lf4(q1 + 40960) + lf4(q1 + 61440)));
      const f32x4 xf = {bflo(xv[r].x), bfhi(xv[r].x), bflo(xv[r].y), bfhi(xv[r].y)};
      const f32x4 df = {bflo(dv[r].x), bfhi(dv[r].x), bflo(dv[r].y), bfhi(dv[r].y)};
      sum += xf + (xf - __uint_as_float(sv[r].x)) * __uint_as_float(sv[r].z) * df;
      *(f32x4*)(p.out + oo[r]) = sum;
    }
  }
  STAMP(22);
}

extern "C" void kernel_launch(void* const* d_in, const int* in_sizes, int n_in, void* d_out, int out_size, void* d_ws, size_t ws_size, hipStream_t stream) {
  Par p;
  p.x = (const u16*)d_in[0]; p.lre = (const u16*)d_in[1]; p.lim = (const u16*)d_in[2];
  p.bre = (const u16*)d_in[3]; p.bim = (const u16*)d_in[4]; p.cre = (const u16*)d_in[5]; p.cim = (const u16*)d_in[6];
  p.ldt = (const u16*)d_in[7]; p.dsk = (const u16*)d_in[8];
  p.wg = (const u16*)d_in[10]; p.wu = (const u16*)d_in[11]; p.wd = (const u16*)d_in[12];
  p.ws = (unsigned char*)d_ws; p.out = (float*)d_out;
  (void)hipFuncSetAttribute((const void*)fused, hipFuncAttributeMaxDynamicSharedMemorySize, LDS1);
  fused<<<256, 512, LDS1, stream>>>(p);
}
```

```cpp
#define ENC 1
#include <hip/hip_runtime.h>
#include <stdint.h>

typedef unsigned short u16;
typedef unsigned v4u __attribute__((ext_vector_type(4)));
typedef unsigned v2u __attribute__((ext_vector_type(2)));
typedef float f32x4 __attribute__((ext_vector_type(4)));
typedef float f32x2 __attribute__((ext_vector_type(2)));
typedef float f32x16 __attribute__((ext_vector_type(16)));
typedef short bf16x8 __attribute__((ext_vector_type(8)));
typedef short s16x4 __attribute__((ext_vector_type(4)));
typedef __bf16 bf2 __attribute__((ext_vector_type(2)));
#define LAS __attribute__((address_space(3)))
#define DI __device__ __forceinline__
typedef LAS unsigned char* lp;

constexpr int L_ = 2048, D_ = 640, P_ = 640, F_ = 1728;
constexpr int NKB = 94;
constexpr unsigned WS_EPOCH = 0;
constexpr unsigned WS_FLH = 1024;
constexpr unsigned WS_FLW = 2048;
constexpr unsigned WS_FLC = WS_FLW + 320;
constexpr unsigned WS_STATS = 4096;
constexpr unsigned WS_AGG = WS_STATS + 32768;
constexpr unsigned WS_CSG = WS_AGG + 8 * 640 * 16;
constexpr unsigned WS_CSS = WS_CSG + 3 * 3456 * 8;
constexpr unsigned WS_CTL_BYTES = WS_CSS + 1280 * 8;
constexpr unsigned WS_W2 = 1u << 20;
constexpr unsigned WS_W2_BYTES = 8 * NKB * 5120;
constexpr unsigned WS_H = 8u << 20;
constexpr unsigned WS_H_BYTES = 32 * NKB * 4096;

constexpr int UPO = 10368;
constexpr int STG = 32768 + 20480 + 128;
constexpr int LDS1 = 163840;
constexpr int L_STATS = 3 * STG;
constexpr int L_CS = L_STATS + 2048;
constexpr int L_TAG = L_CS + 640;
constexpr int L_CNT = L_TAG + 4;
constexpr int L_CST = 49152;
constexpr int L_W = STG;
constexpr int L_GO = L_CNT + 4, L_RDY = L_CNT + 8;
constexpr int L_FULL = L_TAG + 16, L_FREE = L_TAG + 32;
constexpr int L_E = 136000, ESTR = 528;
constexpr int SPIN = 1 << 18;
constexpr unsigned OOB = 0x40000000u;

DI float bf2f(unsigned b) { return __uint_as_float(b << 16); }
DI float bflo(unsigned w) { return __uint_as_float(w << 16); }
DI float bfhi(unsigned w) { return __uint_as_float(w & 0xffff0000u); }
DI unsigned pk2(float a, float b) { f32x2 v = {a, b}; return __builtin_bit_cast(unsigned, __builtin_convertvector(v, bf2)); }
DI float rbf(float a) { return bflo(pk2(a, a) & 0xffffu); }
DI f32x4 mfma16(v4u a, v4u b, f32x4 c) { return __builtin_amdgcn_mfma_f32_16x16x32_bf16(__builtin_bit_cast(bf16x8, a), __builtin_bit_cast(bf16x8, b), c, 0, 0, 0); }
DI f32x16 mfma32(v4u a, v4u b, f32x16 c) { return __builtin_amdgcn_mfma_f32_32x32x16_bf16(__builtin_bit_cast(bf16x8, a), __builtin_bit_cast(bf16x8, b), c, 0, 0, 0); }
DI unsigned rfl(unsigned v) { return __builtin_amdgcn_readfirstlane(v); }
DI __amdgpu_buffer_rsrc_t mkrs(const void* p, unsigned bytes) { return __builtin_amdgcn_make_buffer_rsrc((void*)p, 0, bytes, 0x00020000); }
DI void glds(__amdgpu_buffer_rsrc_t rs, unsigned ldsaddr, unsigned voff, unsigned soff) {
  asm volatile("s_mov_b32 m0, %0\n\ts_nop 0\n\tbuffer_load_dwordx4 %1, %2, %3 offen lds" ::"s"(ldsaddr), "v"(voff), "s"(rs), "s"(soff));
}
DI void gldsc(__amdgpu_buffer_rsrc_t rs, unsigned ldsaddr, unsigned voff, unsigned soff) {
  asm volatile("s_mov_b32 m0, %0\n\ts_nop 0\n\tbuffer_load_dwordx4 %1, %2, %3 offen sc1 lds" ::"s"(ldsaddr), "v"(voff), "s"(rs), "s"(soff));
}
DI lp LP(unsigned a) { return (lp)(uintptr_t)a; }
DI v4u l128(lp a) { return *(LAS v4u*)a; }
DI f32x4 lf4(lp a) { return *(LAS f32x4*)a; }
DI void sf4(lp a, f32x4 v) { *(LAS f32x4*)a = v; }
DI s16x4 ltr(lp a) { return __builtin_amdgcn_ds_read_tr16_b64_v4i16((LAS s16x4*)a); }
DI v4u cat(s16x4 lo, s16x4 hi) { return __builtin_bit_cast(v4u, __builtin_shufflevector(lo, hi, 0, 1, 2, 3, 4, 5, 6, 7)); }
DI v4u ld16(__amdgpu_buffer_rsrc_t r, unsigned vo, unsigned so) { return __builtin_amdgcn_raw_buffer_load_b128(r, vo, so, 0); }
DI v4u ld16c(__amdgpu_buffer_rsrc_t r, unsigned vo, unsigned so) { return __builtin_amdgcn_raw_buffer_load_b128(r, vo, so, 16); }
DI unsigned ld4c(__amdgpu_buffer_rsrc_t r, unsigned vo) { return __builtin_amdgcn_raw_buffer_load_b32(r, vo, 0, 16); }
DI void st16c(v4u v, __amdgpu_buffer_rsrc_t r, unsigned vo) { __builtin_amdgcn_raw_buffer_store_b128(v, r, vo, 0, 16); }
DI void st4c(unsigned v, __amdgpu_buffer_rsrc_t r, unsigned vo) { __builtin_amdgcn_raw_buffer_store_b32(v, r, vo, 0, 16); }
#define BAR() asm volatile("s_waitcnt lgkmcnt(0)\n\ts_barrier" ::: "memory")
#define SB0() __builtin_amdgcn_sched_barrier(0)
#define WAITV(n) asm volatile("s_waitcnt vmcnt(%0)" ::"i"(n) : "memory")
#define FENCE() asm volatile("" ::: "memory")
template <int C> DI float dppf(float v) { return __int_as_float(__builtin_amdgcn_update_dpp(0, __float_as_int(v), C, 0xf, 0xf, true)); }
template <int N> DI f32x4 shr4(f32x4 v) { return f32x4{dppf<0x110 + N>(v.x), dppf<0x110 + N>(v.y), dppf<0x110 + N>(v.z), dppf<0x110 + N>(v.w)}; }
DI float sum8(float v) {
  v += dppf<0xB1>(v); v += dppf<0x4E>(v); v += dppf<0x141>(v);
  return v;
}
DI float dot2(unsigned a, unsigned b, float c) { return __builtin_amdgcn_fdot2_f32_bf16(__builtin_bit_cast(bf2, a), __builtin_bit_cast(bf2, b), c, false); }
DI void gather4(float x, float (&t)[4]) {
  const auto a = __builtin_amdgcn_permlane16_swap(__float_as_uint(x), __float_as_uint(x), false, false);
  const auto e = __builtin_amdgcn_permlane32_swap(a[0], a[0], false, false), o = __builtin_amdgcn_permlane32_swap(a[1], a[1], false, false);
  t[0] = __uint_as_float(e[0]); t[2] = __uint_as_float(e[1]); t[1] = __uint_as_float(o[0]); t[3] = __uint_as_float(o[1]);
}
DI void cm1(float& a, float& b, float lr, float li, float ur, float ui) {
  const float na = fmaf(lr, a, fmaf(-li, b, ur)), nb = fmaf(lr, b, fmaf(li, a, ui));
  a = na; b = nb;
}
DI f32x4 fma4(f32x4 a, f32x4 b, f32x4 c) { return __builtin_elementwise_fma(a, b, c); }
DI f32x4 sp4(float a) { return f32x4{a, a, a, a}; }
DI void cmac(f32x4& a, f32x4& b, f32x4 lr, f32x4 li, f32x4 ur, f32x4 ui) {
  const f32x4 na = fma4(lr, a, fma4(-li, b, ur)), nb = fma4(lr, b, fma4(li, a, ui));
  a = na; b = nb;
}

#define STAMP(i) do { if ((i) == 0) enc_a = (unsigned)__builtin_amdgcn_s_memrealtime(); if ((i) == ENC) enc_b = (unsigned)__builtin_amdgcn_s_memrealtime(); \
  if ((i) == 22) { const unsigned e_ = (unsigned)__builtin_amdgcn_s_memrealtime(); if (rfl(threadIdx.x) == 0) while ((unsigned)__builtin_amdgcn_s_memrealtime() - e_ < 2u * (enc_b - enc_a)) __builtin_amdgcn_s_sleep(1); } } while (0)
#define STAMPC(i)

struct Par {
  const u16 *x, *lre, *lim, *bre, *bim, *cre, *cim, *ldt, *dsk, *wg, *wu, *wd;
  unsigned char* ws;
  float* out;
};
struct Ids { int R, g, cg, rt, ct; };

constexpr int DW = 4, DH = 3, NIT = 12, NL = 3;
#define PIN4(x) asm volatile("" : "+v"(x))
DI void sig(unsigned addr, unsigned v) {
  asm volatile("s_mov_b64 exec, 1\n\tds_write_b32 %0, %1\n\ts_mov_b64 exec, -1" ::"v"(addr), "v"(v) : "memory");
}
DI unsigned lrd(unsigned addr) { return *(volatile LAS unsigned*)LP(addr); }
#define LSLEEP 1
template <int SL = 1> DI void lwait(unsigned addr, unsigned v, unsigned need) {
  if (__builtin_expect(!__all(v >= need), 0)) {
    _Pragma("unroll 1") for (int spin = 0; spin < 64 * SPIN; ++spin) { FENCE();
      if (__all(lrd(addr) >= need)) break;
      __builtin_amdgcn_s_sleep(SL);
    }
  }
  FENCE();
}

template <bool SSM>
DI void compute_wave(const Par& p, const Ids& id, const int w, const int lane, unsigned& enc_a, unsigned& enc_b) {
  STAMP(0);
  const unsigned afull = L_FULL + (lane & 3) * 4, afree = L_FREE + w * 4;
  unsigned fv = 0;
  if constexpr (!SSM) {
    const int r = lane & 31, hh = lane >> 5;
    const unsigned xa = (64 * w + r) * 128 + ((hh ^ ((r >> 1) & 7)) * 16);
    const unsigned wa = 32768 + ((lane >> 4) & 1) * UPO + (8 * hh + ((lane & 15) >> 2)) * 160 + (lane & 3) * 8;
    f32x16 acc[5][2] = {};
#pragma unroll
    for (int e = 0; e < 5; ++e) { PIN4(acc[e][0]); PIN4(acc[e][1]); }
    v4u fx[2][2]; s16x4 fl[4], fh[4];
    lp px[4], pw;
    auto setp = [&](unsigned sb) {
#pragma unroll
      for (int s = 0; s < 4; ++s) px[s] = LP(sb + (xa ^ (32 * s)));
      pw = LP(sb + wa);
    };
    auto woff = [&](int n) { return (n / 5) * 2560 + (n % 5) * 32; };
    setp(0);
    SB0();
    BAR();
    lwait(afull, 0, 1);
    STAMP(1);
    fx[0][0] = l128(px[0]); fx[0][1] = l128(px[0] + 4096);
#pragma unroll
    for (int n = 0; n < 3; ++n) { fl[n] = ltr(pw + woff(n)); fh[n] = ltr(pw + woff(n) + 640); }
    unsigned sb = 0;
#pragma unroll 1
    for (int i = 0; i < 10; ++i) {
#pragma unroll
      for (int n = 0; n < 20; ++n) {
        const int ks = n / 5, e = n % 5, nn = (n + 3) % 20;
        if (n == 14) fv = lrd(afull);
        if (n == 17) { sig(afree, i + 1); lwait(afull, fv, i + 2); STAMP(2 + i); if (i == 3 || i == 7) STAMPC(24 + i); sb = sb == 2 * STG ? 0 : sb + STG; setp(sb); SB0(); }
        const v4u wf = cat(fl[n & 3], fh[n & 3]);
        acc[e][0] = mfma32(wf, fx[ks & 1][0], acc[e][0]); SB0();
        fl[(n + 3) & 3] = ltr(pw + woff(nn));
        fh[(n + 3) & 3] = ltr(pw + woff(nn) + 640); SB0();
        acc[e][1] = mfma32(wf, fx[ks & 1][1], acc[e][1]); SB0();
        if (ks < 3 && e == 1) { fx[(ks + 1) & 1][0] = l128(px[ks + 1]); fx[(ks + 1) & 1][1] = l128(px[ks + 1] + 4096); }
        if (n == 17) { fx[0][0] = l128(px[0]); fx[0][1] = l128(px[0] + 4096); }
        SB0();
      }
    }
    STAMP(12);
    const __amdgpu_buffer_rsrc_t rh = mkrs(p.ws + WS_H + (unsigned)(4 * id.R + w) * (NKB * 4096), 54 * 4096);
    float mu[2], r2[2];
#pragma unroll
    for (int u = 0; u < 2; ++u) {
      const int F = 4 * w + 2 * u + (r >> 4);
      const f32x2 s = *(LAS f32x2*)LP(L_STATS + (128 * (F >> 3) + 8 * (r & 15) + (F & 7)) * 8);
      mu[u] = s.x; r2[u] = s.y * s.y;
    }
    const lp pc = LP(L_CS + hh * 16);
    const unsigned so0 = (r >> 4) * 1024 + (hh * 16 + (r & 15)) * 16;
#pragma unroll
    for (int e = 0; e < 5; ++e) {
      const f32x4 g0 = lf4(pc + e * 64), g1 = lf4(pc + e * 64 + 32), u0 = lf4(pc + e * 64 + 320), u1 = lf4(pc + e * 64 + 352);
      const int T = 5 * id.g + e;
      const unsigned so = so0 + (T >> 1) * 4096 + (T & 1) * 512;
#pragma unroll
      for (int u = 0; u < 2; ++u) {
        const f32x16 a = acc[e][u];
        const f32x4 nm = sp4(-mu[u]);
        const f32x4 h0 = fma4(nm, g0, f32x4{a[0], a[1], a[2], a[3]}) * fma4(nm, u0, f32x4{a[8], a[9], a[10], a[11]}) * r2[u];
        const f32x4 h1 = fma4(nm, g1, f32x4{a[4], a[5], a[6], a[7]}) * fma4(nm, u1, f32x4{a[12], a[13], a[14], a[15]}) * r2[u];
        st16c(v4u{pk2(h0.x, h0.y), pk2(h0.z, h0.w), pk2(h1.x, h1.y), pk2(h1.z, h1.w)}, rh, so + u * 2048);
      }
    }
  } else {
    const int c = lane & 15, q = lane >> 4, G = w >> 1, blk = w & 1;
    const unsigned xa = (128 * G + c) * 128 + ((q ^ (c >> 1)) * 16);
    const unsigned wa = 32768 + (32 * blk + c) * 128 + ((q ^ (c >> 1)) * 16);
    f32x4 acc[8][4] = {};
#pragma unroll
    for (int j = 0; j < 8; ++j)
#pragma unroll
      for (int t = 0; t < 4; ++t) PIN4(acc[j][t]);
    v4u fw[2][4], fx[4];
    lp px[2], pw[2];
    auto setp = [&](unsigned sb) { px[0] = LP(sb + xa); px[1] = LP(sb + (xa ^ 64)); pw[0] = LP(sb + wa); pw[1] = LP(sb + (wa ^ 64)); };
    auto woff = [&](int t) { return (t >> 1) * 8192 + (t & 1) * 2048; };
    setp(0);
    SB0();
    BAR();
    lwait(afull, 0, 1);
    STAMP(1);
#pragma unroll
    for (int t = 0; t < 4; ++t) fw[0][t] = l128(pw[0] + woff(t));
#pragma unroll
    for (int n = 0; n < 3; ++n) fx[n] = l128(px[0] + n * 2048);
    unsigned sb = 0;
#pragma unroll 1
    for (int i = 0; i < 10; ++i) {
#pragma unroll
      for (int n = 0; n < 16; ++n) {
        const int kk = n >> 3, j = n & 7, nn = (n + 3) & 15;
        if (n == 10) fv = lrd(afull);
        if (n == 13) { sig(afree, i + 1); lwait(afull, fv, i + 2); STAMP(2 + i); if (i == 3 || i == 7) STAMPC(24 + i); sb = sb == 2 * STG ? 0 : sb + STG; setp(sb); SB0(); }
        const v4u xf = fx[n & 3];
        acc[j][0] = mfma16(fw[kk][0], xf, acc[j][0]); SB0();
        fx[(n + 3) & 3] = l128(px[nn >> 3] + (nn & 7) * 2048); SB0();
        acc[j][1] = mfma16(fw[kk][1], xf, acc[j][1]); SB0();
        if (n >= 2 && n < 6) fw[1][n - 2] = l128(pw[1] + woff(n - 2));
        if (n == 13) fw[0][0] = l128(pw[0] + woff(0));
        if (n == 14) fw[0][2] = l128(pw[0] + woff(2));
        SB0();
        acc[j][2] = mfma16(fw[kk][2], xf, acc[j][2]); SB0();
        if (n == 13) fw[0][1] = l128(pw[0] + woff(1));
        if (n == 14) fw[0][3] = l128(pw[0] + woff(3));
        SB0();
        acc[j][3] = mfma16(fw[kk][3], xf, acc[j][3]); SB0();
      }
    }
    STAMP(12);
    const lp ps = LP(L_STATS + (128 * G + 8 * c) * 8);
    const lp pc = LP(L_CS + (32 * blk + 8 * q) * 4);
    const lp pk = LP(L_CST + (32 * blk + 8 * q) * 4);
    const lp pe = LP(L_E + (16 * G + c) * ESTR + (32 * blk + 8 * q) * 4);
    f32x4 lr[2], li[2], er[2], ei[2];
    const unsigned frv = lrd(L_FREE + (lane & 3) * 4);
    {
      f32x4 cs[4], st[4];
#pragma unroll
      for (int t = 0; t < 4; ++t) { cs[t] = lf4(pc + (t >> 1) * 320 + (t & 1) * 16); st[t] = lf4(ps + t * 16); }
#pragma unroll
      for (int ab = 0; ab < 2; ++ab) { lr[ab] = lf4(pk + ab * 16); li[ab] = lf4(pk + 256 + ab * 16); }
#pragma unroll
      for (int jj = 0; jj < 4; ++jj) {
#pragma unroll
        for (int t = 0; t < 4; ++t) {
          acc[2 * jj][t] = fma4(sp4(-st[jj].x), cs[t], acc[2 * jj][t]) * st[jj].y;
          acc[2 * jj + 1][t] = fma4(sp4(-st[jj].z), cs[t], acc[2 * jj + 1][t]) * st[jj].w;
        }
      }
#pragma unroll
      for (int ab = 0; ab < 2; ++ab) { er[ab] = acc[0][ab]; ei[ab] = acc[0][2 + ab]; }
#pragma unroll
      for (int j = 1; j < 8; ++j)
#pragma unroll
        for (int ab = 0; ab < 2; ++ab) cmac(er[ab], ei[ab], lr[ab], li[ab], acc[j][ab], acc[j][2 + ab]);
      lwait(L_FREE + (lane & 3) * 4, frv, 9);
#pragma unroll
      for (int ab = 0; ab < 2; ++ab) { sf4(pe + ab * 16, er[ab]); sf4(pe + 256 + ab * 16, ei[ab]); }
    }
    SB0();
    BAR();
    STAMP(13);
#pragma unroll
    for (int ab = 0; ab < 2; ++ab) {
      const f32x4 kr = lf4(pk + 512 * 4 + ab * 16), ki = lf4(pk + 512 * 4 + 256 + ab * 16);
#pragma unroll
      for (int j = 0; j < 8; ++j) {
        const f32x4 ur = acc[j][ab], ui = acc[j][2 + ab];
        acc[j][ab] = fma4(-ki, ui, kr * ur); acc[j][2 + ab] = fma4(-ki, ur, -kr * ui);
        PIN4(acc[j][ab]); PIN4(acc[j][2 + ab]);
      }
      li[ab] = -li[ab];
    }
    const __amdgpu_buffer_rsrc_t rh = mkrs(p.ws + WS_H + (unsigned)(4 * id.R + 2 * G) * (NKB * 4096), 2 * NKB * 4096);
    unsigned vo[4];
#pragma unroll
    for (int z = 0; z < 4; ++z) { vo[z] = (54 + 2 * id.g + blk + 20 * (z & 1) + NKB * (z >> 1)) * 4096 + lane * 16; PIN4(vo[z]); }
    SB0();
    BAR();
    STAMP(14);
#pragma unroll
    for (int ab = 0; ab < 2; ++ab) { er[ab] = lf4(pe + ab * 16); ei[ab] = lf4(pe + 256 + ab * 16); }
#pragma unroll
    for (int j = 0; j < 8; ++j) {
#pragma unroll
      for (int ab = 0; ab < 2; ++ab) cmac(er[ab], ei[ab], lr[ab], li[ab], acc[j][ab], acc[j][2 + ab]);
      st16c(v4u{pk2(er[0].x, er[0].y), pk2(er[0].z, er[0].w), pk2(er[1].x, er[1].y), pk2(er[1].z, er[1].w)}, rh, vo[2 * (j >> 2)] + (j & 3) * 1024);
      st16c(v4u{pk2(ei[0].x, ei[0].y), pk2(ei[0].z, ei[0].w), pk2(ei[1].x, ei[1].y), pk2(ei[1].z, ei[1].w)}, rh, vo[2 * (j >> 2) + 1] + (j & 3) * 1024);
    }
  }
  SB0();
  STAMP(15);
}

template <bool SSM>
DI void helper_wave(const Par& p, const Ids& id, const int h, const int lane, unsigned& enc_a, unsigned& enc_b) {
  const __amdgpu_buffer_rsrc_t rctl = mkrs(p.ws, WS_CTL_BYTES);
  const int c = lane & 15, q = lane >> 4;
  const unsigned afree = L_FREE + (lane & 3) * 4, afull = L_FULL + h * 4;
  STAMP(0);
#define HBAR() do { SB0(); BAR(); SB0(); } while (0)
  if (h < 3) {
    if (h < 2) {
      const __amdgpu_buffer_rsrc_t rx = mkrs(p.x, L_ * D_ * 2);
      const unsigned v0 = (8 * (lane >> 3)) * 1280 + (((lane & 7) ^ (lane >> 4)) * 16), v1 = v0 ^ 64;
      unsigned so = (256 * id.R + 128 * h) * 1280;
      auto issue = [&](unsigned s, unsigned sb) {
#pragma unroll
        for (int n = 0; n < 16; ++n) glds(rx, sb + h * 16384 + n * 1024, (n & 1) ? v1 : v0, s + (64 * (n & 1) + (n >> 1)) * 1280);
      };
      asm volatile("s_nop 4");
      issue(so, 0);
      BAR();
      issue(so + 128, STG);
      so += 256;
      unsigned sb = 2 * STG;
#pragma unroll 1
      for (int j = 0; j < 8; ++j) {
        WAITV(16); sig(afull, j + 1);
        lwait<LSLEEP>(afree, lrd(afree), j);
        issue(so, sb);
        so += 128; sb = sb == 2 * STG ? 0 : sb + STG;
      }
      WAITV(16); sig(afull, 9);
    } else {
      constexpr int NV = SSM ? 2 : 10;
      const __amdgpu_buffer_rsrc_t ra = mkrs(SSM ? p.bre : p.wg, SSM ? P_ * D_ * 2 : D_ * F_ * 2), rb = mkrs(SSM ? p.bim : p.wu, SSM ? P_ * D_ * 2 : D_ * F_ * 2);
      unsigned vw[NV];
      if constexpr (SSM) {
        vw[0] = (8 * (lane >> 5) + ((lane >> 3) & 3)) * 1280 + (((lane & 7) ^ (lane >> 4)) * 16); vw[1] = vw[0] ^ 64;
      } else {
#pragma unroll
        for (int i = 0; i < 10; ++i) { const int lc = 64 * i + lane, pos = lc / 10; vw[i] = pos * 3456 + (lc - pos * 10) * 16; }
      }
      constexpr unsigned WSTEP = SSM ? 128u : 64u * 3456u;
      constexpr int NPW = SSM ? 16 : 20;
      unsigned so = SSM ? 64 * id.g * 1280 : 160 * id.g;
      auto issue = [&](unsigned s, unsigned sb) {
#pragma unroll
        for (int n = 0; n < NPW / 2; ++n) {
          if constexpr (SSM) {
            const unsigned o = s + (32 * (n >> 2) + 16 * (n & 1) + 4 * ((n >> 1) & 1)) * 1280;
            glds(ra, sb + 32768 + n * 1024, vw[n & 1], o); glds(rb, sb + 32768 + 8192 + n * 1024, vw[n & 1], o);
          } else {
            glds(ra, sb + 32768 + n * 1024, vw[n], s); glds(rb, sb + 32768 + UPO + n * 1024, vw[n], s);
          }
        }
      };
      asm volatile("s_nop 4");
      issue(so, 0);
      BAR();
      issue(so + WSTEP, STG);
      so += 2 * WSTEP;
      unsigned sb = 2 * STG;
#pragma unroll 1
      for (int j = 0; j < 8; ++j) {
        WAITV(NPW); sig(afull, j + 1);
        lwait<LSLEEP>(afree, lrd(afree), j);
        issue(so, sb);
        so += WSTEP; sb = sb == 2 * STG ? 0 : sb + STG;
      }
      WAITV(NPW); sig(afull, 9);
    }
    WAITV(0); sig(afull, 11);
  } else {
    if (lane < 12) *(LAS unsigned*)LP(L_TAG + lane * 4) = lane == 7 ? 10u : 0u;
    const int gi = id.g * 8 + id.R;
    const unsigned epoch = ld4c(rctl, WS_EPOCH);
    const int srow = 256 * id.R + 8 * id.cg + (lane >> 3);
    v4u sx[10];
#pragma unroll
    for (int i = 0; i < 10; ++i) sx[i] = *(const v4u*)(p.x + srow * D_ + (lane & 7) * 8 + i * 64);
    HBAR();
    const __amdgpu_buffer_rsrc_t r2 = mkrs(p.ws + WS_W2, WS_W2_BYTES);
    unsigned tag;
    auto stats = [&]() {
      tag = rfl(epoch) * 0x9E3779B1u + 0x7F4A7C15u;
      f32x4 s1 = {}, s2 = {};
#pragma unroll
      for (int i = 0; i < 10; ++i)
#pragma unroll
        for (int e = 0; e < 4; ++e) { s1[e] = dot2(sx[i][e], 0x3F803F80u, s1[e]); s2[e] = dot2(sx[i][e], sx[i][e], s2[e]); }
      const float mean = sum8((s1.x + s1.y) + (s1.z + s1.w)) * (1.f / D_);
      const float var = rbf(fmaxf(sum8((s2.x + s2.y) + (s2.z + s2.w)) * (1.f / D_) - mean * mean, 0.f));
      const float sd = rbf(sqrtf(rbf(var + 1e-5f)));
      st16c(v4u{__float_as_uint(rbf(mean)), tag, __float_as_uint(1.f / sd), tag}, rctl, (lane & 7) == 0 ? WS_STATS + srow * 16 : OOB);
      if (lane == 0) *(LAS unsigned*)LP(L_TAG) = tag;
      SB0();
    };
    if constexpr (SSM) {
      const __amdgpu_buffer_rsrc_t rd = mkrs(p.wd, F_ * D_ * 2);
      const unsigned rvo = (lane >> 3) * 1280 + (((lane & 7) ^ (2 * ((lane >> 4) & 1) + 4 * ((lane >> 5) & 1))) * 16);
      unsigned rta[4], rto[4];
      const int f0 = 16 * (q >> 1) + 4 * (q & 1) + (c >> 2);
#pragma unroll
      for (int i = 0; i < 4; ++i) {
        rta[i] = STG + 49152 + f0 * 128 + (((2 * i + ((c >> 1) & 1)) ^ (f0 & 6)) * 16) + (c & 1) * 8;
        const unsigned dt = 4 * id.g + i, ct = dt / 5;
        rto[i] = ((ct * NKB + id.R) * 5 + dt - 5 * ct) * 1024 + lane * 16;
      }
      auto rt_load = [&](int k) {
#pragma unroll
        for (int z = 0; z < 2; ++z) {
          const unsigned so = (id.R + 8 * (2 * k + z)) * (32 * 1280) + id.g * 128;
#pragma unroll
          for (int n = 0; n < 4; ++n) glds(rd, (z + 1) * STG + 49152 + n * 1024, rvo, so + n * (8 * 1280));
        }
        SB0();
      };
      v4u tf[8];
      auto rt_read = [&]() {
#pragma unroll
        for (int i = 0; i < 8; ++i) { const lp a = LP(rta[i & 3]) + (i >> 2) * STG; tf[i] = cat(ltr(a), ltr(a + 1024)); }
        asm volatile("s_waitcnt lgkmcnt(0)" : "+v"(tf[0]), "+v"(tf[1]), "+v"(tf[2]), "+v"(tf[3]), "+v"(tf[4]), "+v"(tf[5]), "+v"(tf[6]), "+v"(tf[7])::"memory");
      };
      auto rt_store = [&](int k) {
#pragma unroll
        for (int i = 0; i < 8; ++i) {
          const int n = 2 * k + (i >> 2);
          if (n < 7) st16c(tf[i], r2, id.R + 8 * n < 54 ? rto[i & 3] + n * (8 * 5120) : OOB);
        }
        SB0();
      };
      const unsigned pl = p.ldt[64 * id.g + lane], pr = p.lre[64 * id.g + lane], pi = p.lim[64 * id.g + lane];
      asm volatile("s_nop 4");
      rt_load(0);
      stats();
      v4u cw[20];
#pragma unroll
      for (int i = 0; i < 20; ++i) cw[i] = *(const v4u*)((i < 10 ? p.bre : p.bim) + (64 * id.g + 8 * id.R + (lane >> 3)) * D_ + (lane & 7) * 8 + (i % 10) * 64);
      SB0();
      WAITV(21); rt_read(); rt_load(1); rt_store(0);
      {
        const float dt = rbf(expf(bf2f(pl))), lr = bf2f(pr), li = bf2f(pi);
        const float ex = expf(dt * lr);
        float sn, cn;
        sincosf(dt * li, &sn, &cn);
        float br = ex * cn, bi = ex * sn;
        const float nr = br - 1.f, ni = bi, den = lr * lr + li * li;
        LAS float* cst = (LAS float*)LP(L_CST);
        cst[4 * 128 + lane] = 2.f * (nr * lr + ni * li) / den; cst[4 * 128 + 64 + lane] = 2.f * (ni * lr - nr * li) / den;
        cst[lane] = br; cst[64 + lane] = bi;
#pragma unroll
        for (int s = 1; s <= 8; ++s) {
          const float a = br * br - bi * bi, b = 2.f * br * bi;
          br = a; bi = b;
          const int n = s == 3 ? 1 : s == 6 ? 2 : s == 8 ? 3 : 0;
          if (n) { cst[n * 128 + lane] = br; cst[n * 128 + 64 + lane] = bi; }
        }
        SB0();
      }
      WAITV(8); rt_read(); rt_load(2); rt_store(1);
      {
        f32x4 a = {};
#pragma unroll
        for (int i = 0; i < 20; ++i)
#pragma unroll
          for (int e = 0; e < 4; ++e) a[2 * (i / 10) + (e & 1)] = dot2(cw[i][e], 0x3F803F80u, a[2 * (i / 10) + (e & 1)]);
        const float s0 = sum8(a.x + a.y), s1 = sum8(a.z + a.w);
        const unsigned row = 64 * id.g + 8 * id.R + (lane >> 3);
        __builtin_amdgcn_raw_buffer_store_b64(v2u{__float_as_uint((lane & 1) ? s1 : s0), tag}, rctl, (lane & 6) == 0 ? WS_CSS + ((lane & 1) * 640 + row) * 8 : OOB, 0, 16);
        SB0();
      }
      WAITV(9); rt_read(); rt_load(3); rt_store(2);
      WAITV(8); rt_read(); rt_store(3);
      WAITV(0);
      st4c(tag, rctl, lane == 0 ? WS_FLW + gi * 4 : OOB);
    } else {
      stats();
      v4u cf[10];
      const __amdgpu_buffer_rsrc_t rcc = mkrs((gi >> 2) < 20 ? p.cre : p.cim, gi < 160 ? D_ * P_ * 2 : 0);
#pragma unroll
      for (int i = 0; i < 10; ++i) cf[i] = ld16(rcc, (160 * (gi & 3) + c) * 1280 + ((gi >> 2) % 20) * 64 + q * 16, i * (16 * 1280));
      const int cu = gi % 54, ckt = gi / 54;
      const __amdgpu_buffer_rsrc_t rc = mkrs(cu < 27 ? p.wg : p.wu, gi < 162 ? D_ * F_ * 2 : 0);
      v4u cw[27], sel;
#pragma unroll
      for (int i = 0; i < 27; ++i) cw[i] = ld16(rc, (216 * ckt + (lane >> 3)) * 3456 + (cu < 27 ? cu : cu - 27) * 128 + (lane & 7) * 16, i * (8 * 3456));
      SB0();
      {
        const unsigned o = gi < 160 ? ((2 * (gi & 3) * NKB + 54 + (gi >> 2)) * 5) * 1024 + lane * 16 : OOB;
#pragma unroll
        for (int i = 0; i < 10; ++i) st16c(cf[i], r2, o + (i / 5) * (NKB * 5120) + (i % 5) * 1024);
      }
      SB0();
#pragma unroll
      for (int e = 0; e < 4; ++e) sel[e] = (c == 2 * e ? 0x3F80u : 0u) | (c == 2 * e + 1 ? 0x3F800000u : 0u);
      f32x4 cacc = {};
#pragma unroll
      for (int i = 0; i < 27; ++i) cacc = mfma16(sel, cw[i], cacc);
      f32x4 v;
#pragma unroll
      for (int e = 0; e < 4; ++e) v[e] = cacc[e] + dppf<0x128>(cacc[e]);
      const unsigned o = (q < 2 && gi < 162) ? WS_CSG + (ckt * 3456 + cu * 64 + (c & 7) * 8 + 4 * q + 2 * (c >> 3)) * 8 : OOB;
      st16c(v4u{__float_as_uint(c < 8 ? v.x : v.z), tag, __float_as_uint(c < 8 ? v.y : v.w), tag}, rctl, o);
      SB0();
      WAITV(1);
      st4c(tag, rctl, (lane == 0 && gi < 160) ? WS_FLC + gi * 4 : OOB);
      if (gi == 175) {
        _Pragma("unroll 1") for (int spin = 0; spin < SPIN; ++spin) { FENCE();
          bool ok = true;
#pragma unroll
          for (int i = 0; i < 4; ++i) ok = ok & (ld4c(rctl, WS_STATS + (8 * (64 * i + lane)) * 16 + 4) == tag);
          if (__all(ok)) break;
          __builtin_amdgcn_s_sleep(8);
        }
        st4c(tag, rctl, lane == 0 ? WS_EPOCH : OOB);
      }
    }
    STAMP(1);
    lwait<8>(afree, lrd(afree), 6);
    v4u stv[4], cv[SSM ? 1 : 6];
    const __amdgpu_buffer_rsrc_t rcs = mkrs(p.ws + WS_CSG, 3 * 3456 * 8);
    _Pragma("unroll 1") for (int spin = 0; spin < SPIN; ++spin) { FENCE();
#pragma unroll
      for (int i = 0; i < 4; ++i) stv[i] = ld16c(rctl, WS_STATS + (256 * id.R + 64 * i + lane) * 16, 0);
      if constexpr (SSM) cv[0] = ld16c(rctl, WS_CSS + ((lane >> 5) * 640 + 64 * id.g + 2 * (lane & 31)) * 8, 0);
      else {
#pragma unroll
        for (int m = 0; m < 2; ++m) {
          const int col = 80 * id.g + 2 * lane;
          const unsigned vo = (lane < 40 && col < F_) ? (m * F_ + col) * 8 : OOB;
#pragma unroll
          for (int k = 0; k < 3; ++k) cv[3 * m + k] = ld16c(rcs, vo, k * (3456 * 8));
        }
      }
      bool ok = true;
#pragma unroll
      for (int i = 0; i < 4; ++i) ok = ok & (stv[i].y == tag) & (stv[i].w == tag);
      bool ok2 = true;
#pragma unroll
      for (int i = 0; i < (SSM ? 1 : 6); ++i) ok2 = ok2 & (cv[i].y == tag) & (cv[i].w == tag);
      if (!SSM) ok2 = ok2 | (lane >= 40) | (80 * id.g + 2 * lane >= F_);
      if (__all(ok & ok2)) break;
      __builtin_amdgcn_s_sleep(16);
    }
#pragma unroll
    for (int i = 0; i < 4; ++i) *(LAS v2u*)LP(L_STATS + (64 * i + lane) * 8) = v2u{stv[i].x, stv[i].z};
    if constexpr (SSM) *(LAS v2u*)LP(L_CS + (lane >> 5) * 320 + (lane & 31) * 8) = v2u{cv[0].x, cv[0].z};
    else if (lane < 40) {
#pragma unroll
      for (int m = 0; m < 2; ++m)
        *(LAS f32x2*)LP(L_CS + m * 320 + lane * 8) = f32x2{__uint_as_float(cv[3 * m].x) + __uint_as_float(cv[3 * m + 1].x) + __uint_as_float(cv[3 * m + 2].x),
                                                             __uint_as_float(cv[3 * m].z) + __uint_as_float(cv[3 * m + 1].z) + __uint_as_float(cv[3 * m + 2].z)};
    }
    sig(afull, 11);
    STAMP(2);
    lwait<8>(afree, lrd(afree), 8);
    _Pragma("unroll 1") for (int spin = 0; spin < SPIN; ++spin) { FENCE();
      const v4u fl = ld16c(rctl, WS_FLW + min(lane, 59) * 16, 0);
      if (__all((fl.x == tag) & (fl.y == tag) & (fl.z == tag) & (fl.w == tag))) break;
      __builtin_amdgcn_s_sleep(16);
    }
    sig(L_RDY, 1);
    STAMP(3);
  }
}

DI void carries(const Par& p, const Ids& id, const int h, const int lane) {
  const __amdgpu_buffer_rsrc_t rctl = mkrs(p.ws, WS_CTL_BYTES);
  const int c = lane & 15, q = lane >> 4;
  {
    const unsigned tag = *(LAS unsigned*)LP(L_TAG);
    const int ch = 16 * h + c;
    LAS float* cst = (LAS float*)LP(L_CST);
    const float l8r = cst[128 + ch], l8i = cst[192 + ch], l64r = cst[256 + ch], l64i = cst[320 + ch], lwr = cst[384 + ch], lwi = cst[448 + ch];
    const float kr = cst[512 + ch], ki = cst[576 + ch];
    float w0r = 1.f, w0i = 0.f, w1r = 1.f, w1i = 0.f, qr = kr, qi = ki;
#pragma unroll
    for (int s = 0; s < 6; ++s) {
      float a = w0r, b = w0i; cm1(a, b, lwr, lwi, 0.f, 0.f);
      if (s < id.R - 1 - q) { w0r = a; w0i = b; }
      if (s < id.R - 5 - q) { w1r = a; w1i = b; }
    }
#pragma unroll
    for (int s = 0; s < 3; ++s) { float a = qr, b = qi; cm1(a, b, l64r, l64i, 0.f, 0.f); if (s < q) { qr = a; qi = b; } }
    const __amdgpu_buffer_rsrc_t rag = mkrs(p.ws + WS_AGG, id.R * 640 * 16);
    LAS float* pe = (LAS float*)LP(L_E + 8 * q * ESTR + ch * 4);
    HBAR();
    float er[8], ei[8];
#pragma unroll
    for (int s = 0; s < 8; ++s) { er[s] = pe[s * (ESTR / 4)]; ei[s] = pe[s * (ESTR / 4) + 64]; }
    float a = er[0], b = ei[0];
#pragma unroll
    for (int s = 1; s < 8; ++s) cm1(a, b, l8r, l8i, er[s], ei[s]);
    float ta[4], tb[4];
    gather4(a, ta); gather4(b, tb);
    float pa = 0.f, pb = 0.f;
    a = ta[0]; b = tb[0];
#pragma unroll
    for (int s = 1; s < 4; ++s) {
      if (q == s) { pa = a; pb = b; }
      cm1(a, b, l64r, l64i, ta[s], tb[s]);
    }
    st16c(v4u{__float_as_uint(a), tag, __float_as_uint(b), tag}, rctl, (q == 0 && id.R < 7) ? WS_AGG + (id.R * 640 + 64 * id.g + ch) * 16 : OOB);
    v4u p0 = ld16c(rag, (q * 640 + 64 * id.g + ch) * 16, 0), p1 = ld16c(rag, ((q + 4) * 640 + 64 * id.g + ch) * 16, 0);
    SB0();
    cm1(pa, pb, kr, ki, 0.f, 0.f);
#pragma unroll
    for (int s = 0; s < 7; ++s) cm1(er[s], ei[s], kr, ki, 0.f, 0.f);
    _Pragma("unroll 1") for (int spin = 0; spin < SPIN; ++spin) { FENCE();
      const bool ok = ((q >= id.R) | ((p0.y == tag) & (p0.w == tag))) & ((q + 4 >= id.R) | ((p1.y == tag) & (p1.w == tag)));
      if (__all(ok)) break;
      p0 = ld16c(rag, (q * 640 + 64 * id.g + ch) * 16, 0); p1 = ld16c(rag, ((q + 4) * 640 + 64 * id.g + ch) * 16, 0);
    }
    float ca = __uint_as_float(p1.x), cb = __uint_as_float(p1.z);
    cm1(ca, cb, w1r, w1i, 0.f, 0.f);
    { float x = __uint_as_float(p0.x), y = __uint_as_float(p0.z); cm1(x, y, w0r, w0i, ca, cb); ca = x; cb = y; }
    gather4(ca, ta); gather4(cb, tb);
    ca = (ta[0] + ta[1]) + (ta[2] + ta[3]); cb = (tb[0] + tb[1]) + (tb[2] + tb[3]);
    cm1(ca, cb, qr, qi, pa, pb);
#pragma unroll
    for (int s = 0; s < 8; ++s) {
      pe[s * (ESTR / 4)] = ca; pe[s * (ESTR / 4) + 64] = -cb;
      if (s < 7) cm1(ca, cb, l8r, l8i, er[s], ei[s]);
    }
    HBAR();
  }
}

__global__ __launch_bounds__(512, 2) void fused(Par p) {
  const int bid = blockIdx.x, xcd = bid & 7, idx = bid >> 3, a = xcd >> 2, b = xcd & 3, s = idx >> 2;
  Ids id;
  id.R = 4 * a + (idx & 3);
  const int ns = b < 2 ? 3 : 2, s0 = b < 2 ? 3 * b : 2 * b + 2, g0 = b < 2 ? 5 * b : 6 * b - 2;
  const bool ssm = s < ns;
  id.g = ssm ? s0 + s : g0 + s - ns;
  id.cg = ssm ? id.g : 10 + id.g;
  id.rt = 16 * a + 8 * (b >> 1) + (idx >> 2);
  id.ct = 4 * (b & 1) + (idx & 3);
  const int wv = rfl(threadIdx.x >> 6), lane = threadIdx.x & 63;
  unsigned enc_a = 0, enc_b = 0;
  if (ssm) { if (wv < 4) compute_wave<true>(p, id, wv, lane, enc_a, enc_b); else helper_wave<true>(p, id, wv - 4, lane, enc_a, enc_b); }
  else { if (wv < 4) compute_wave<false>(p, id, wv, lane, enc_a, enc_b); else helper_wave<false>(p, id, wv - 4, lane, enc_a, enc_b); }

  const __amdgpu_buffer_rsrc_t rctl = mkrs(p.ws, WS_CTL_BYTES);
  if (wv >= 4) lwait<8>(L_RDY, lrd(L_RDY), 1);
  const unsigned tag = rfl(*(LAS unsigned*)LP(L_TAG));
  if (wv < 4) {
    WAITV(0);
    unsigned old = 0;
    if (lane == 0) old = __hip_atomic_fetch_add((LAS unsigned*)LP(L_CNT), 1u, __ATOMIC_RELAXED, __HIP_MEMORY_SCOPE_WORKGROUP);
    st4c(tag, rctl, (lane == 0 && old == 3) ? WS_FLH + (id.R * 32 + id.cg) * 4 : OOB);
    lwait(L_RDY, lrd(L_RDY), 1);
  }
  STAMP(16);
  const __amdgpu_buffer_rsrc_t rw = mkrs(p.ws + WS_W2 + (unsigned)id.ct * (NKB * 5120), NKB * 5120);
  const __amdgpu_buffer_rsrc_t rh = mkrs(p.ws + WS_H + (unsigned)id.rt * (NKB * 4096), NKB * 4096);
  const unsigned vl = lane * 16;
  v4u wb[DW][5], hb[DH][4];
  auto loadw = [&](int it) {
#pragma unroll
    for (int i = 0; i < 5; ++i) wb[it % DW][i] = ld16c(rw, vl + (i & 3) * 1024, (8 * it + wv) * 5120 + (i >> 2) * 4096);
  };
  auto loadh = [&](int it) {
#pragma unroll
    for (int j = 0; j < 4; ++j) hb[it % DH][j] = ld16c(rh, vl + j * 1024, (8 * it + wv) * 4096);
  };
  SB0();
#pragma unroll
  for (int it = NL; it < NL + DW; ++it) { loadw(it); SB0(); }
  SB0();
  if (wv >= 4) {
    lwait<4>(L_FREE + (lane & 3) * 4, 0, 9);
    asm volatile("s_nop 4");
#pragma unroll
    for (int n = 0; n < 20; ++n) gldsc(rw, L_W + ((wv - 4) * 20 + n) * 1024, vl, ((wv - 4) * 20 + n) * 1024);
    lwait<4>(L_FREE + (lane & 3) * 4, 0, 10);
#pragma unroll
    for (int n = 0; n < 10; ++n) gldsc(rw, ((wv - 4) * 10 + n) * 1024, vl, (80 + (wv - 4) * 10 + n) * 1024);
  }
  SB0();
  if (ssm && wv >= 4) carries(p, id, wv - 4, lane);
  const unsigned fo = WS_FLH + ((id.rt >> 2) * 32 + (lane & 31)) * 4;
  if (wv >= 4) {
    WAITV(0);
    if (wv == 4) {
      _Pragma("unroll 1") for (int spin = 0; spin < SPIN; ++spin) { FENCE();
        if (__all((ld4c(rctl, fo) == tag) | ((lane & 31) < 10))) break;
        __builtin_amdgcn_s_sleep(1);
      }
    }
    FENCE();
    if (lane == 0) __hip_atomic_fetch_add((LAS unsigned*)LP(L_GO), 1u, __ATOMIC_RELAXED, __HIP_MEMORY_SCOPE_WORKGROUP);
  }
  _Pragma("unroll 1") for (int spin = 0; spin < 64 * SPIN; ++spin) { FENCE();
    if (rfl(__hip_atomic_load((LAS unsigned*)LP(L_GO), __ATOMIC_RELAXED, __HIP_MEMORY_SCOPE_WORKGROUP)) >= 4) break;
    __builtin_amdgcn_s_sleep(1);
  }
  FENCE();
  SB0();
  STAMP(20);
#pragma unroll
  for (int it = 0; it < DH; ++it) { loadh(it); SB0(); }
  f32x4 acc[4][5];
#pragma unroll
  for (int j = 0; j < 4; ++j)
#pragma unroll
    for (int i = 0; i < 5; ++i) acc[j][i] = f32x4{0.f, 0.f, 0.f, 0.f};
  const int tid = wv * 64 + lane;
  v2u xv[3], dv[3]; v4u sv[3]; int oo[3];
  unsigned sfl = 0;
  const lp pw = LP(L_W + wv * 5120 + vl), pw2 = LP(wv * 5120 + vl);
  v4u wl = l128(pw);
  SB0();
#pragma unroll
  for (int it = 0; it < NIT; ++it) {
    if (it < NL) {
#pragma unroll
      for (int i = 0; i < 5; ++i) {
        const v4u wf = wl;
        if (5 * it + i + 1 < 5 * NL) wl = l128((5 * it + i + 1) / 5 == 2 ? pw2 + ((5 * it + i + 1) % 5) * 1024 : pw + ((5 * it + i + 1) / 5) * 40960 + ((5 * it + i + 1) % 5) * 1024);
#pragma unroll
        for (int j = 0; j < 4; ++j) acc[j][i] = mfma16(wf, hb[it % DH][j], acc[j][i]);
        SB0();
      }
    } else {
#pragma unroll
      for (int j = 0; j < 4; ++j)
#pragma unroll
        for (int i = 0; i < 5; ++i) acc[j][i] = mfma16(wb[it % DW][i], hb[it % DH][j], acc[j][i]);
    }
    SB0();
    if (it + DH == 6) {
      _Pragma("unroll 1") for (int spin = 0; spin < SPIN; ++spin) { FENCE();
        if (__all(sfl == tag)) break;
        __builtin_amdgcn_s_sleep(1);
        sfl = ld4c(rctl, fo);
      }
      FENCE();
      STAMP(18);
    }
    if (it == 0) sfl = ld4c(rctl, fo);
    if (it + DH < NIT) loadh(it + DH);
    if (it >= NL && it + DW < NIT) loadw(it + DW);
    if (it == NIT - DH) {
#pragma unroll
      for (int r = 0; r < 3; ++r) {
        const int tl = min(wv + 8 * r, 19), j = tl / 5, i = tl - 5 * j;
        const int tok = 128 * (id.rt >> 1) + 8 * (lane & 15) + 4 * (id.rt & 1) + j, d = 80 * id.ct + 16 * i + 4 * (lane >> 4);
        oo[r] = tok * D_ + d;
        xv[r] = *(const v2u*)(p.x + oo[r]); dv[r] = *(const v2u*)(p.dsk + d);
        sv[r] = ld16c(rctl, WS_STATS + tok * 16, 0);
      }
      BAR();
    }
    SB0();
  }
  STAMP(21);
  const lp rb = LP(wv * 20480 + lane * 16);
#pragma unroll
  for (int j = 0; j < 4; ++j)
#pragma unroll
    for (int i = 0; i < 5; ++i) sf4(rb + (j * 5 + i) * 1024, acc[j][i]);
  BAR();
  STAMP(19);
#pragma unroll
  for (int r = 0; r < 3; ++r) {
    if (r < 2 || wv < 4) {
      const lp q0 = LP(tid * 16 + r * 8192), q1 = LP(tid * 16 + r * 8192 + 4 * 20480);
      f32x4 sum = ((lf4(q0) + lf4(q0 + 20480)) + (lf4(q0 + 40960) + lf4(q0 + 61440))) + ((lf4(q1) + lf4(q1 + 20480)) + (lf4(q1 + 40960) + lf4(q1 + 61440)));
      const f32x4 xf = {bflo(xv[r].x), bfhi(xv[r].x), bflo(xv[r].y), bfhi(xv[r].y)};
      const f32x4 df = {bflo(dv[r].x), bfhi(dv[r].x), bflo(dv[r].y), bfhi(dv[r].y)};
      sum += xf + (xf - __uint_as_float(sv[r].x)) * __uint_as_float(sv[r].z) * df;
      *(f32x4*)(p.out + oo[r]) = sum;
    }
  }
  STAMP(22);
}

extern "C" void kernel_launch(void* const* d_in, const int* in_sizes, int n_in, void* d_out, int out_size, void* d_ws, size_t ws_size, hipStream_t stream) {
  Par p;
  p.x = (const u16*)d_in[0]; p.lre = (const u16*)d_in[1]; p.lim = (const u16*)d_in[2];
  p.bre = (const u16*)d_in[3]; p.bim = (const u16*)d_in[4]; p.cre = (const u16*)d_in[5]; p.cim = (const u16*)d_in[6];
  p.ldt = (const u16*)d_in[7]; p.dsk = (const u16*)d_in[8];
  p.wg = (const u16*)d_in[10]; p.wu = (const u16*)d_in[11]; p.wd = (const u16*)d_in[12];
  p.ws = (unsigned char*)d_ws; p.out = (float*)d_out;
  (void)hipFuncSetAttribute((const void*)fused, hipFuncAttributeMaxDynamicSharedMemorySize, LDS1);
  fused<<<256, 512, LDS1, stream>>>(p);
}
```

```cpp
#define CODEPF 47104
#define OUTAUX 16
#include <hip/hip_runtime.h>
#include <stdint.h>

typedef unsigned short u16;
typedef unsigned v4u __attribute__((ext_vector_type(4)));
typedef unsigned v2u __attribute__((ext_vector_type(2)));
typedef float f32x4 __attribute__((ext_vector_type(4)));
typedef float f32x2 __attribute__((ext_vector_type(2)));
typedef float f32x16 __attribute__((ext_vector_type(16)));
typedef short bf16x8 __attribute__((ext_vector_type(8)));
typedef short s16x4 __attribute__((ext_vector_type(4)));
typedef __bf16 bf2 __attribute__((ext_vector_type(2)));
#define LAS __attribute__((address_space(3)))
#define DI __device__ __forceinline__
typedef LAS unsigned char* lp;

constexpr int L_ = 2048, D_ = 640, P_ = 640, F_ = 1728;
constexpr int NKB = 94;
constexpr unsigned WS_EPOCH = 0;
constexpr unsigned WS_FLW = 2048;
constexpr unsigned WS_FLH = 3072;
constexpr unsigned WS_AGG = 4096;
constexpr unsigned WS_STATS = WS_AGG + 8 * 640 * 16;
constexpr unsigned WS_CTL_BYTES = WS_STATS + 32768;
constexpr unsigned WS_W2 = 1u << 20;
constexpr unsigned WS_W2_BYTES = 8 * NKB * 5120;
constexpr unsigned WS_H = 8u << 20;
constexpr unsigned WS_H_BYTES = 32 * NKB * 4096;

constexpr int UPO = 10368;
constexpr int STG = 32768 + 20480 + 128;
constexpr int LDS1 = 163840;
constexpr int L_STATS = 3 * STG;
constexpr int L_CS = L_STATS + 2048;
constexpr int L_CNT = L_CS + 1280, L_GO = L_CNT + 4, L_RDY = L_CNT + 8, L_CSF = L_CNT + 16;
constexpr int L_CST = 49152;
constexpr int L_W = STG;
constexpr int L_E = 136000, ESTR = 528;
constexpr int SPIN = 1 << 18;
constexpr unsigned OOB = 0x40000000u;

DI float bf2f(unsigned b) { return __uint_as_float(b << 16); }
DI float bflo(unsigned w) { return __uint_as_float(w << 16); }
DI float bfhi(unsigned w) { return __uint_as_float(w & 0xffff0000u); }
DI unsigned pk2(float a, float b) { f32x2 v = {a, b}; return __builtin_bit_cast(unsigned, __builtin_convertvector(v, bf2)); }
DI float rbf(float a) { return bflo(pk2(a, a) & 0xffffu); }
DI f32x4 mfma16(v4u a, v4u b, f32x4 c) { return __builtin_amdgcn_mfma_f32_16x16x32_bf16(__builtin_bit_cast(bf16x8, a), __builtin_bit_cast(bf16x8, b), c, 0, 0, 0); }
DI f32x16 mfma32(v4u a, v4u b, f32x16 c) { return __builtin_amdgcn_mfma_f32_32x32x16_bf16(__builtin_bit_cast(bf16x8, a), __builtin_bit_cast(bf16x8, b), c, 0, 0, 0); }
DI unsigned rfl(unsigned v) { return __builtin_amdgcn_readfirstlane(v); }
DI __amdgpu_buffer_rsrc_t mkrs(const void* p, unsigned bytes) { return __builtin_amdgcn_make_buffer_rsrc((void*)p, 0, bytes, 0x00020000); }
DI void glds(__amdgpu_buffer_rsrc_t rs, unsigned ldsaddr, unsigned voff, unsigned soff) {
  asm volatile("s_mov_b32 m0, %0\n\ts_nop 0\n\tbuffer_load_dwordx4 %1, %2, %3 offen lds" ::"s"(ldsaddr), "v"(voff), "s"(rs), "s"(soff));
}
DI void gldsc(__amdgpu_buffer_rsrc_t rs, unsigned ldsaddr, unsigned voff, unsigned soff) {
  asm volatile("s_mov_b32 m0, %0\n\ts_nop 0\n\tbuffer_load_dwordx4 %1, %2, %3 offen sc1 lds" ::"s"(ldsaddr), "v"(voff), "s"(rs), "s"(soff));
}
DI lp LP(unsigned a) { return (lp)(uintptr_t)a; }
DI v4u l128(lp a) { return *(LAS v4u*)a; }
DI f32x4 lf4(lp a) { return *(LAS f32x4*)a; }
DI void sf4(lp a, f32x4 v) { *(LAS f32x4*)a = v; }
DI s16x4 ltr(lp a) { return __builtin_amdgcn_ds_read_tr16_b64_v4i16((LAS s16x4*)a); }
DI v4u cat(s16x4 lo, s16x4 hi) { return __builtin_bit_cast(v4u, __builtin_shufflevector(lo, hi, 0, 1, 2, 3, 4, 5, 6, 7)); }
DI v4u ld16(__amdgpu_buffer_rsrc_t r, unsigned vo, unsigned so) { return __builtin_amdgcn_raw_buffer_load_b128(r, vo, so, 0); }
DI v4u ld16c(__amdgpu_buffer_rsrc_t r, unsigned vo, unsigned so) { return __builtin_amdgcn_raw_buffer_load_b128(r, vo, so, 16); }
DI unsigned ld4c(__amdgpu_buffer_rsrc_t r, unsigned vo) { return __builtin_amdgcn_raw_buffer_load_b32(r, vo, 0, 16); }
DI void st16c(v4u v, __amdgpu_buffer_rsrc_t r, unsigned vo) { __builtin_amdgcn_raw_buffer_store_b128(v, r, vo, 0, 16); }
DI void st4c(unsigned v, __amdgpu_buffer_rsrc_t r, unsigned vo) { __builtin_amdgcn_raw_buffer_store_b32(v, r, vo, 0, 16); }
#define BAR() asm volatile("s_waitcnt lgkmcnt(0)\n\ts_barrier" ::: "memory")
#define SB0() __builtin_amdgcn_sched_barrier(0)
#define WAITV(n) asm volatile("s_waitcnt vmcnt(%0)" ::"i"(n) : "memory")
#define FENCE() asm volatile("" ::: "memory")
template <int C> DI float dppf(float v) { return __int_as_float(__builtin_amdgcn_update_dpp(0, __float_as_int(v), C, 0xf, 0xf, true)); }
template <int N> DI f32x4 shr4(f32x4 v) { return f32x4{dppf<0x110 + N>(v.x), dppf<0x110 + N>(v.y), dppf<0x110 + N>(v.z), dppf<0x110 + N>(v.w)}; }
DI float sum8(float v) {
  v += dppf<0xB1>(v); v += dppf<0x4E>(v); v += dppf<0x141>(v);
  return v;
}
DI float dot2(unsigned a, unsigned b, float c) { return __builtin_amdgcn_fdot2_f32_bf16(__builtin_bit_cast(bf2, a), __builtin_bit_cast(bf2, b), c, false); }
DI void gather4(float x, float (&t)[4]) {
  const auto a = __builtin_amdgcn_permlane16_swap(__float_as_uint(x), __float_as_uint(x), false, false);
  const auto e = __builtin_amdgcn_permlane32_swap(a[0], a[0], false, false), o = __builtin_amdgcn_permlane32_swap(a[1], a[1], false, false);
  t[0] = __uint_as_float(e[0]); t[2] = __uint_as_float(e[1]); t[1] = __uint_as_float(o[0]); t[3] = __uint_as_float(o[1]);
}
DI void cm1(float& a, float& b, float lr, float li, float ur, float ui) {
  const float na = fmaf(lr, a, fmaf(-li, b, ur)), nb = fmaf(lr, b, fmaf(li, a, ui));
  a = na; b = nb;
}
DI f32x4 fma4(f32x4 a, f32x4 b, f32x4 c) { return __builtin_elementwise_fma(a, b, c); }
DI f32x4 sp4(float a) { return f32x4{a, a, a, a}; }
DI void cmac(f32x4& a, f32x4& b, f32x4 lr, f32x4 li, f32x4 ur, f32x4 ui) {
  const f32x4 na = fma4(lr, a, fma4(-li, b, ur)), nb = fma4(lr, b, fma4(li, a, ui));
  a = na; b = nb;
}

#define STAMP(i)
#define STAMPC(i)

#define LSTAMP(i)
struct Par {
  const u16 *x, *lre, *lim, *bre, *bim, *cre, *cim, *ldt, *dsk, *wg, *wu, *wd;
  unsigned char* ws;
  float* out;
};
struct Ids { int R, g, cg, rt, ct; };

#define PDW 4
#define PDH 3
constexpr int DW = PDW, DH = PDH, NIT = 12, NL = 3;
#define PIN4(x) asm volatile("" : "+v"(x))
DI void sig(unsigned addr, unsigned v) {
  asm volatile("s_mov_b64 exec, 1\n\tds_write_b32 %0, %1\n\ts_mov_b64 exec, -1" ::"v"(addr), "v"(v) : "memory");
}
DI unsigned lrd(unsigned addr) { return *(volatile LAS unsigned*)LP(addr); }
#define LSLEEP 1
template <int SL = 1> DI void lwait(unsigned addr, unsigned v, unsigned need) {
  if (__builtin_expect(!__all(v >= need), 0)) {
    _Pragma("unroll 1") for (int spin = 0; spin < 64 * SPIN; ++spin) { FENCE();
      if (__all(lrd(addr) >= need)) break;
      __builtin_amdgcn_s_sleep(SL);
    }
  }
  FENCE();
}

DI float wsum(float v) {
  v = sum8(v); v += dppf<0x128>(v);
  float t[4]; gather4(v, t);
  return (t[0] + t[1]) + (t[2] + t[3]);
}
DI unsigned mktag(unsigned epoch) { return rfl(epoch) * 0x9E3779B1u + 0x7F4A7C15u; }
DI unsigned row_stats(const Par& p, const Ids& id, const int w, const int lane) {
  const __amdgpu_buffer_rsrc_t rctl = mkrs(p.ws, WS_CTL_BYTES);
  const int row = 256 * id.R + 8 * id.cg + 2 * w;
  v4u xa[2]; unsigned xb[2];
#pragma unroll
  for (int z = 0; z < 2; ++z) { xa[z] = *(const v4u*)(p.x + (row + z) * D_ + lane * 8); xb[z] = *(const unsigned*)(p.x + (row + z) * D_ + 512 + lane * 2); }
  const unsigned epoch = ld4c(rctl, WS_EPOCH);
  float v[2][10], mean[2], s2[2];
#pragma unroll
  for (int z = 0; z < 2; ++z) {
    const float t[10] = {bflo(xa[z].x), bfhi(xa[z].x), bflo(xa[z].y), bfhi(xa[z].y), bflo(xa[z].z), bfhi(xa[z].z), bflo(xa[z].w), bfhi(xa[z].w), bflo(xb[z]), bfhi(xb[z])};
#pragma unroll
    for (int e = 0; e < 10; ++e) v[z][e] = t[e];
    mean[z] = wsum(((t[0] + t[1]) + (t[2] + t[3])) + ((t[4] + t[5]) + (t[6] + t[7])) + (t[8] + t[9])) * (1.f / D_);
  }
#pragma unroll
  for (int z = 0; z < 2; ++z) {
    float a = 0.f, b = 0.f;
#pragma unroll
    for (int e = 0; e < 5; ++e) { a = fmaf(v[z][e] - mean[z], v[z][e] - mean[z], a); b = fmaf(v[z][5 + e] - mean[z], v[z][5 + e] - mean[z], b); }
    s2[z] = wsum(a + b);
  }
  const unsigned tag = mktag(epoch);
  const float m = rbf((lane & 1) ? mean[1] : mean[0]), rs = 1.f / rbf(sqrtf(rbf(rbf(((lane & 1) ? s2[1] : s2[0]) * (1.f / D_)) + 1e-5f)));
  st16c(v4u{__float_as_uint(m), tag, __float_as_uint(rs), tag}, rctl, lane < 2 ? WS_STATS + (row + lane) * 16 : OOB);
  return tag;
}

template <bool SSM>
DI unsigned compute_wave(const Par& p, const Ids& id, const int w, const int lane) {
  STAMP(0);
  unsigned tag, cf = 0;
  if constexpr (!SSM) {
    const int r = lane & 31, hh = lane >> 5;
    const unsigned xa = (64 * w + r) * 128 + ((hh ^ ((r >> 1) & 7)) * 16);
    const unsigned wa = 32768 + ((lane >> 4) & 1) * UPO + (8 * hh + ((lane & 15) >> 2)) * 160 + (lane & 3) * 8;
    f32x16 acc[5][2] = {};
#pragma unroll
    for (int e = 0; e < 5; ++e) { PIN4(acc[e][0]); PIN4(acc[e][1]); }
    v4u fx[2][2]; s16x4 fl[4], fh[4];
    lp px[4], pw;
    auto setp = [&](unsigned sb) {
#pragma unroll
      for (int s = 0; s < 4; ++s) px[s] = LP(sb + (xa ^ (32 * s)));
      pw = LP(sb + wa);
    };
    auto woff = [&](int n) { return (n / 5) * 2560 + (n % 5) * 32; };
    setp(0);
    SB0();
    tag = row_stats(p, id, w, lane);
    STAMP(17);
    SB0();
    BAR();
    STAMP(1);
    fx[0][0] = l128(px[0]); fx[0][1] = l128(px[0] + 4096);
#pragma unroll
    for (int n = 0; n < 3; ++n) { fl[n] = ltr(pw + woff(n)); fh[n] = ltr(pw + woff(n) + 640); }
    unsigned sb = 0;
#pragma unroll 1
    for (int i = 0; i < 10; ++i) {
#pragma unroll
      for (int n = 0; n < 20; ++n) {
        const int ks = n / 5, e = n % 5, nn = (n + 3) % 20;
        if (n == 17) { if (i < 9) BAR(); LSTAMP(i); sb = sb == 2 * STG ? 0 : sb + STG; setp(sb); SB0(); }
        const v4u wf = cat(fl[n & 3], fh[n & 3]);
        acc[e][0] = mfma32(wf, fx[ks & 1][0], acc[e][0]); SB0();
        fl[(n + 3) & 3] = ltr(pw + woff(nn));
        fh[(n + 3) & 3] = ltr(pw + woff(nn) + 640); SB0();
        acc[e][1] = mfma32(wf, fx[ks & 1][1], acc[e][1]); SB0();
        if (ks < 3 && e == 1) { fx[(ks + 1) & 1][0] = l128(px[ks + 1]); fx[(ks + 1) & 1][1] = l128(px[ks + 1] + 4096); }
        if (n == 17) { fx[0][0] = l128(px[0]); fx[0][1] = l128(px[0] + 4096); }
        if (n == 18) cf = lrd(L_CSF + (lane & 3) * 4);
        SB0();
      }
    }
    lwait(L_CSF + (lane & 3) * 4, cf, 1);
    STAMP(12);
    const __amdgpu_buffer_rsrc_t rh = mkrs(p.ws + WS_H + (unsigned)(4 * id.R + w) * (NKB * 4096), 54 * 4096);
    const unsigned so0 = (r >> 4) * 1024 + (hh * 16 + (r & 15)) * 16;
    float mu[2], r2[2];
#pragma unroll
    for (int u = 0; u < 2; ++u) {
      const int F = 4 * w + 2 * u + (r >> 4);
      const f32x2 s = *(LAS f32x2*)LP(L_STATS + (128 * (F >> 3) + 8 * (r & 15) + (F & 7)) * 8);
      mu[u] = s.x; r2[u] = s.y * s.y;
    }
    const lp pc = LP(L_CS + hh * 16);
#pragma unroll
    for (int e = 0; e < 5; ++e) {
      const f32x4 g0 = lf4(pc + e * 64) + lf4(pc + e * 64 + 640), g1 = lf4(pc + e * 64 + 32) + lf4(pc + e * 64 + 672);
      const f32x4 u0 = lf4(pc + e * 64 + 320) + lf4(pc + e * 64 + 960), u1 = lf4(pc + e * 64 + 352) + lf4(pc + e * 64 + 992);
      const int T = 5 * id.g + e;
      const unsigned so = so0 + (T >> 1) * 4096 + (T & 1) * 512;
#pragma unroll
      for (int u = 0; u < 2; ++u) {
        const f32x16 a = acc[e][u];
        const f32x4 nm = sp4(-mu[u]);
        const f32x4 h0 = fma4(nm, g0, f32x4{a[0], a[1], a[2], a[3]}) * fma4(nm, u0, f32x4{a[8], a[9], a[10], a[11]}) * r2[u];
        const f32x4 h1 = fma4(nm, g1, f32x4{a[4], a[5], a[6], a[7]}) * fma4(nm, u1, f32x4{a[12], a[13], a[14], a[15]}) * r2[u];
        st16c(v4u{pk2(h0.x, h0.y), pk2(h0.z, h0.w), pk2(h1.x, h1.y), pk2(h1.z, h1.w)}, rh, so + u * 2048);
      }
    }
  } else {
    const int c = lane & 15, q = lane >> 4, G = w >> 1, blk = w & 1;
    const unsigned xa = (128 * G + c) * 128 + ((q ^ (c >> 1)) * 16);
    const unsigned wa = 32768 + (32 * blk + c) * 128 + ((q ^ (c >> 1)) * 16);
    f32x4 acc[8][4] = {};
#pragma unroll
    for (int j = 0; j < 8; ++j)
#pragma unroll
      for (int t = 0; t < 4; ++t) PIN4(acc[j][t]);
    v4u fw[2][4], fx[4];
    lp px[2], pw[2];
    auto setp = [&](unsigned sb) { px[0] = LP(sb + xa); px[1] = LP(sb + (xa ^ 64)); pw[0] = LP(sb + wa); pw[1] = LP(sb + (wa ^ 64)); };
    auto woff = [&](int t) { return (t >> 1) * 8192 + (t & 1) * 2048; };
    setp(0);
    SB0();
    tag = row_stats(p, id, w, lane);
    STAMP(17);
    SB0();
    BAR();
    STAMP(1);
#pragma unroll
    for (int t = 0; t < 4; ++t) fw[0][t] = l128(pw[0] + woff(t));
#pragma unroll
    for (int n = 0; n < 3; ++n) fx[n] = l128(px[0] + n * 2048);
    unsigned sb = 0;
#pragma unroll 1
    for (int i = 0; i < 10; ++i) {
#pragma unroll
      for (int n = 0; n < 16; ++n) {
        const int kk = n >> 3, j = n & 7, nn = (n + 3) & 15;
        if (n == 13) { if (i < 9) BAR(); LSTAMP(i); sb = sb == 2 * STG ? 0 : sb + STG; setp(sb); SB0(); }
        const v4u xf = fx[n & 3];
        acc[j][0] = mfma16(fw[kk][0], xf, acc[j][0]); SB0();
        fx[(n + 3) & 3] = l128(px[nn >> 3] + (nn & 7) * 2048); SB0();
        acc[j][1] = mfma16(fw[kk][1], xf, acc[j][1]); SB0();
        if (n >= 2 && n < 6) fw[1][n - 2] = l128(pw[1] + woff(n - 2));
        if (n == 13) fw[0][0] = l128(pw[0] + woff(0));
        if (n == 14) fw[0][2] = l128(pw[0] + woff(2));
        SB0();
        acc[j][2] = mfma16(fw[kk][2], xf, acc[j][2]); SB0();
        if (n == 13) fw[0][1] = l128(pw[0] + woff(1));
        if (n == 14) fw[0][3] = l128(pw[0] + woff(3));
        SB0();
        acc[j][3] = mfma16(fw[kk][3], xf, acc[j][3]); SB0();
        if (n == 14) { cf = lrd(L_CSF + (lane & 3) * 4); SB0(); }
      }
    }
    lwait(L_CSF + (lane & 3) * 4, cf, 1);
    STAMP(12);
    const lp pe = LP(L_E + (16 * G + c) * ESTR + (32 * blk + 8 * q) * 4);
    const lp pk = LP(L_CST + (32 * blk + 8 * q) * 4);
    f32x4 lr[2], li[2], er[2], ei[2];
    {
      const lp ps = LP(L_STATS + (128 * G + 8 * c) * 8), pc = LP(L_CS + (32 * blk + 8 * q) * 4);
      f32x4 cs[4], st[4];
#pragma unroll
      for (int t = 0; t < 4; ++t) { cs[t] = lf4(pc + (t >> 1) * 320 + (t & 1) * 16) + lf4(pc + 640 + (t >> 1) * 320 + (t & 1) * 16); st[t] = lf4(ps + t * 16); }
#pragma unroll
      for (int ab = 0; ab < 2; ++ab) { lr[ab] = lf4(pk + ab * 16); li[ab] = lf4(pk + 256 + ab * 16); }
#pragma unroll
      for (int jj = 0; jj < 4; ++jj)
#pragma unroll
        for (int t = 0; t < 4; ++t) {
          acc[2 * jj][t] = fma4(sp4(-st[jj].x), cs[t], acc[2 * jj][t]) * st[jj].y;
          acc[2 * jj + 1][t] = fma4(sp4(-st[jj].z), cs[t], acc[2 * jj + 1][t]) * st[jj].w;
        }
    }
#pragma unroll
    for (int ab = 0; ab < 2; ++ab) { er[ab] = acc[0][ab]; ei[ab] = acc[0][2 + ab]; }
#pragma unroll
    for (int j = 1; j < 8; ++j)
#pragma unroll
      for (int ab = 0; ab < 2; ++ab) cmac(er[ab], ei[ab], lr[ab], li[ab], acc[j][ab], acc[j][2 + ab]);
#pragma unroll
    for (int ab = 0; ab < 2; ++ab) { sf4(pe + ab * 16, er[ab]); sf4(pe + 256 + ab * 16, ei[ab]); }
    SB0();
    BAR();
    STAMP(13);
#pragma unroll
    for (int ab = 0; ab < 2; ++ab) {
      const f32x4 kr = lf4(pk + 512 * 4 + ab * 16), ki = lf4(pk + 512 * 4 + 256 + ab * 16);
#pragma unroll
      for (int j = 0; j < 8; ++j) {
        const f32x4 ur = acc[j][ab], ui = acc[j][2 + ab];
        acc[j][ab] = fma4(-ki, ui, kr * ur); acc[j][2 + ab] = fma4(-ki, ur, -kr * ui);
        PIN4(acc[j][ab]); PIN4(acc[j][2 + ab]);
      }
      li[ab] = -li[ab];
    }
    const __amdgpu_buffer_rsrc_t rh = mkrs(p.ws + WS_H + (unsigned)(4 * id.R + 2 * G) * (NKB * 4096), 2 * NKB * 4096);
    unsigned vo[4];
#pragma unroll
    for (int z = 0; z < 4; ++z) { vo[z] = (54 + 2 * id.g + blk + 20 * (z & 1) + NKB * (z >> 1)) * 4096 + lane * 16; PIN4(vo[z]); }
    SB0();
    BAR();
    STAMP(14);
#pragma unroll
    for (int ab = 0; ab < 2; ++ab) { er[ab] = lf4(pe + ab * 16); ei[ab] = lf4(pe + 256 + ab * 16); }
#pragma unroll
    for (int j = 0; j < 8; ++j) {
#pragma unroll
      for (int ab = 0; ab < 2; ++ab) cmac(er[ab], ei[ab], lr[ab], li[ab], acc[j][ab], acc[j][2 + ab]);
      st16c(v4u{pk2(er[0].x, er[0].y), pk2(er[0].z, er[0].w), pk2(er[1].x, er[1].y), pk2(er[1].z, er[1].w)}, rh, vo[2 * (j >> 2)] + (j & 3) * 1024);
      st16c(v4u{pk2(ei[0].x, ei[0].y), pk2(ei[0].z, ei[0].w), pk2(ei[1].x, ei[1].y), pk2(ei[1].z, ei[1].w)}, rh, vo[2 * (j >> 2) + 1] + (j & 3) * 1024);
    }
  }
  SB0();
  STAMP(15);
  return tag;
}

#define HBAR() do { SB0(); BAR(); SB0(); } while (0)
template <bool SSM>
DI unsigned helper_wave(const Par& p, const Ids& id, const int h, const int lane) {
  STAMP(0);
  constexpr int NWP = SSM ? 4 : 5, NP = 8 + NWP;
  constexpr unsigned WSTEP = SSM ? 128u : 64u * 3456u;
  const int c = lane & 15, q = lane >> 4;
  const __amdgpu_buffer_rsrc_t rctl = mkrs(p.ws, WS_CTL_BYTES);
  const __amdgpu_buffer_rsrc_t rx = mkrs(p.x, L_ * D_ * 2);
  const __amdgpu_buffer_rsrc_t rw = mkrs(SSM ? ((h >> 1) ? p.bim : p.bre) : ((h >> 1) ? p.wu : p.wg), SSM ? P_ * D_ * 2 : D_ * F_ * 2);
  const unsigned v0 = (8 * (lane >> 3)) * 1280 + (((lane & 7) ^ (lane >> 4)) * 16), v1 = v0 ^ 64;
  unsigned sx = (256 * id.R + 128 * (h >> 1) + 4 * (h & 1)) * 1280;
  auto issue_x = [&](unsigned s, unsigned sb) {
#pragma unroll
    for (int n = 0; n < 8; ++n) glds(rx, sb + h * 8192 + n * 1024, (n & 1) ? v1 : v0, s + (64 * (n & 1) + (n >> 1)) * 1280);
  };
  asm volatile("s_nop 4");
  issue_x(sx, 0);
  unsigned vw[NWP];
  if constexpr (SSM) {
    vw[0] = (8 * (lane >> 5) + ((lane >> 3) & 3)) * 1280 + (((lane & 7) ^ (lane >> 4)) * 16); vw[1] = vw[0] ^ 64;
  } else {
#pragma unroll
    for (int i = 0; i < 5; ++i) { const int lc = 64 * (5 * (h & 1) + i) + lane, pos = lc / 10; vw[i] = pos * 3456 + (lc - pos * 10) * 16; }
  }
  unsigned sw = SSM ? (64 * id.g + 32 * (h & 1)) * 1280 : 160 * id.g;
  const unsigned dw = 32768 + (h >> 1) * (SSM ? 8192 : UPO) + (h & 1) * NWP * 1024;
  auto issue_w = [&](unsigned s, unsigned sb) {
#pragma unroll
    for (int n = 0; n < NWP; ++n) {
      if constexpr (SSM) glds(rw, sb + dw + n * 1024, vw[n & 1], s + (16 * (n & 1) + 4 * (n >> 1)) * 1280);
      else glds(rw, sb + dw + n * 1024, vw[n], s);
    }
  };
  issue_w(sw, 0);
  sx += 128; sw += WSTEP;
  unsigned sb = STG;
  auto step = [&]() {
    issue_x(sx, sb); issue_w(sw, sb);
    sx += 128; sw += WSTEP; sb = sb == 2 * STG ? 0 : sb + STG;
  };
  step();
  if (h == 3 && lane < 8) *(LAS unsigned*)LP(L_CNT + lane * 4) = 0u;
  f32x4 acs[NWP] = {};
  v4u fc[NWP];
  const unsigned ca = SSM ? 32768 + (h >> 1) * 8192 + c * 128 + (((4 * (h & 1) + q) ^ (c >> 1)) * 16)
                          : 32768 + (h >> 1) * UPO + (32 * (h & 1) + 4 * q + (c >> 2)) * 160 + (c & 3) * 8;
  const v4u ones = {0x3F803F80u, 0x3F803F80u, 0x3F803F80u, 0x3F803F80u};
  auto cs_rd = [&](unsigned s) {
    const lp a = LP(s + ca);
#pragma unroll
    for (int t = 0; t < NWP; ++t) { if constexpr (SSM) fc[t] = l128(a + t * 2048); else fc[t] = cat(ltr(a + t * 32), ltr(a + t * 32 + 2560)); }
    SB0();
  };
  auto cs_mm = [&]() {
    SB0();
    __builtin_amdgcn_s_setprio(2);
#pragma unroll
    for (int t = 0; t < NWP; ++t) acs[t] = mfma16(fc[t], ones, acs[t]);
    __builtin_amdgcn_s_setprio(0);
    SB0();
  };
  WAITV(NP); BAR();
  cs_rd(0);
  step();
  const int n = blockIdx.x * 4 + h, kb = n / 10, dq = n - 10 * kb;
  const bool wd = kb < 54;
  v4u f[4];
  unsigned epoch, pl = 0, pr = 0, pi = 0;
  {
    const __amdgpu_buffer_rsrc_t r = mkrs(wd ? p.wd : kb < 74 ? p.cre : p.cim, kb < NKB ? (wd ? F_ * D_ * 2 : D_ * P_ * 2) : 0);
    const unsigned vo = wd ? (32 * kb + 16 * (c >> 3) + 4 * ((c >> 2) & 1) + (c & 3)) * 1280 + dq * 128 + q * 16
                           : (64 * dq + c) * 1280 + (kb < 74 ? kb - 54 : kb - 74) * 64 + q * 16;
#pragma unroll
    for (int i = 0; i < 4; ++i) f[i] = ld16(r, vo, wd ? (i >> 1) * 64 + (i & 1) * (8 * 1280) : i * (16 * 1280));
    epoch = ld4c(rctl, WS_EPOCH);
    if constexpr (SSM) { pl = p.ldt[64 * id.g + lane]; pr = p.lre[64 * id.g + lane]; pi = p.lim[64 * id.g + lane]; }
  }
  cs_mm();
  WAITV(NP + (SSM ? 8 : 5)); BAR();
  cs_rd(STG); step(); cs_mm();
  WAITV(NP); BAR();
  cs_rd(2 * STG);
  asm volatile("" : "+v"(f[0]), "+v"(f[1]), "+v"(f[2]), "+v"(f[3]), "+v"(epoch), "+v"(pl), "+v"(pr), "+v"(pi));
  step();
  const unsigned tag = mktag(epoch);
  if (wd) {
    v4u sel[2], o[4];
#pragma unroll
    for (int hf = 0; hf < 2; ++hf)
#pragma unroll
      for (int e = 0; e < 4; ++e) sel[hf][e] = (8 * q + 2 * e == 16 * hf + c ? 0x3F80u : 0u) | (8 * q + 2 * e + 1 == 16 * hf + c ? 0x3F800000u : 0u);
    __builtin_amdgcn_s_setprio(2);
#pragma unroll
    for (int i = 0; i < 4; ++i) {
      const f32x4 a = mfma16(f[i & ~1], sel[i & 1], f32x4{0.f, 0.f, 0.f, 0.f}), b = mfma16(f[i | 1], sel[i & 1], f32x4{0.f, 0.f, 0.f, 0.f});
      o[i] = v4u{pk2(a.x, a.y), pk2(a.z, a.w), pk2(b.x, b.y), pk2(b.z, b.w)};
    }
#pragma unroll
    for (int i = 0; i < 4; ++i) f[i] = o[i];
  }
  cs_mm();
  WAITV(NP); BAR();
  cs_rd(0);
  {
    const __amdgpu_buffer_rsrc_t r2 = mkrs(p.ws + WS_W2, WS_W2_BYTES);
#pragma unroll
    for (int i = 0; i < 4; ++i) {
      const int dt = 4 * dq + i, ct = dt / 5;
      st16c(f[i], r2, kb < NKB ? ((ct * NKB + kb) * 5 + dt - 5 * ct) * 1024 + lane * 16 : OOB);
    }
    SB0();
  }
  step(); cs_mm();
  WAITV(NP + 4); BAR();
  cs_rd(STG); step(); cs_mm();
  WAITV(NP); BAR();
  cs_rd(2 * STG);
  st4c(tag, rctl, (h == 3 && lane == 0) ? WS_FLW + blockIdx.x * 4 : OOB);
  SB0();
  step(); cs_mm();
  WAITV(NP + 1); BAR();
  cs_rd(0);
  v4u sv;
  asm volatile("buffer_load_dwordx4 %0, %1, %2, 0 offen sc1" : "=v"(sv) : "v"(WS_STATS + (256 * id.R + 64 * h + lane) * 16), "s"(rctl));
  step(); cs_mm();
  asm volatile("s_waitcnt vmcnt(%1)" : "+v"(sv) : "i"(NP) : "memory");
  if (!__all((sv.y == tag) & (sv.w == tag))) {
    _Pragma("unroll 1") for (int spin = 0; spin < SPIN; ++spin) { FENCE();
      sv = ld16c(rctl, WS_STATS + (256 * id.R + 64 * h + lane) * 16, 0);
      if (__all((sv.y == tag) & (sv.w == tag))) break;
      __builtin_amdgcn_s_sleep(4);
    }
  }
  *(LAS v2u*)LP(L_STATS + (64 * h + lane) * 8) = v2u{sv.x, sv.z};
  BAR();
  cs_rd(STG); step(); cs_mm();
  WAITV(NP); BAR();
  cs_rd(2 * STG);
  if (h == 3) {
    _Pragma("unroll 1") for (int spin = 0; spin < SPIN; ++spin) { FENCE();
      const v4u f2 = ld16c(rctl, WS_FLW + lane * 16, 0);
      if (__all((f2.x == tag) & (f2.y == tag) & (f2.z == tag) & (f2.w == tag))) break;
      __builtin_amdgcn_s_sleep(4);
    }
    st4c(tag, rctl, (lane == 0 && blockIdx.x == 255) ? WS_EPOCH : OOB);
  }
  if (SSM && h == 2) {
    const float dt = rbf(expf(bf2f(pl))), lr = bf2f(pr), li = bf2f(pi);
    const float ex = expf(dt * lr);
    float sn, cn;
    sincosf(dt * li, &sn, &cn);
    float br = ex * cn, bi = ex * sn;
    const float nr = br - 1.f, ni = bi, den = lr * lr + li * li;
    LAS float* cst = (LAS float*)LP(L_CST);
    cst[4 * 128 + lane] = 2.f * (nr * lr + ni * li) / den; cst[4 * 128 + 64 + lane] = 2.f * (ni * lr - nr * li) / den;
    cst[lane] = br; cst[64 + lane] = bi;
#pragma unroll
    for (int s = 1; s <= 8; ++s) {
      const float a = br * br - bi * bi, b = 2.f * br * bi;
      br = a; bi = b;
      const int k = s == 3 ? 1 : s == 6 ? 2 : s == 8 ? 3 : 0;
      if (k) { cst[k * 128 + lane] = br; cst[k * 128 + 64 + lane] = bi; }
    }
  }
  cs_mm();
  WAITV(0); BAR();
  cs_rd(0); cs_mm();
  if (c == 0) {
#pragma unroll
    for (int t = 0; t < NWP; ++t) {
      const int col = SSM ? 32 * (t >> 1) + 8 * q + 4 * (t & 1) : 16 * t + 4 * q;
      sf4(LP(L_CS + ((h & 1) * 160 + (h >> 1) * 80 + col) * 4), acs[t]);
    }
  }
  sig(L_CSF + h * 4, 1);
  return tag;
}

DI void carries(const Par& p, const Ids& id, const int h, const int lane, const unsigned tag) {
  const __amdgpu_buffer_rsrc_t rctl = mkrs(p.ws, WS_CTL_BYTES);
  const int c = lane & 15, q = lane >> 4;
  {
    const int ch = 16 * h + c;
    LAS float* cst = (LAS float*)LP(L_CST);
    const float l8r = cst[128 + ch], l8i = cst[192 + ch], l64r = cst[256 + ch], l64i = cst[320 + ch], lwr = cst[384 + ch], lwi = cst[448 + ch];
    const float kr = cst[512 + ch], ki = cst[576 + ch];
    float w0r = 1.f, w0i = 0.f, w1r = 1.f, w1i = 0.f, qr = kr, qi = ki;
#pragma unroll
    for (int s = 0; s < 6; ++s) {
      float a = w0r, b = w0i; cm1(a, b, lwr, lwi, 0.f, 0.f);
      if (s < id.R - 1 - q) { w0r = a; w0i = b; }
      if (s < id.R - 5 - q) { w1r = a; w1i = b; }
    }
#pragma unroll
    for (int s = 0; s < 3; ++s) { float a = qr, b = qi; cm1(a, b, l64r, l64i, 0.f, 0.f); if (s < q) { qr = a; qi = b; } }
    const __amdgpu_buffer_rsrc_t rag = mkrs(p.ws + WS_AGG, id.R * 640 * 16);
    LAS float* pe = (LAS float*)LP(L_E + 8 * q * ESTR + ch * 4);
    HBAR();
    float er[8], ei[8];
#pragma unroll
    for (int s = 0; s < 8; ++s) { er[s] = pe[s * (ESTR / 4)]; ei[s] = pe[s * (ESTR / 4) + 64]; }
    float a = er[0], b = ei[0];
#pragma unroll
    for (int s = 1; s < 8; ++s) cm1(a, b, l8r, l8i, er[s], ei[s]);
    float ta[4], tb[4];
    gather4(a, ta); gather4(b, tb);
    float pa = 0.f, pb = 0.f;
    a = ta[0]; b = tb[0];
#pragma unroll
    for (int s = 1; s < 4; ++s) {
      if (q == s) { pa = a; pb = b; }
      cm1(a, b, l64r, l64i, ta[s], tb[s]);
    }
    st16c(v4u{__float_as_uint(a), tag, __float_as_uint(b), tag}, rctl, (q == 0 && id.R < 7) ? WS_AGG + (id.R * 640 + 64 * id.g + ch) * 16 : OOB);
    v4u p0 = ld16c(rag, (q * 640 + 64 * id.g + ch) * 16, 0), p1 = ld16c(rag, ((q + 4) * 640 + 64 * id.g + ch) * 16, 0);
    SB0();
    cm1(pa, pb, kr, ki, 0.f, 0.f);
#pragma unroll
    for (int s = 0; s < 7; ++s) cm1(er[s], ei[s], kr, ki, 0.f, 0.f);
    _Pragma("unroll 1") for (int spin = 0; spin < SPIN; ++spin) { FENCE();
      const bool ok = ((q >= id.R) | ((p0.y == tag) & (p0.w == tag))) & ((q + 4 >= id.R) | ((p1.y == tag) & (p1.w == tag)));
      if (__all(ok)) break;
      p0 = ld16c(rag, (q * 640 + 64 * id.g + ch) * 16, 0); p1 = ld16c(rag, ((q + 4) * 640 + 64 * id.g + ch) * 16, 0);
    }
    float ca = __uint_as_float(p1.x), cb = __uint_as_float(p1.z);
    cm1(ca, cb, w1r, w1i, 0.f, 0.f);
    { float x = __uint_as_float(p0.x), y = __uint_as_float(p0.z); cm1(x, y, w0r, w0i, ca, cb); ca = x; cb = y; }
    gather4(ca, ta); gather4(cb, tb);
    ca = (ta[0] + ta[1]) + (ta[2] + ta[3]); cb = (tb[0] + tb[1]) + (tb[2] + tb[3]);
    cm1(ca, cb, qr, qi, pa, pb);
#pragma unroll
    for (int s = 0; s < 8; ++s) {
      pe[s * (ESTR / 4)] = ca; pe[s * (ESTR / 4) + 64] = -cb;
      if (s < 7) cm1(ca, cb, l8r, l8i, er[s], ei[s]);
    }
    HBAR();
  }
}

__global__ __launch_bounds__(512, 2) void fused(Par p) {
  const int bid = blockIdx.x, xcd = bid & 7, idx = bid >> 3, a = xcd >> 2, b = xcd & 3, s = idx >> 2;
  Ids id;
  id.R = 4 * a + (idx & 3);
  const int ns = b < 2 ? 3 : 2, s0 = b < 2 ? 3 * b : 2 * b + 2, g0 = b < 2 ? 5 * b : 6 * b - 2;
  const bool ssm = s < ns;
  id.g = ssm ? s0 + s : g0 + s - ns;
  id.cg = ssm ? id.g : 10 + id.g;
  id.rt = 16 * a + 8 * (b >> 1) + (idx >> 2);
  id.ct = 4 * (b & 1) + (idx & 3);
  const int wv = rfl(threadIdx.x >> 6), lane = threadIdx.x & 63;
  unsigned cpf = 0;
  if (wv == 3) {
    const unsigned long long pc = __builtin_amdgcn_s_getpc() & ~127ull;
    cpf = __builtin_amdgcn_raw_buffer_load_b32(mkrs((const void*)pc, CODEPF), lane < 12 ? (idx * 12 + lane) * 128 : OOB, 0, 0);
  }
  unsigned tag;
  if (ssm) { if (wv < 4) tag = compute_wave<true>(p, id, wv, lane); else tag = helper_wave<true>(p, id, wv - 4, lane); }
  else { if (wv < 4) tag = compute_wave<false>(p, id, wv, lane); else tag = helper_wave<false>(p, id, wv - 4, lane); }

  const __amdgpu_buffer_rsrc_t rctl = mkrs(p.ws, WS_CTL_BYTES);
  if (wv < 4) {
    WAITV(0);
    unsigned old = 0;
    if (lane == 0) old = __hip_atomic_fetch_add((LAS unsigned*)LP(L_CNT), 1u, __ATOMIC_RELAXED, __HIP_MEMORY_SCOPE_WORKGROUP);
    st4c(tag, rctl, (lane == 0 && old == 3) ? WS_FLH + (id.R * 32 + id.cg) * 4 : OOB);
  }
  asm volatile("" ::"v"(cpf));
  STAMP(16);
  const __amdgpu_buffer_rsrc_t rw = mkrs(p.ws + WS_W2 + (unsigned)id.ct * (NKB * 5120), NKB * 5120);
  const __amdgpu_buffer_rsrc_t rh = mkrs(p.ws + WS_H + (unsigned)id.rt * (NKB * 4096), NKB * 4096);
  const unsigned vl = lane * 16;
  v4u wb[DW][5], hb[DH][4];
  auto loadw = [&](int it) {
#pragma unroll
    for (int i = 0; i < 5; ++i) { wb[it % DW][i] = ld16c(rw, vl + (i & 3) * 1024, (8 * it + wv) * 5120 + (i >> 2) * 4096); SB0(); }
  };
  auto loadh = [&](int it) {
#pragma unroll
    for (int j = 0; j < 4; ++j) { hb[it % DH][j] = ld16c(rh, vl + j * 1024, (8 * it + wv) * 4096); SB0(); }
  };
  SB0();
#pragma unroll
  for (int it = NL; it < NL + DW; ++it) {
    loadw(it); SB0();
  }
  const unsigned fo = WS_FLH + ((id.rt >> 2) * 32 + (lane & 31)) * 4;
  if (wv >= 4) {
    asm volatile("s_nop 4");
#pragma unroll
    for (int n = 0; n < 20; ++n) gldsc(rw, L_W + ((wv - 4) * 20 + n) * 1024, vl, ((wv - 4) * 20 + n) * 1024);
    if (ssm) carries(p, id, wv - 4, lane, tag);
    lwait<2>(L_CNT, 0, 4);
    if (wv > 4) {
#pragma unroll
      for (int n = 0; n < 14; ++n) gldsc(rw, min((wv - 5) * 14 + n, 39) * 1024, vl, (80 + min((wv - 5) * 14 + n, 39)) * 1024);
    } else {
      _Pragma("unroll 1") for (int spin = 0; spin < SPIN; ++spin) { FENCE();
        if (__all((ld4c(rctl, fo) == tag) | ((lane & 31) < 10))) break;
        __builtin_amdgcn_s_sleep(4);
      }
      sig(L_GO, 1);
    }
  }
  lwait<1>(L_GO, 0, 1);
  SB0();
  STAMP(20);
#pragma unroll
  for (int it = 0; it < DH; ++it) { loadh(it); SB0(); }
  f32x4 acc[4][5];
#pragma unroll
  for (int j = 0; j < 4; ++j)
#pragma unroll
    for (int i = 0; i < 5; ++i) acc[j][i] = f32x4{0.f, 0.f, 0.f, 0.f};
  const int tid = wv * 64 + lane;
  v2u xv[3], dv[3]; v4u sv[3]; int oo[3], ra[3];
  unsigned sfl = 0;
  const lp pw = LP(L_W + wv * 5120 + vl), pw2 = LP(wv * 5120 + vl);
  SB0();
  if (wv > 4) {
    WAITV(4 * DH);
    if (lane == 0) __hip_atomic_fetch_add((LAS unsigned*)LP(L_RDY), 1u, __ATOMIC_RELAXED, __HIP_MEMORY_SCOPE_WORKGROUP);
  }
  lwait<1>(L_RDY, 0, 3);
  v4u wl = l128(pw);
  SB0();
#pragma unroll
  for (int it = 0; it < NIT; ++it) {
    if (it < NL) {
#pragma unroll
      for (int i = 0; i < 5; ++i) {
        const v4u wf = wl;
        if (5 * it + i + 1 < 5 * NL) wl = l128((5 * it + i + 1) / 5 == 2 ? pw2 + ((5 * it + i + 1) % 5) * 1024 : pw + ((5 * it + i + 1) / 5) * 40960 + ((5 * it + i + 1) % 5) * 1024);
#pragma unroll
        for (int j = 0; j < 4; ++j) acc[j][i] = mfma16(wf, hb[it % DH][j], acc[j][i]);
        SB0();
      }
    } else {
#pragma unroll
      for (int j = 0; j < 4; ++j)
#pragma unroll
        for (int i = 0; i < 5; ++i) acc[j][i] = mfma16(wb[it % DW][i], hb[it % DH][j], acc[j][i]);
    }
    SB0();
    if (it + DH == 6) {
      if (__builtin_expect(!__all(sfl == tag), 0)) {
        _Pragma("unroll 1") for (int spin = 0; spin < SPIN; ++spin) { FENCE();
          if (__all(ld4c(rctl, fo) == tag)) break;
          __builtin_amdgcn_s_sleep(2);
        }
      }
      FENCE();
      STAMP(18); STAMPC(28);
    }
    if (it == 0) sfl = ld4c(rctl, fo);
    if (it + DH < NIT) loadh(it + DH);
    if (it >= NL && it + DW < NIT) loadw(it + DW);
    if (it == NIT - DH) {
#pragma unroll
      for (int r = 0; r < 3; ++r) {
        const int sl = min(tid + 512 * r, 1279), row = sl / 20, qd = sl - 20 * row;
        const int tok = 128 * (id.rt >> 1) + 8 * (row >> 2) + 4 * (id.rt & 1) + (row & 3), d = 80 * id.ct + 4 * qd;
        oo[r] = tok * D_ + d;
        ra[r] = ((row & 3) * 5 + (qd >> 2)) * 1024 + (qd & 3) * 256 + (((row >> 2) + qd) & 15) * 16;
        xv[r] = *(const v2u*)(p.x + oo[r]); dv[r] = *(const v2u*)(p.dsk + d);
        sv[r] = ld16c(rctl, WS_STATS + tok * 16, 0);
      }
    }
#define BARIT 2
    if (it == BARIT) BAR();
    SB0();
  }
  STAMP(21); STAMPC(29);
#pragma unroll
  for (int i = 0; i < 5; ++i) {
    const lp rb = LP(wv * 20480 + (lane >> 4) * 256 + (((lane & 15) + 4 * i + (lane >> 4)) & 15) * 16);
#pragma unroll
    for (int j = 0; j < 4; ++j) sf4(rb + (j * 5 + i) * 1024, acc[j][i]);
  }
  BAR();
  STAMP(19);
#pragma unroll
  for (int r = 0; r < 3; ++r) {
    if (r < 2 || wv < 4) {
      const lp q0 = LP(ra[r]), q1 = LP(ra[r] + 4 * 20480);
      f32x4 sum = ((lf4(q0) + lf4(q0 + 20480)) + (lf4(q0 + 40960) + lf4(q0 + 61440))) + ((lf4(q1) + lf4(q1 + 20480)) + (lf4(q1 + 40960) + lf4(q1 + 61440)));
      const f32x4 xf = {bflo(xv[r].x), bfhi(xv[r].x), bflo(xv[r].y), bfhi(xv[r].y)};
      const f32x4 df = {bflo(dv[r].x), bfhi(dv[r].x), bflo(dv[r].y), bfhi(dv[r].y)};
      sum += xf + (xf - __uint_as_float(sv[r].x)) * __uint_as_float(sv[r].z) * df;
      __builtin_amdgcn_raw_buffer_store_b128(__builtin_bit_cast(v4u, sum), mkrs(p.out, L_ * D_ * 4), oo[r] * 4, 0, OUTAUX);
    }
  }
  STAMP(22);
}

extern "C" void kernel_launch(void* const* d_in, const int* in_sizes, int n_in, void* d_out, int out_size, void* d_ws, size_t ws_size, hipStream_t stream) {
  Par p;
  p.x = (const u16*)d_in[0]; p.lre = (const u16*)d_in[1]; p.lim = (const u16*)d_in[2];
  p.bre = (const u16*)d_in[3]; p.bim = (const u16*)d_in[4]; p.cre = (const u16*)d_in[5]; p.cim = (const u16*)d_in[6];
  p.ldt = (const u16*)d_in[7]; p.dsk = (const u16*)d_in[8];
  p.wg = (const u16*)d_in[10]; p.wu = (const u16*)d_in[11]; p.wd = (const u16*)d_in[12];
  p.ws = (unsigned char*)d_ws; p.out = (float*)d_out;
  (void)hipFuncSetAttribute((const void*)fused, hipFuncAttributeMaxDynamicSharedMemorySize, LDS1);
  fused<<<256, 512, LDS1, stream>>>(p);
}
```

```cpp
#define ENC 1
#define ENCM 2u
#define CODEPF 47104
#define OUTAUX 16
#include <hip/hip_runtime.h>
#include <stdint.h>

typedef unsigned short u16;
typedef unsigned v4u __attribute__((ext_vector_type(4)));
typedef unsigned v2u __attribute__((ext_vector_type(2)));
typedef float f32x4 __attribute__((ext_vector_type(4)));
typedef float f32x2 __attribute__((ext_vector_type(2)));
typedef float f32x16 __attribute__((ext_vector_type(16)));
typedef short bf16x8 __attribute__((ext_vector_type(8)));
typedef short s16x4 __attribute__((ext_vector_type(4)));
typedef __bf16 bf2 __attribute__((ext_vector_type(2)));
#define LAS __attribute__((address_space(3)))
#define DI __device__ __forceinline__
typedef LAS unsigned char* lp;

constexpr int L_ = 2048, D_ = 640, P_ = 640, F_ = 1728;
constexpr int NKB = 94;
constexpr unsigned WS_EPOCH = 0;
constexpr unsigned WS_FLW = 2048;
constexpr unsigned WS_FLH = 3072;
constexpr unsigned WS_AGG = 4096;
constexpr unsigned WS_STATS = WS_AGG + 8 * 640 * 16;
constexpr unsigned WS_CTL_BYTES = WS_STATS + 32768;
constexpr unsigned WS_W2 = 1u << 20;
constexpr unsigned WS_W2_BYTES = 8 * NKB * 5120;
constexpr unsigned WS_H = 8u << 20;
constexpr unsigned WS_H_BYTES = 32 * NKB * 4096;

constexpr int UPO = 10368;
constexpr int STG = 32768 + 20480 + 128;
constexpr int LDS1 = 163840;
constexpr int L_STATS = 3 * STG;
constexpr int L_CS = L_STATS + 2048;
constexpr int L_CNT = L_CS + 1280, L_GO = L_CNT + 4, L_RDY = L_CNT + 8, L_CSF = L_CNT + 16;
constexpr int L_CST = 49152;
constexpr int L_W = STG;
constexpr int L_E = 136000, ESTR = 528;
constexpr int SPIN = 1 << 18;
constexpr unsigned OOB = 0x40000000u;

DI float bf2f(unsigned b) { return __uint_as_float(b << 16); }
DI float bflo(unsigned w) { return __uint_as_float(w << 16); }
DI float bfhi(unsigned w) { return __uint_as_float(w & 0xffff0000u); }
DI unsigned pk2(float a, float b) { f32x2 v = {a, b}; return __builtin_bit_cast(unsigned, __builtin_convertvector(v, bf2)); }
DI float rbf(float a) { return bflo(pk2(a, a) & 0xffffu); }
DI f32x4 mfma16(v4u a, v4u b, f32x4 c) { return __builtin_amdgcn_mfma_f32_16x16x32_bf16(__builtin_bit_cast(bf16x8, a), __builtin_bit_cast(bf16x8, b), c, 0, 0, 0); }
DI f32x16 mfma32(v4u a, v4u b, f32x16 c) { return __builtin_amdgcn_mfma_f32_32x32x16_bf16(__builtin_bit_cast(bf16x8, a), __builtin_bit_cast(bf16x8, b), c, 0, 0, 0); }
DI unsigned rfl(unsigned v) { return __builtin_amdgcn_readfirstlane(v); }
DI __amdgpu_buffer_rsrc_t mkrs(const void* p, unsigned bytes) { return __builtin_amdgcn_make_buffer_rsrc((void*)p, 0, bytes, 0x00020000); }
DI void glds(__amdgpu_buffer_rsrc_t rs, unsigned ldsaddr, unsigned voff, unsigned soff) {
  asm volatile("s_mov_b32 m0, %0\n\ts_nop 0\n\tbuffer_load_dwordx4 %1, %2, %3 offen lds" ::"s"(ldsaddr), "v"(voff), "s"(rs), "s"(soff));
}
DI void gldsc(__amdgpu_buffer_rsrc_t rs, unsigned ldsaddr, unsigned voff, unsigned soff) {
  asm volatile("s_mov_b32 m0, %0\n\ts_nop 0\n\tbuffer_load_dwordx4 %1, %2, %3 offen sc1 lds" ::"s"(ldsaddr), "v"(voff), "s"(rs), "s"(soff));
}
DI lp LP(unsigned a) { return (lp)(uintptr_t)a; }
DI v4u l128(lp a) { return *(LAS v4u*)a; }
DI f32x4 lf4(lp a) { return *(LAS f32x4*)a; }
DI void sf4(lp a, f32x4 v) { *(LAS f32x4*)a = v; }
DI s16x4 ltr(lp a) { return __builtin_amdgcn_ds_read_tr16_b64_v4i16((LAS s16x4*)a); }
DI v4u cat(s16x4 lo, s16x4 hi) { return __builtin_bit_cast(v4u, __builtin_shufflevector(lo, hi, 0, 1, 2, 3, 4, 5, 6, 7)); }
DI v4u ld16(__amdgpu_buffer_rsrc_t r, unsigned vo, unsigned so) { return __builtin_amdgcn_raw_buffer_load_b128(r, vo, so, 0); }
DI v4u ld16c(__amdgpu_buffer_rsrc_t r, unsigned vo, unsigned so) { return __builtin_amdgcn_raw_buffer_load_b128(r, vo, so, 16); }
DI unsigned ld4c(__amdgpu_buffer_rsrc_t r, unsigned vo) { return __builtin_amdgcn_raw_buffer_load_b32(r, vo, 0, 16); }
DI void st16c(v4u v, __amdgpu_buffer_rsrc_t r, unsigned vo) { __builtin_amdgcn_raw_buffer_store_b128(v, r, vo, 0, 16); }
DI void st4c(unsigned v, __amdgpu_buffer_rsrc_t r, unsigned vo) { __builtin_amdgcn_raw_buffer_store_b32(v, r, vo, 0, 16); }
#define BAR() asm volatile("s_waitcnt lgkmcnt(0)\n\ts_barrier" ::: "memory")
#define SB0() __builtin_amdgcn_sched_barrier(0)
#define WAITV(n) asm volatile("s_waitcnt vmcnt(%0)" ::"i"(n) : "memory")
#define FENCE() asm volatile("" ::: "memory")
template <int C> DI float dppf(float v) { return __int_as_float(__builtin_amdgcn_update_dpp(0, __float_as_int(v), C, 0xf, 0xf, true)); }
template <int N> DI f32x4 shr4(f32x4 v) { return f32x4{dppf<0x110 + N>(v.x), dppf<0x110 + N>(v.y), dppf<0x110 + N>(v.z), dppf<0x110 + N>(v.w)}; }
DI float sum8(float v) {
  v += dppf<0xB1>(v); v += dppf<0x4E>(v); v += dppf<0x141>(v);
  return v;
}
DI float dot2(unsigned a, unsigned b, float c) { return __builtin_amdgcn_fdot2_f32_bf16(__builtin_bit_cast(bf2, a), __builtin_bit_cast(bf2, b), c, false); }
DI void gather4(float x, float (&t)[4]) {
  const auto a = __builtin_amdgcn_permlane16_swap(__float_as_uint(x), __float_as_uint(x), false, false);
  const auto e = __builtin_amdgcn_permlane32_swap(a[0], a[0], false, false), o = __builtin_amdgcn_permlane32_swap(a[1], a[1], false, false);
  t[0] = __uint_as_float(e[0]); t[2] = __uint_as_float(e[1]); t[1] = __uint_as_float(o[0]); t[3] = __uint_as_float(o[1]);
}
DI void cm1(float& a, float& b, float lr, float li, float ur, float ui) {
  const float na = fmaf(lr, a, fmaf(-li, b, ur)), nb = fmaf(lr, b, fmaf(li, a, ui));
  a = na; b = nb;
}
DI f32x4 fma4(f32x4 a, f32x4 b, f32x4 c) { return __builtin_elementwise_fma(a, b, c); }
DI f32x4 sp4(float a) { return f32x4{a, a, a, a}; }
DI void cmac(f32x4& a, f32x4& b, f32x4 lr, f32x4 li, f32x4 ur, f32x4 ui) {
  const f32x4 na = fma4(lr, a, fma4(-li, b, ur)), nb = fma4(lr, b, fma4(li, a, ui));
  a = na; b = nb;
}

#define STAMP(i) do { if ((i) == 0) enc_a = (unsigned)__builtin_amdgcn_s_memrealtime(); if ((i) == ENC) enc_b = (unsigned)__builtin_amdgcn_s_memrealtime(); \
  if ((i) == 22) { const unsigned e_ = (unsigned)__builtin_amdgcn_s_memrealtime(); if (rfl(threadIdx.x) == 0) while ((unsigned)__builtin_amdgcn_s_memrealtime() - e_ < ENCM * (enc_b - enc_a)) __builtin_amdgcn_s_sleep(1); } } while (0)
#define STAMPC(i)

#define LSTAMP(i)
struct Par {
  const u16 *x, *lre, *lim, *bre, *bim, *cre, *cim, *ldt, *dsk, *wg, *wu, *wd;
  unsigned char* ws;
  float* out;
};
struct Ids { int R, g, cg, rt, ct; };

#define PDW 4
#define PDH 3
constexpr int DW = PDW, DH = PDH, NIT = 12, NL = 3;
#define PIN4(x) asm volatile("" : "+v"(x))
DI void sig(unsigned addr, unsigned v) {
  asm volatile("s_mov_b64 exec, 1\n\tds_write_b32 %0, %1\n\ts_mov_b64 exec, -1" ::"v"(addr), "v"(v) : "memory");
}
DI unsigned lrd(unsigned addr) { return *(volatile LAS unsigned*)LP(addr); }
#define LSLEEP 1
template <int SL = 1> DI void lwait(unsigned addr, unsigned v, unsigned need) {
  if (__builtin_expect(!__all(v >= need), 0)) {
    _Pragma("unroll 1") for (int spin = 0; spin < 64 * SPIN; ++spin) { FENCE();
      if (__all(lrd(addr) >= need)) break;
      __builtin_amdgcn_s_sleep(SL);
    }
  }
  FENCE();
}

DI float wsum(float v) {
  v = sum8(v); v += dppf<0x128>(v);
  float t[4]; gather4(v, t);
  return (t[0] + t[1]) + (t[2] + t[3]);
}
DI unsigned mktag(unsigned epoch) { return rfl(epoch) * 0x9E3779B1u + 0x7F4A7C15u; }
DI unsigned row_stats(const Par& p, const Ids& id, const int w, const int lane) {
  const __amdgpu_buffer_rsrc_t rctl = mkrs(p.ws, WS_CTL_BYTES);
  const int row = 256 * id.R + 8 * id.cg + 2 * w;
  v4u xa[2]; unsigned xb[2];
#pragma unroll
  for (int z = 0; z < 2; ++z) { xa[z] = *(const v4u*)(p.x + (row + z) * D_ + lane * 8); xb[z] = *(const unsigned*)(p.x + (row + z) * D_ + 512 + lane * 2); }
  const unsigned epoch = ld4c(rctl, WS_EPOCH);
  float v[2][10], mean[2], s2[2];
#pragma unroll
  for (int z = 0; z < 2; ++z) {
    const float t[10] = {bflo(xa[z].x), bfhi(xa[z].x), bflo(xa[z].y), bfhi(xa[z].y), bflo(xa[z].z), bfhi(xa[z].z), bflo(xa[z].w), bfhi(xa[z].w), bflo(xb[z]), bfhi(xb[z])};
#pragma unroll
    for (int e = 0; e < 10; ++e) v[z][e] = t[e];
    mean[z] = wsum(((t[0] + t[1]) + (t[2] + t[3])) + ((t[4] + t[5]) + (t[6] + t[7])) + (t[8] + t[9])) * (1.f / D_);
  }
#pragma unroll
  for (int z = 0; z < 2; ++z) {
    float a = 0.f, b = 0.f;
#pragma unroll
    for (int e = 0; e < 5; ++e) { a = fmaf(v[z][e] - mean[z], v[z][e] - mean[z], a); b = fmaf(v[z][5 + e] - mean[z], v[z][5 + e] - mean[z], b); }
    s2[z] = wsum(a + b);
  }
  const unsigned tag = mktag(epoch);
  const float m = rbf((lane & 1) ? mean[1] : mean[0]), rs = 1.f / rbf(sqrtf(rbf(rbf(((lane & 1) ? s2[1] : s2[0]) * (1.f / D_)) + 1e-5f)));
  st16c(v4u{__float_as_uint(m), tag, __float_as_uint(rs), tag}, rctl, lane < 2 ? WS_STATS + (row + lane) * 16 : OOB);
  return tag;
}

template <bool SSM>
DI unsigned compute_wave(const Par& p, const Ids& id, const int w, const int lane, unsigned& enc_a, unsigned& enc_b) {
  STAMP(0);
  unsigned tag, cf = 0;
  if constexpr (!SSM) {
    const int r = lane & 31, hh = lane >> 5;
    const unsigned xa = (64 * w + r) * 128 + ((hh ^ ((r >> 1) & 7)) * 16);
    const unsigned wa = 32768 + ((lane >> 4) & 1) * UPO + (8 * hh + ((lane & 15) >> 2)) * 160 + (lane & 3) * 8;
    f32x16 acc[5][2] = {};
#pragma unroll
    for (int e = 0; e < 5; ++e) { PIN4(acc[e][0]); PIN4(acc[e][1]); }
    v4u fx[2][2]; s16x4 fl[4], fh[4];
    lp px[4], pw;
    auto setp = [&](unsigned sb) {
#pragma unroll
      for (int s = 0; s < 4; ++s) px[s] = LP(sb + (xa ^ (32 * s)));
      pw = LP(sb + wa);
    };
    auto woff = [&](int n) { return (n / 5) * 2560 + (n % 5) * 32; };
    setp(0);
    SB0();
    tag = row_stats(p, id, w, lane);
    STAMP(17);
    SB0();
    BAR();
    STAMP(1);
    fx[0][0] = l128(px[0]); fx[0][1] = l128(px[0] + 4096);
#pragma unroll
    for (int n = 0; n < 3; ++n) { fl[n] = ltr(pw + woff(n)); fh[n] = ltr(pw + woff(n) + 640); }
    unsigned sb = 0;
#pragma unroll 1
    for (int i = 0; i < 10; ++i) {
#pragma unroll
      for (int n = 0; n < 20; ++n) {
        const int ks = n / 5, e = n % 5, nn = (n + 3) % 20;
        if (n == 17) { if (i < 9) BAR(); LSTAMP(i); sb = sb == 2 * STG ? 0 : sb + STG; setp(sb); SB0(); }
        const v4u wf = cat(fl[n & 3], fh[n & 3]);
        acc[e][0] = mfma32(wf, fx[ks & 1][0], acc[e][0]); SB0();
        fl[(n + 3) & 3] = ltr(pw + woff(nn));
        fh[(n + 3) & 3] = ltr(pw + woff(nn) + 640); SB0();
        acc[e][1] = mfma32(wf, fx[ks & 1][1], acc[e][1]); SB0();
        if (ks < 3 && e == 1) { fx[(ks + 1) & 1][0] = l128(px[ks + 1]); fx[(ks + 1) & 1][1] = l128(px[ks + 1] + 4096); }
        if (n == 17) { fx[0][0] = l128(px[0]); fx[0][1] = l128(px[0] + 4096); }
        if (n == 18) cf = lrd(L_CSF + (lane & 3) * 4);
        SB0();
      }
    }
    lwait(L_CSF + (lane & 3) * 4, cf, 1);
    STAMP(12);
    const __amdgpu_buffer_rsrc_t rh = mkrs(p.ws + WS_H + (unsigned)(4 * id.R + w) * (NKB * 4096), 54 * 4096);
    const unsigned so0 = (r >> 4) * 1024 + (hh * 16 + (r & 15)) * 16;
    float mu[2], r2[2];
#pragma unroll
    for (int u = 0; u < 2; ++u) {
      const int F = 4 * w + 2 * u + (r >> 4);
      const f32x2 s = *(LAS f32x2*)LP(L_STATS + (128 * (F >> 3) + 8 * (r & 15) + (F & 7)) * 8);
      mu[u] = s.x; r2[u] = s.y * s.y;
    }
    const lp pc = LP(L_CS + hh * 16);
#pragma unroll
    for (int e = 0; e < 5; ++e) {
      const f32x4 g0 = lf4(pc + e * 64) + lf4(pc + e * 64 + 640), g1 = lf4(pc + e * 64 + 32) + lf4(pc + e * 64 + 672);
      const f32x4 u0 = lf4(pc + e * 64 + 320) + lf4(pc + e * 64 + 960), u1 = lf4(pc + e * 64 + 352) + lf4(pc + e * 64 + 992);
      const int T = 5 * id.g + e;
      const unsigned so = so0 + (T >> 1) * 4096 + (T & 1) * 512;
#pragma unroll
      for (int u = 0; u < 2; ++u) {
        const f32x16 a = acc[e][u];
        const f32x4 nm = sp4(-mu[u]);
        const f32x4 h0 = fma4(nm, g0, f32x4{a[0], a[1], a[2], a[3]}) * fma4(nm, u0, f32x4{a[8], a[9], a[10], a[11]}) * r2[u];
        const f32x4 h1 = fma4(nm, g1, f32x4{a[4], a[5], a[6], a[7]}) * fma4(nm, u1, f32x4{a[12], a[13], a[14], a[15]}) * r2[u];
        st16c(v4u{pk2(h0.x, h0.y), pk2(h0.z, h0.w), pk2(h1.x, h1.y), pk2(h1.z, h1.w)}, rh, so + u * 2048);
      }
    }
  } else {
    const int c = lane & 15, q = lane >> 4, G = w >> 1, blk = w & 1;
    const unsigned xa = (128 * G + c) * 128 + ((q ^ (c >> 1)) * 16);
    const unsigned wa = 32768 + (32 * blk + c) * 128 + ((q ^ (c >> 1)) * 16);
    f32x4 acc[8][4] = {};
#pragma unroll
    for (int j = 0; j < 8; ++j)
#pragma unroll
      for (int t = 0; t < 4; ++t) PIN4(acc[j][t]);
    v4u fw[2][4], fx[4];
    lp px[2], pw[2];
    auto setp = [&](unsigned sb) { px[0] = LP(sb + xa); px[1] = LP(sb + (xa ^ 64)); pw[0] = LP(sb + wa); pw[1] = LP(sb + (wa ^ 64)); };
    auto woff = [&](int t) { return (t >> 1) * 8192 + (t & 1) * 2048; };
    setp(0);
    SB0();
    tag = row_stats(p, id, w, lane);
    STAMP(17);
    SB0();
    BAR();
    STAMP(1);
#pragma unroll
    for (int t = 0; t < 4; ++t) fw[0][t] = l128(pw[0] + woff(t));
#pragma unroll
    for (int n = 0; n < 3; ++n) fx[n] = l128(px[0] + n * 2048);
    unsigned sb = 0;
#pragma unroll 1
    for (int i = 0; i < 10; ++i) {
#pragma unroll
      for (int n = 0; n < 16; ++n) {
        const int kk = n >> 3, j = n & 7, nn = (n + 3) & 15;
        if (n == 13) { if (i < 9) BAR(); LSTAMP(i); sb = sb == 2 * STG ? 0 : sb + STG; setp(sb); SB0(); }
        const v4u xf = fx[n & 3];
        acc[j][0] = mfma16(fw[kk][0], xf, acc[j][0]); SB0();
        fx[(n + 3) & 3] = l128(px[nn >> 3] + (nn & 7) * 2048); SB0();
        acc[j][1] = mfma16(fw[kk][1], xf, acc[j][1]); SB0();
        if (n >= 2 && n < 6) fw[1][n - 2] = l128(pw[1] + woff(n - 2));
        if (n == 13) fw[0][0] = l128(pw[0] + woff(0));
        if (n == 14) fw[0][2] = l128(pw[0] + woff(2));
        SB0();
        acc[j][2] = mfma16(fw[kk][2], xf, acc[j][2]); SB0();
        if (n == 13) fw[0][1] = l128(pw[0] + woff(1));
        if (n == 14) fw[0][3] = l128(pw[0] + woff(3));
        SB0();
        acc[j][3] = mfma16(fw[kk][3], xf, acc[j][3]); SB0();
        if (n == 14) { cf = lrd(L_CSF + (lane & 3) * 4); SB0(); }
      }
    }
    lwait(L_CSF + (lane & 3) * 4, cf, 1);
    STAMP(12);
    const lp pe = LP(L_E + (16 * G + c) * ESTR + (32 * blk + 8 * q) * 4);
    const lp pk = LP(L_CST + (32 * blk + 8 * q) * 4);
    f32x4 lr[2], li[2], er[2], ei[2];
    {
      const lp ps = LP(L_STATS + (128 * G + 8 * c) * 8), pc = LP(L_CS + (32 * blk + 8 * q) * 4);
      f32x4 cs[4], st[4];
#pragma unroll
      for (int t = 0; t < 4; ++t) { cs[t] = lf4(pc + (t >> 1) * 320 + (t & 1) * 16) + lf4(pc + 640 + (t >> 1) * 320 + (t & 1) * 16); st[t] = lf4(ps + t * 16); }
#pragma unroll
      for (int ab = 0; ab < 2; ++ab) { lr[ab] = lf4(pk + ab * 16); li[ab] = lf4(pk + 256 + ab * 16); }
#pragma unroll
      for (int jj = 0; jj < 4; ++jj)
#pragma unroll
        for (int t = 0; t < 4; ++t) {
          acc[2 * jj][t] = fma4(sp4(-st[jj].x), cs[t], acc[2 * jj][t]) * st[jj].y;
          acc[2 * jj + 1][t] = fma4(sp4(-st[jj].z), cs[t], acc[2 * jj + 1][t]) * st[jj].w;
        }
    }
#pragma unroll
    for (int ab = 0; ab < 2; ++ab) { er[ab] = acc[0][ab]; ei[ab] = acc[0][2 + ab]; }
#pragma unroll
    for (int j = 1; j < 8; ++j)
#pragma unroll
      for (int ab = 0; ab < 2; ++ab) cmac(er[ab], ei[ab], lr[ab], li[ab], acc[j][ab], acc[j][2 + ab]);
#pragma unroll
    for (int ab = 0; ab < 2; ++ab) { sf4(pe + ab * 16, er[ab]); sf4(pe + 256 + ab * 16, ei[ab]); }
    SB0();
    BAR();
    STAMP(13);
#pragma unroll
    for (int ab = 0; ab < 2; ++ab) {
      const f32x4 kr = lf4(pk + 512 * 4 + ab * 16), ki = lf4(pk + 512 * 4 + 256 + ab * 16);
#pragma unroll
      for (int j = 0; j < 8; ++j) {
        const f32x4 ur = acc[j][ab], ui = acc[j][2 + ab];
        acc[j][ab] = fma4(-ki, ui, kr * ur); acc[j][2 + ab] = fma4(-ki, ur, -kr * ui);
        PIN4(acc[j][ab]); PIN4(acc[j][2 + ab]);
      }
      li[ab] = -li[ab];
    }
    const __amdgpu_buffer_rsrc_t rh = mkrs(p.ws + WS_H + (unsigned)(4 * id.R + 2 * G) * (NKB * 4096), 2 * NKB * 4096);
    unsigned vo[4];
#pragma unroll
    for (int z = 0; z < 4; ++z) { vo[z] = (54 + 2 * id.g + blk + 20 * (z & 1) + NKB * (z >> 1)) * 4096 + lane * 16; PIN4(vo[z]); }
    SB0();
    BAR();
    STAMP(14);
#pragma unroll
    for (int ab = 0; ab < 2; ++ab) { er[ab] = lf4(pe + ab * 16); ei[ab] = lf4(pe + 256 + ab * 16); }
#pragma unroll
    for (int j = 0; j < 8; ++j) {
#pragma unroll
      for (int ab = 0; ab < 2; ++ab) cmac(er[ab], ei[ab], lr[ab], li[ab], acc[j][ab], acc[j][2 + ab]);
      st16c(v4u{pk2(er[0].x, er[0].y), pk2(er[0].z, er[0].w), pk2(er[1].x, er[1].y), pk2(er[1].z, er[1].w)}, rh, vo[2 * (j >> 2)] + (j & 3) * 1024);
      st16c(v4u{pk2(ei[0].x, ei[0].y), pk2(ei[0].z, ei[0].w), pk2(ei[1].x, ei[1].y), pk2(ei[1].z, ei[1].w)}, rh, vo[2 * (j >> 2) + 1] + (j & 3) * 1024);
    }
  }
  SB0();
  STAMP(15);
  return tag;
}

#define HBAR() do { SB0(); BAR(); SB0(); } while (0)
template <bool SSM>
DI unsigned helper_wave(const Par& p, const Ids& id, const int h, const int lane, unsigned& enc_a, unsigned& enc_b) {
  STAMP(0);
  constexpr int NWP = SSM ? 4 : 5, NP = 8 + NWP;
  constexpr unsigned WSTEP = SSM ? 128u : 64u * 3456u;
  const int c = lane & 15, q = lane >> 4;
  const __amdgpu_buffer_rsrc_t rctl = mkrs(p.ws, WS_CTL_BYTES);
  const __amdgpu_buffer_rsrc_t rx = mkrs(p.x, L_ * D_ * 2);
  const __amdgpu_buffer_rsrc_t rw = mkrs(SSM ? ((h >> 1) ? p.bim : p.bre) : ((h >> 1) ? p.wu : p.wg), SSM ? P_ * D_ * 2 : D_ * F_ * 2);
  const unsigned v0 = (8 * (lane >> 3)) * 1280 + (((lane & 7) ^ (lane >> 4)) * 16), v1 = v0 ^ 64;
  unsigned sx = (256 * id.R + 128 * (h >> 1) + 4 * (h & 1)) * 1280;
  auto issue_x = [&](unsigned s, unsigned sb) {
#pragma unroll
    for (int n = 0; n < 8; ++n) glds(rx, sb + h * 8192 + n * 1024, (n & 1) ? v1 : v0, s + (64 * (n & 1) + (n >> 1)) * 1280);
  };
  asm volatile("s_nop 4");
  issue_x(sx, 0);
  unsigned vw[NWP];
  if constexpr (SSM) {
    vw[0] = (8 * (lane >> 5) + ((lane >> 3) & 3)) * 1280 + (((lane & 7) ^ (lane >> 4)) * 16); vw[1] = vw[0] ^ 64;
  } else {
#pragma unroll
    for (int i = 0; i < 5; ++i) { const int lc = 64 * (5 * (h & 1) + i) + lane, pos = lc / 10; vw[i] = pos * 3456 + (lc - pos * 10) * 16; }
  }
  unsigned sw = SSM ? (64 * id.g + 32 * (h & 1)) * 1280 : 160 * id.g;
  const unsigned dw = 32768 + (h >> 1) * (SSM ? 8192 : UPO) + (h & 1) * NWP * 1024;
  auto issue_w = [&](unsigned s, unsigned sb) {
#pragma unroll
    for (int n = 0; n < NWP; ++n) {
      if constexpr (SSM) glds(rw, sb + dw + n * 1024, vw[n & 1], s + (16 * (n & 1) + 4 * (n >> 1)) * 1280);
      else glds(rw, sb + dw + n * 1024, vw[n], s);
    }
  };
  issue_w(sw, 0);
  sx += 128; sw += WSTEP;
  unsigned sb = STG;
  auto step = [&]() {
    issue_x(sx, sb); issue_w(sw, sb);
    sx += 128; sw += WSTEP; sb = sb == 2 * STG ? 0 : sb + STG;
  };
  step();
  if (h == 3 && lane < 8) *(LAS unsigned*)LP(L_CNT + lane * 4) = 0u;
  f32x4 acs[NWP] = {};
  v4u fc[NWP];
  const unsigned ca = SSM ? 32768 + (h >> 1) * 8192 + c * 128 + (((4 * (h & 1) + q) ^ (c >> 1)) * 16)
                          : 32768 + (h >> 1) * UPO + (32 * (h & 1) + 4 * q + (c >> 2)) * 160 + (c & 3) * 8;
  const v4u ones = {0x3F803F80u, 0x3F803F80u, 0x3F803F80u, 0x3F803F80u};
  auto cs_rd = [&](unsigned s) {
    const lp a = LP(s + ca);
#pragma unroll
    for (int t = 0; t < NWP; ++t) { if constexpr (SSM) fc[t] = l128(a + t * 2048); else fc[t] = cat(ltr(a + t * 32), ltr(a + t * 32 + 2560)); }
    SB0();
  };
  auto cs_mm = [&]() {
    SB0();
    __builtin_amdgcn_s_setprio(2);
#pragma unroll
    for (int t = 0; t < NWP; ++t) acs[t] = mfma16(fc[t], ones, acs[t]);
    __builtin_amdgcn_s_setprio(0);
    SB0();
  };
  WAITV(NP); BAR();
  cs_rd(0);
  step();
  const int n = blockIdx.x * 4 + h, kb = n / 10, dq = n - 10 * kb;
  const bool wd = kb < 54;
  v4u f[4];
  unsigned epoch, pl = 0, pr = 0, pi = 0;
  {
    const __amdgpu_buffer_rsrc_t r = mkrs(wd ? p.wd : kb < 74 ? p.cre : p.cim, kb < NKB ? (wd ? F_ * D_ * 2 : D_ * P_ * 2) : 0);
    const unsigned vo = wd ? (32 * kb + 16 * (c >> 3) + 4 * ((c >> 2) & 1) + (c & 3)) * 1280 + dq * 128 + q * 16
                           : (64 * dq + c) * 1280 + (kb < 74 ? kb - 54 : kb - 74) * 64 + q * 16;
#pragma unroll
    for (int i = 0; i < 4; ++i) f[i] = ld16(r, vo, wd ? (i >> 1) * 64 + (i & 1) * (8 * 1280) : i * (16 * 1280));
    epoch = ld4c(rctl, WS_EPOCH);
    if constexpr (SSM) { pl = p.ldt[64 * id.g + lane]; pr = p.lre[64 * id.g + lane]; pi = p.lim[64 * id.g + lane]; }
  }
  cs_mm();
  WAITV(NP + (SSM ? 8 : 5)); BAR();
  cs_rd(STG); step(); cs_mm();
  WAITV(NP); BAR();
  cs_rd(2 * STG);
  asm volatile("" : "+v"(f[0]), "+v"(f[1]), "+v"(f[2]), "+v"(f[3]), "+v"(epoch), "+v"(pl), "+v"(pr), "+v"(pi));
  step();
  const unsigned tag = mktag(epoch);
  if (wd) {
    v4u sel[2], o[4];
#pragma unroll
    for (int hf = 0; hf < 2; ++hf)
#pragma unroll
      for (int e = 0; e < 4; ++e) sel[hf][e] = (8 * q + 2 * e == 16 * hf + c ? 0x3F80u : 0u) | (8 * q + 2 * e + 1 == 16 * hf + c ? 0x3F800000u : 0u);
    __builtin_amdgcn_s_setprio(2);
#pragma unroll
    for (int i = 0; i < 4; ++i) {
      const f32x4 a = mfma16(f[i & ~1], sel[i & 1], f32x4{0.f, 0.f, 0.f, 0.f}), b = mfma16(f[i | 1], sel[i & 1], f32x4{0.f, 0.f, 0.f, 0.f});
      o[i] = v4u{pk2(a.x, a.y), pk2(a.z, a.w), pk2(b.x, b.y), pk2(b.z, b.w)};
    }
#pragma unroll
    for (int i = 0; i < 4; ++i) f[i] = o[i];
  }
  cs_mm();
  WAITV(NP); BAR();
  cs_rd(0);
  {
    const __amdgpu_buffer_rsrc_t r2 = mkrs(p.ws + WS_W2, WS_W2_BYTES);
#pragma unroll
    for (int i = 0; i < 4; ++i) {
      const int dt = 4 * dq + i, ct = dt / 5;
      st16c(f[i], r2, kb < NKB ? ((ct * NKB + kb) * 5 + dt - 5 * ct) * 1024 + lane * 16 : OOB);
    }
    SB0();
  }
  step(); cs_mm();
  WAITV(NP + 4); BAR();
  cs_rd(STG); step(); cs_mm();
  WAITV(NP); BAR();
  cs_rd(2 * STG);
  st4c(tag, rctl, (h == 3 && lane == 0) ? WS_FLW + blockIdx.x * 4 : OOB);
  SB0();
  step(); cs_mm();
  WAITV(NP + 1); BAR();
  cs_rd(0);
  v4u sv;
  asm volatile("buffer_load_dwordx4 %0, %1, %2, 0 offen sc1" : "=v"(sv) : "v"(WS_STATS + (256 * id.R + 64 * h + lane) * 16), "s"(rctl));
  step(); cs_mm();
  asm volatile("s_waitcnt vmcnt(%1)" : "+v"(sv) : "i"(NP) : "memory");
  if (!__all((sv.y == tag) & (sv.w == tag))) {
    _Pragma("unroll 1") for (int spin = 0; spin < SPIN; ++spin) { FENCE();
      sv = ld16c(rctl, WS_STATS + (256 * id.R + 64 * h + lane) * 16, 0);
      if (__all((sv.y == tag) & (sv.w == tag))) break;
      __builtin_amdgcn_s_sleep(4);
    }
  }
  *(LAS v2u*)LP(L_STATS + (64 * h + lane) * 8) = v2u{sv.x, sv.z};
  BAR();
  cs_rd(STG); step(); cs_mm();
  WAITV(NP); BAR();
  cs_rd(2 * STG);
  if (h == 3) {
    _Pragma("unroll 1") for (int spin = 0; spin < SPIN; ++spin) { FENCE();
      const v4u f2 = ld16c(rctl, WS_FLW + lane * 16, 0);
      if (__all((f2.x == tag) & (f2.y == tag) & (f2.z == tag) & (f2.w == tag))) break;
      __builtin_amdgcn_s_sleep(4);
    }
    st4c(tag, rctl, (lane == 0 && blockIdx.x == 255) ? WS_EPOCH : OOB);
  }
  if (SSM && h == 2) {
    const float dt = rbf(expf(bf2f(pl))), lr = bf2f(pr), li = bf2f(pi);
    const float ex = expf(dt * lr);
    float sn, cn;
    sincosf(dt * li, &sn, &cn);
    float br = ex * cn, bi = ex * sn;
    const float nr = br - 1.f, ni = bi, den = lr * lr + li * li;
    LAS float* cst = (LAS float*)LP(L_CST);
    cst[4 * 128 + lane] = 2.f * (nr * lr + ni * li) / den; cst[4 * 128 + 64 + lane] = 2.f * (ni * lr - nr * li) / den;
    cst[lane] = br; cst[64 + lane] = bi;
#pragma unroll
    for (int s = 1; s <= 8; ++s) {
      const float a = br * br - bi * bi, b = 2.f * br * bi;
      br = a; bi = b;
      const int k = s == 3 ? 1 : s == 6 ? 2 : s == 8 ? 3 : 0;
      if (k) { cst[k * 128 + lane] = br; cst[k * 128 + 64 + lane] = bi; }
    }
  }
  cs_mm();
  WAITV(0); BAR();
  cs_rd(0); cs_mm();
  if (c == 0) {
#pragma unroll
    for (int t = 0; t < NWP; ++t) {
      const int col = SSM ? 32 * (t >> 1) + 8 * q + 4 * (t & 1) : 16 * t + 4 * q;
      sf4(LP(L_CS + ((h & 1) * 160 + (h >> 1) * 80 + col) * 4), acs[t]);
    }
  }
  sig(L_CSF + h * 4, 1);
  return tag;
}

DI void carries(const Par& p, const Ids& id, const int h, const int lane, const unsigned tag) {
  const __amdgpu_buffer_rsrc_t rctl = mkrs(p.ws, WS_CTL_BYTES);
  const int c = lane & 15, q = lane >> 4;
  {
    const int ch = 16 * h + c;
    LAS float* cst = (LAS float*)LP(L_CST);
    const float l8r = cst[128 + ch], l8i = cst[192 + ch], l64r = cst[256 + ch], l64i = cst[320 + ch], lwr = cst[384 + ch], lwi = cst[448 + ch];
    const float kr = cst[512 + ch], ki = cst[576 + ch];
    float w0r = 1.f, w0i = 0.f, w1r = 1.f, w1i = 0.f, qr = kr, qi = ki;
#pragma unroll
    for (int s = 0; s < 6; ++s) {
      float a = w0r, b = w0i; cm1(a, b, lwr, lwi, 0.f, 0.f);
      if (s < id.R - 1 - q) { w0r = a; w0i = b; }
      if (s < id.R - 5 - q) { w1r = a; w1i = b; }
    }
#pragma unroll
    for (int s = 0; s < 3; ++s) { float a = qr, b = qi; cm1(a, b, l64r, l64i, 0.f, 0.f); if (s < q) { qr = a; qi = b; } }
    const __amdgpu_buffer_rsrc_t rag = mkrs(p.ws + WS_AGG, id.R * 640 * 16);
    LAS float* pe = (LAS float*)LP(L_E + 8 * q * ESTR + ch * 4);
    HBAR();
    float er[8], ei[8];
#pragma unroll
    for (int s = 0; s < 8; ++s) { er[s] = pe[s * (ESTR / 4)]; ei[s] = pe[s * (ESTR / 4) + 64]; }
    float a = er[0], b = ei[0];
#pragma unroll
    for (int s = 1; s < 8; ++s) cm1(a, b, l8r, l8i, er[s], ei[s]);
    float ta[4], tb[4];
    gather4(a, ta); gather4(b, tb);
    float pa = 0.f, pb = 0.f;
    a = ta[0]; b = tb[0];
#pragma unroll
    for (int s = 1; s < 4; ++s) {
      if (q == s) { pa = a; pb = b; }
      cm1(a, b, l64r, l64i, ta[s], tb[s]);
    }
    st16c(v4u{__float_as_uint(a), tag, __float_as_uint(b), tag}, rctl, (q == 0 && id.R < 7) ? WS_AGG + (id.R * 640 + 64 * id.g + ch) * 16 : OOB);
    v4u p0 = ld16c(rag, (q * 640 + 64 * id.g + ch) * 16, 0), p1 = ld16c(rag, ((q + 4) * 640 + 64 * id.g + ch) * 16, 0);
    SB0();
    cm1(pa, pb, kr, ki, 0.f, 0.f);
#pragma unroll
    for (int s = 0; s < 7; ++s) cm1(er[s], ei[s], kr, ki, 0.f, 0.f);
    _Pragma("unroll 1") for (int spin = 0; spin < SPIN; ++spin) { FENCE();
      const bool ok = ((q >= id.R) | ((p0.y == tag) & (p0.w == tag))) & ((q + 4 >= id.R) | ((p1.y == tag) & (p1.w == tag)));
      if (__all(ok)) break;
      p0 = ld16c(rag, (q * 640 + 64 * id.g + ch) * 16, 0); p1 = ld16c(rag, ((q + 4) * 640 + 64 * id.g + ch) * 16, 0);
    }
    float ca = __uint_as_float(p1.x), cb = __uint_as_float(p1.z);
    cm1(ca, cb, w1r, w1i, 0.f, 0.f);
    { float x = __uint_as_float(p0.x), y = __uint_as_float(p0.z); cm1(x, y, w0r, w0i, ca, cb); ca = x; cb = y; }
    gather4(ca, ta); gather4(cb, tb);
    ca = (ta[0] + ta[1]) + (ta[2] + ta[3]); cb = (tb[0] + tb[1]) + (tb[2] + tb[3]);
    cm1(ca, cb, qr, qi, pa, pb);
#pragma unroll
    for (int s = 0; s < 8; ++s) {
      pe[s * (ESTR / 4)] = ca; pe[s * (ESTR / 4) + 64] = -cb;
      if (s < 7) cm1(ca, cb, l8r, l8i, er[s], ei[s]);
    }
    HBAR();
  }
}

__global__ __launch_bounds__(512, 2) void fused(Par p) {
  const int bid = blockIdx.x, xcd = bid & 7, idx = bid >> 3, a = xcd >> 2, b = xcd & 3, s = idx >> 2;
  Ids id;
  id.R = 4 * a + (idx & 3);
  const int ns = b < 2 ? 3 : 2, s0 = b < 2 ? 3 * b : 2 * b + 2, g0 = b < 2 ? 5 * b : 6 * b - 2;
  const bool ssm = s < ns;
  id.g = ssm ? s0 + s : g0 + s - ns;
  id.cg = ssm ? id.g : 10 + id.g;
  id.rt = 16 * a + 8 * (b >> 1) + (idx >> 2);
  id.ct = 4 * (b & 1) + (idx & 3);
  const int wv = rfl(threadIdx.x >> 6), lane = threadIdx.x & 63;
  unsigned enc_a = 0, enc_b = 0;
  unsigned cpf = 0;
  if (wv == 3) {
    const unsigned long long pc = __builtin_amdgcn_s_getpc() & ~127ull;
    cpf = __builtin_amdgcn_raw_buffer_load_b32(mkrs((const void*)pc, CODEPF), lane < 12 ? (idx * 12 + lane) * 128 : OOB, 0, 0);
  }
  unsigned tag;
  if (ssm) { if (wv < 4) tag = compute_wave<true>(p, id, wv, lane, enc_a, enc_b); else tag = helper_wave<true>(p, id, wv - 4, lane, enc_a, enc_b); }
  else { if (wv < 4) tag = compute_wave<false>(p, id, wv, lane, enc_a, enc_b); else tag = helper_wave<false>(p, id, wv - 4, lane, enc_a, enc_b); }

  const __amdgpu_buffer_rsrc_t rctl = mkrs(p.ws, WS_CTL_BYTES);
  if (wv < 4) {
    WAITV(0);
    unsigned old = 0;
    if (lane == 0) old = __hip_atomic_fetch_add((LAS unsigned*)LP(L_CNT), 1u, __ATOMIC_RELAXED, __HIP_MEMORY_SCOPE_WORKGROUP);
    st4c(tag, rctl, (lane == 0 && old == 3) ? WS_FLH + (id.R * 32 + id.cg) * 4 : OOB);
  }
  asm volatile("" ::"v"(cpf));
  STAMP(16);
  const __amdgpu_buffer_rsrc_t rw = mkrs(p.ws + WS_W2 + (unsigned)id.ct * (NKB * 5120), NKB * 5120);
  const __amdgpu_buffer_rsrc_t rh = mkrs(p.ws + WS_H + (unsigned)id.rt * (NKB * 4096), NKB * 4096);
  const unsigned vl = lane * 16;
  v4u wb[DW][5], hb[DH][4];
  auto loadw = [&](int it) {
#pragma unroll
    for (int i = 0; i < 5; ++i) { wb[it % DW][i] = ld16c(rw, vl + (i & 3) * 1024, (8 * it + wv) * 5120 + (i >> 2) * 4096); SB0(); }
  };
  auto loadh = [&](int it) {
#pragma unroll
    for (int j = 0; j < 4; ++j) { hb[it % DH][j] = ld16c(rh, vl + j * 1024, (8 * it + wv) * 4096); SB0(); }
  };
  SB0();
#pragma unroll
  for (int it = NL; it < NL + DW; ++it) {
    loadw(it); SB0();
  }
  const unsigned fo = WS_FLH + ((id.rt >> 2) * 32 + (lane & 31)) * 4;
  if (wv >= 4) {
    asm volatile("s_nop 4");
#pragma unroll
    for (int n = 0; n < 20; ++n) gldsc(rw, L_W + ((wv - 4) * 20 + n) * 1024, vl, ((wv - 4) * 20 + n) * 1024);
    if (ssm) carries(p, id, wv - 4, lane, tag);
    lwait<2>(L_CNT, 0, 4);
    if (wv > 4) {
#pragma unroll
      for (int n = 0; n < 14; ++n) gldsc(rw, min((wv - 5) * 14 + n, 39) * 1024, vl, (80 + min((wv - 5) * 14 + n, 39)) * 1024);
    } else {
      _Pragma("unroll 1") for (int spin = 0; spin < SPIN; ++spin) { FENCE();
        if (__all((ld4c(rctl, fo) == tag) | ((lane & 31) < 10))) break;
        __builtin_amdgcn_s_sleep(4);
      }
      sig(L_GO, 1);
    }
  }
  lwait<1>(L_GO, 0, 1);
  SB0();
  STAMP(20);
#pragma unroll
  for (int it = 0; it < DH; ++it) { loadh(it); SB0(); }
  f32x4 acc[4][5];
#pragma unroll
  for (int j = 0; j < 4; ++j)
#pragma unroll
    for (int i = 0; i < 5; ++i) acc[j][i] = f32x4{0.f, 0.f, 0.f, 0.f};
  const int tid = wv * 64 + lane;
  v2u xv[3], dv[3]; v4u sv[3]; int oo[3], ra[3];
  unsigned sfl = 0;
  const lp pw = LP(L_W + wv * 5120 + vl), pw2 = LP(wv * 5120 + vl);
  SB0();
  if (wv > 4) {
    WAITV(4 * DH);
    if (lane == 0) __hip_atomic_fetch_add((LAS unsigned*)LP(L_RDY), 1u, __ATOMIC_RELAXED, __HIP_MEMORY_SCOPE_WORKGROUP);
  }
  lwait<1>(L_RDY, 0, 3);
  v4u wl = l128(pw);
  SB0();
#pragma unroll
  for (int it = 0; it < NIT; ++it) {
    if (it < NL) {
#pragma unroll
      for (int i = 0; i < 5; ++i) {
        const v4u wf = wl;
        if (5 * it + i + 1 < 5 * NL) wl = l128((5 * it + i + 1) / 5 == 2 ? pw2 + ((5 * it + i + 1) % 5) * 1024 : pw + ((5 * it + i + 1) / 5) * 40960 + ((5 * it + i + 1) % 5) * 1024);
#pragma unroll
        for (int j = 0; j < 4; ++j) acc[j][i] = mfma16(wf, hb[it % DH][j], acc[j][i]);
        SB0();
      }
    } else {
#pragma unroll
      for (int j = 0; j < 4; ++j)
#pragma unroll
        for (int i = 0; i < 5; ++i) acc[j][i] = mfma16(wb[it % DW][i], hb[it % DH][j], acc[j][i]);
    }
    SB0();
    if (it + DH == 6) {
      if (__builtin_expect(!__all(sfl == tag), 0)) {
        _Pragma("unroll 1") for (int spin = 0; spin < SPIN; ++spin) { FENCE();
          if (__all(ld4c(rctl, fo) == tag)) break;
          __builtin_amdgcn_s_sleep(2);
        }
      }
      FENCE();
      STAMP(18); STAMPC(28);
    }
    if (it == 0) sfl = ld4c(rctl, fo);
    if (it + DH < NIT) loadh(it + DH);
    if (it >= NL && it + DW < NIT) loadw(it + DW);
    if (it == NIT - DH) {
#pragma unroll
      for (int r = 0; r < 3; ++r) {
        const int sl = min(tid + 512 * r, 1279), row = sl / 20, qd = sl - 20 * row;
        const int tok = 128 * (id.rt >> 1) + 8 * (row >> 2) + 4 * (id.rt & 1) + (row & 3), d = 80 * id.ct + 4 * qd;
        oo[r] = tok * D_ + d;
        ra[r] = ((row & 3) * 5 + (qd >> 2)) * 1024 + (qd & 3) * 256 + (((row >> 2) + qd) & 15) * 16;
        xv[r] = *(const v2u*)(p.x + oo[r]); dv[r] = *(const v2u*)(p.dsk + d);
        sv[r] = ld16c(rctl, WS_STATS + tok * 16, 0);
      }
    }
#define BARIT 2
    if (it == BARIT) BAR();
    SB0();
  }
  STAMP(21); STAMPC(29);
#pragma unroll
  for (int i = 0; i < 5; ++i) {
    const lp rb = LP(wv * 20480 + (lane >> 4) * 256 + (((lane & 15) + 4 * i + (lane >> 4)) & 15) * 16);
#pragma unroll
    for (int j = 0; j < 4; ++j) sf4(rb + (j * 5 + i) * 1024, acc[j][i]);
  }
  BAR();
  STAMP(19);
#pragma unroll
  for (int r = 0; r < 3; ++r) {
    if (r < 2 || wv < 4) {
      const lp q0 = LP(ra[r]), q1 = LP(ra[r] + 4 * 20480);
      f32x4 sum = ((lf4(q0) + lf4(q0 + 20480)) + (lf4(q0 + 40960) + lf4(q0 + 61440))) + ((lf4(q1) + lf4(q1 + 20480)) + (lf4(q1 + 40960) + lf4(q1 + 61440)));
      const f32x4 xf = {bflo(xv[r].x), bfhi(xv[r].x), bflo(xv[r].y), bfhi(xv[r].y)};
      const f32x4 df = {bflo(dv[r].x), bfhi(dv[r].x), bflo(dv[r].y), bfhi(dv[r].y)};
      sum += xf + (xf - __uint_as_float(sv[r].x)) * __uint_as_float(sv[r].z) * df;
      __builtin_amdgcn_raw_buffer_store_b128(__builtin_bit_cast(v4u, sum), mkrs(p.out, L_ * D_ * 4), oo[r] * 4, 0, OUTAUX);
    }
  }
  STAMP(22);
}

extern "C" void kernel_launch(void* const* d_in, const int* in_sizes, int n_in, void* d_out, int out_size, void* d_ws, size_t ws_size, hipStream_t stream) {
  Par p;
  p.x = (const u16*)d_in[0]; p.lre = (const u16*)d_in[1]; p.lim = (const u16*)d_in[2];
  p.bre = (const u16*)d_in[3]; p.bim = (const u16*)d_in[4]; p.cre = (const u16*)d_in[5]; p.cim = (const u16*)d_in[6];
  p.ldt = (const u16*)d_in[7]; p.dsk = (const u16*)d_in[8];
  p.wg = (const u16*)d_in[10]; p.wu = (const u16*)d_in[11]; p.wd = (const u16*)d_in[12];
  p.ws = (unsigned char*)d_ws; p.out = (float*)d_out;
  (void)hipFuncSetAttribute((const void*)fused, hipFuncAttributeMaxDynamicSharedMemorySize, LDS1);
  fused<<<256, 512, LDS1, stream>>>(p);
}
```

```cpp
#include <hip/hip_runtime.h>
#include <stdint.h>

typedef unsigned short u16;
typedef unsigned v4u __attribute__((ext_vector_type(4)));
typedef unsigned v2u __attribute__((ext_vector_type(2)));
typedef float f32x4 __attribute__((ext_vector_type(4)));
typedef float f32x2 __attribute__((ext_vector_type(2)));
typedef float f32x16 __attribute__((ext_vector_type(16)));
typedef short bf16x8 __attribute__((ext_vector_type(8)));
typedef short s16x4 __attribute__((ext_vector_type(4)));
typedef __bf16 bf2 __attribute__((ext_vector_type(2)));
#define LAS __attribute__((address_space(3)))
#define DI __device__ __forceinline__
typedef LAS unsigned char* lp;

constexpr int L_ = 2048, D_ = 640, P_ = 640, F_ = 1728;
constexpr int NKB = 94;
constexpr unsigned WS_EPOCH = 0;
constexpr unsigned WS_FLW = 2048;
constexpr unsigned WS_FLH = 4096;
constexpr unsigned WS_AGG = WS_FLH + 32768;
constexpr unsigned WS_STATS = WS_AGG + 64 * 640 * 16;
constexpr unsigned WS_CTL_BYTES = WS_STATS + 32768;
constexpr unsigned WS_W2 = 1u << 20;
constexpr unsigned WS_W2_BYTES = 8 * NKB * 5120;
constexpr unsigned WS_H = 8u << 20;
constexpr unsigned WS_H_BYTES = 32 * NKB * 4096;

constexpr int UPO = 10368;
constexpr int STG = 32768 + 20480 + 128;
constexpr int LDS1 = 163840;
constexpr int L_STATS = 3 * STG;
constexpr int L_CS = L_STATS + 2048;
constexpr int L_CNT = L_CS + 1280, L_GO = L_CNT + 4, L_RDY = L_CNT + 8, L_CSF = L_CNT + 16;
constexpr int L_CST = 49152;
constexpr int L_W = STG;
constexpr int L_E = 136000, ESTR = 528;
constexpr int SPIN = 1 << 18;
constexpr unsigned OOB = 0x40000000u;

DI float bf2f(unsigned b) { return __uint_as_float(b << 16); }
DI float bflo(unsigned w) { return __uint_as_float(w << 16); }
DI float bfhi(unsigned w) { return __uint_as_float(w & 0xffff0000u); }
DI unsigned pk2(float a, float b) { f32x2 v = {a, b}; return __builtin_bit_cast(unsigned, __builtin_convertvector(v, bf2)); }
DI float rbf(float a) { return bflo(pk2(a, a) & 0xffffu); }
DI f32x4 mfma16(v4u a, v4u b, f32x4 c) { return __builtin_amdgcn_mfma_f32_16x16x32_bf16(__builtin_bit_cast(bf16x8, a), __builtin_bit_cast(bf16x8, b), c, 0, 0, 0); }
DI f32x16 mfma32(v4u a, v4u b, f32x16 c) { return __builtin_amdgcn_mfma_f32_32x32x16_bf16(__builtin_bit_cast(bf16x8, a), __builtin_bit_cast(bf16x8, b), c, 0, 0, 0); }
DI unsigned rfl(unsigned v) { return __builtin_amdgcn_readfirstlane(v); }
DI __amdgpu_buffer_rsrc_t mkrs(const void* p, unsigned bytes) { return __builtin_amdgcn_make_buffer_rsrc((void*)p, 0, bytes, 0x00020000); }
template <int LI, int SI> DI void glds(__amdgpu_buffer_rsrc_t rs, unsigned lb, unsigned voff, unsigned sb) {
  unsigned t;
  asm volatile("s_add_u32 m0, %1, %5\n\ts_add_u32 %0, %4, %6\n\tbuffer_load_dwordx4 %2, %3, %0 offen lds" : "=&s"(t) : "s"(lb), "v"(voff), "s"(rs), "s"(sb), "i"(LI), "i"(SI) : "scc");
}
template <int LI, int SI> DI void gldsc(__amdgpu_buffer_rsrc_t rs, unsigned lb, unsigned voff, unsigned sb) {
  unsigned t;
  asm volatile("s_add_u32 m0, %1, %5\n\ts_add_u32 %0, %4, %6\n\tbuffer_load_dwordx4 %2, %3, %0 offen sc1 lds" : "=&s"(t) : "s"(lb), "v"(voff), "s"(rs), "s"(sb), "i"(LI), "i"(SI) : "scc");
}
template <int... I> struct Seq {};
template <int N, int... I> struct MkSeq : MkSeq<N - 1, N - 1, I...> {};
template <int... I> struct MkSeq<0, I...> { typedef Seq<I...> T; };
template <int... I> DI void dma_lin(Seq<I...>, __amdgpu_buffer_rsrc_t rs, unsigned lb, unsigned vl, unsigned sb) { (gldsc<I * 1024, I * 1024>(rs, lb, vl, sb), ...); }
template <int... I> DI void dma_x(Seq<I...>, __amdgpu_buffer_rsrc_t rs, unsigned lb, unsigned v0, unsigned v1, unsigned sb) { (glds<I * 1024, (64 * (I & 1) + (I >> 1)) * 1280>(rs, lb, (I & 1) ? v1 : v0, sb), ...); }
template <int... I> DI void dma_b(Seq<I...>, __amdgpu_buffer_rsrc_t rs, unsigned lb, const unsigned* v, unsigned sb) { (glds<I * 1024, (16 * (I & 1) + 4 * (I >> 1)) * 1280>(rs, lb, v[I & 1], sb), ...); }
template <int... I> DI void dma_g(Seq<I...>, __amdgpu_buffer_rsrc_t rs, unsigned lb, const unsigned* v, unsigned sb) { (glds<I * 1024, 0>(rs, lb, v[I], sb), ...); }
DI lp LP(unsigned a) { return (lp)(uintptr_t)a; }
DI v4u l128(lp a) { return *(LAS v4u*)a; }
DI f32x4 lf4(lp a) { return *(LAS f32x4*)a; }
DI void sf4(lp a, f32x4 v) { *(LAS f32x4*)a = v; }
DI s16x4 ltr(lp a) { return __builtin_amdgcn_ds_read_tr16_b64_v4i16((LAS s16x4*)a); }
DI v4u cat(s16x4 lo, s16x4 hi) { return __builtin_bit_cast(v4u, __builtin_shufflevector(lo, hi, 0, 1, 2, 3, 4, 5, 6, 7)); }
DI v4u ld16(__amdgpu_buffer_rsrc_t r, unsigned vo, unsigned so) { return __builtin_amdgcn_raw_buffer_load_b128(r, vo, so, 0); }
DI v4u ld16c(__amdgpu_buffer_rsrc_t r, unsigned vo, unsigned so) { return __builtin_amdgcn_raw_buffer_load_b128(r, vo, so, 16); }
DI unsigned ld4c(__amdgpu_buffer_rsrc_t r, unsigned vo) { return __builtin_amdgcn_raw_buffer_load_b32(r, vo, 0, 16); }
DI void st16c(v4u v, __amdgpu_buffer_rsrc_t r, unsigned vo) { __builtin_amdgcn_raw_buffer_store_b128(v, r, vo, 0, 16); }
DI void st4c(unsigned v, __amdgpu_buffer_rsrc_t r, unsigned vo) { __builtin_amdgcn_raw_buffer_store_b32(v, r, vo, 0, 16); }
#define BAR() asm volatile("s_waitcnt lgkmcnt(0)\n\ts_barrier" ::: "memory")
#define SB0() __builtin_amdgcn_sched_barrier(0)
#define WAITV(n) asm volatile("s_waitcnt vmcnt(%0)" ::"i"(n) : "memory")
#define FENCE() asm volatile("" ::: "memory")
template <int C> DI float dppf(float v) { return __int_as_float(__builtin_amdgcn_update_dpp(0, __float_as_int(v), C, 0xf, 0xf, true)); }
template <int N> DI f32x4 shr4(f32x4 v) { return f32x4{dppf<0x110 + N>(v.x), dppf<0x110 + N>(v.y), dppf<0x110 + N>(v.z), dppf<0x110 + N>(v.w)}; }
DI float sum8(float v) {
  v += dppf<0xB1>(v); v += dppf<0x4E>(v); v += dppf<0x141>(v);
  return v;
}
DI float dot2(unsigned a, unsigned b, float c) { return __builtin_amdgcn_fdot2_f32_bf16(__builtin_bit_cast(bf2, a), __builtin_bit_cast(bf2, b), c, false); }
DI void gather4(float x, float (&t)[4]) {
  const auto a = __builtin_amdgcn_permlane16_swap(__float_as_uint(x), __float_as_uint(x), false, false);
  const auto e = __builtin_amdgcn_permlane32_swap(a[0], a[0], false, false), o = __builtin_amdgcn_permlane32_swap(a[1], a[1], false, false);
  t[0] = __uint_as_float(e[0]); t[2] = __uint_as_float(e[1]); t[1] = __uint_as_float(o[0]); t[3] = __uint_as_float(o[1]);
}
DI void cm1(float& a, float& b, float lr, float li, float ur, float ui) {
  const float na = fmaf(lr, a, fmaf(-li, b, ur)), nb = fmaf(lr, b, fmaf(li, a, ui));
  a = na; b = nb;
}
DI f32x4 fma4(f32x4 a, f32x4 b, f32x4 c) { return __builtin_elementwise_fma(a, b, c); }
DI f32x4 sp4(float a) { return f32x4{a, a, a, a}; }
DI void cmac(f32x4& a, f32x4& b, f32x4 lr, f32x4 li, f32x4 ur, f32x4 ui) {
  const f32x4 na = fma4(lr, a, fma4(-li, b, ur)), nb = fma4(lr, b, fma4(li, a, ui));
  a = na; b = nb;
}

#define STAMP(i)
#define STAMPC(i)

#define LSTAMP(i)
struct Par {
  const u16 *x, *lre, *lim, *bre, *bim, *cre, *cim, *ldt, *dsk, *wg, *wu, *wd;
  unsigned char* ws;
  float* out;
};
struct Ids { int R, g, cg, rt, ct; };
#define CODEPF 46080

#define PDW 4
#define PDH 3
constexpr int DW = PDW, DH = PDH, NIT = 12, NL = 3;
#define PIN4(x) asm volatile("" : "+v"(x))
DI void sig(unsigned addr, unsigned v) {
  asm volatile("s_mov_b64 exec, 1\n\tds_write_b32 %0, %1\n\ts_mov_b64 exec, -1" ::"v"(addr), "v"(v) : "memory");
}
DI unsigned lrd(unsigned addr) { return *(volatile LAS unsigned*)LP(addr); }
#define LSLEEP 1
template <int SL = 1> DI void lwait(unsigned addr, unsigned v, unsigned need) {
  if (__builtin_expect(!__all(v >= need), 0)) {
    _Pragma("unroll 1") for (int spin = 0; spin < 64 * SPIN; ++spin) { FENCE();
      if (__all(lrd(addr) >= need)) break;
      __builtin_amdgcn_s_sleep(SL);
    }
  }
  FENCE();
}

struct WRing {
  v4u wb[DW][5];
  __amdgpu_buffer_rsrc_t rw;
  unsigned vl, wv;
  DI void load(int it) {
#pragma unroll
    for (int i = 0; i < 5; ++i) { wb[it % DW][i] = ld16c(rw, vl + (i & 3) * 1024, (8 * it + wv) * 5120 + (i >> 2) * 4096); SB0(); }
  }
  DI void dma01() {
    asm volatile("s_nop 4");
    dma_lin(MkSeq<20>::T{}, rw, L_W + (wv - 4) * 20480, vl, (wv - 4) * 20480);
  }
};

DI float wsum(float v) {
  v = sum8(v); v += dppf<0x128>(v);
  float t[4]; gather4(v, t);
  return (t[0] + t[1]) + (t[2] + t[3]);
}
DI unsigned mktag(unsigned epoch) { return rfl(epoch) * 0x9E3779B1u + 0x7F4A7C15u; }
struct RowIn { v4u xa[2], f[5]; unsigned xb[2], epoch; };
template <bool SSM> DI void row_load(const Par& p, const Ids& id, const int w, const int lane, RowIn& in) {
  const int row = 256 * id.R + 8 * id.cg + 2 * w;
#pragma unroll
  for (int z = 0; z < 2; ++z) { in.xa[z] = *(const v4u*)(p.x + (row + z) * D_ + lane * 8); in.xb[z] = *(const unsigned*)(p.x + (row + z) * D_ + 512 + lane * 2); }
  in.epoch = ld4c(mkrs(p.ws, WS_CTL_BYTES), WS_EPOCH);
  if constexpr (SSM) {
    const int kb = 4 * id.g + w;
    const __amdgpu_buffer_rsrc_t r = mkrs(kb < 20 ? p.cre : p.cim, D_ * P_ * 2);
#pragma unroll
    for (int i = 0; i < 5; ++i) in.f[i] = ld16(r, (80 * id.R + (lane >> 2)) * 1280 + (kb < 20 ? kb : kb - 20) * 64 + (lane & 3) * 16, i * (16 * 1280));
  }
  SB0();
}
DI unsigned row_stats(const Par& p, const Ids& id, const int w, const int lane, const RowIn& in) {
  const __amdgpu_buffer_rsrc_t rctl = mkrs(p.ws, WS_CTL_BYTES);
  const int row = 256 * id.R + 8 * id.cg + 2 * w;
  const v4u* xa = in.xa; const unsigned* xb = in.xb; const unsigned epoch = in.epoch;
  float v[2][10], mean[2], s2[2];
#pragma unroll
  for (int z = 0; z < 2; ++z) {
    const float t[10] = {bflo(xa[z].x), bfhi(xa[z].x), bflo(xa[z].y), bfhi(xa[z].y), bflo(xa[z].z), bfhi(xa[z].z), bflo(xa[z].w), bfhi(xa[z].w), bflo(xb[z]), bfhi(xb[z])};
#pragma unroll
    for (int e = 0; e < 10; ++e) v[z][e] = t[e];
    mean[z] = wsum(((t[0] + t[1]) + (t[2] + t[3])) + ((t[4] + t[5]) + (t[6] + t[7])) + (t[8] + t[9])) * (1.f / D_);
  }
#pragma unroll
  for (int z = 0; z < 2; ++z) {
    float a = 0.f, b = 0.f;
#pragma unroll
    for (int e = 0; e < 5; ++e) { a = fmaf(v[z][e] - mean[z], v[z][e] - mean[z], a); b = fmaf(v[z][5 + e] - mean[z], v[z][5 + e] - mean[z], b); }
    s2[z] = wsum(a + b);
  }
  const unsigned tag = mktag(epoch);
  const float m = rbf((lane & 1) ? mean[1] : mean[0]), rs = 1.f / rbf(sqrtf(rbf(rbf(((lane & 1) ? s2[1] : s2[0]) * (1.f / D_)) + 1e-5f)));
  st16c(v4u{__float_as_uint(m), tag, __float_as_uint(rs), tag}, rctl, lane < 2 ? WS_STATS + (row + lane) * 16 : OOB);
  return tag;
}

template <bool SSM>
DI unsigned compute_wave(const Par& p, const Ids& id, const int w, const int lane) {
  STAMP(0);
  unsigned tag, cf = 0;
  RowIn rin;
  row_load<SSM>(p, id, w, lane, rin);
  if constexpr (!SSM) {
    const int r = lane & 31, hh = lane >> 5;
    const unsigned xa = (64 * w + r) * 128 + ((hh ^ ((r >> 1) & 7)) * 16);
    const unsigned wa = 32768 + ((lane >> 4) & 1) * UPO + (8 * hh + ((lane & 15) >> 2)) * 160 + (lane & 3) * 8;
    f32x16 acc[5][2] = {};
#pragma unroll
    for (int e = 0; e < 5; ++e) { PIN4(acc[e][0]); PIN4(acc[e][1]); }
    v4u fx[2][2]; s16x4 fl[4], fh[4];
    lp px[4], pw;
    auto setp = [&](unsigned sb) {
#pragma unroll
      for (int s = 0; s < 4; ++s) px[s] = LP(sb + (xa ^ (32 * s)));
      pw = LP(sb + wa);
    };
    auto woff = [&](int n) { return (n / 5) * 2560 + (n % 5) * 32; };
    setp(0);
    SB0();
    tag = row_stats(p, id, w, lane, rin);
    STAMP(17);
    SB0();
    BAR();
    STAMP(1);
    fx[0][0] = l128(px[0]); fx[0][1] = l128(px[0] + 4096);
#pragma unroll
    for (int n = 0; n < 3; ++n) { fl[n] = ltr(pw + woff(n)); fh[n] = ltr(pw + woff(n) + 640); }
    unsigned sb = 0;
#pragma unroll 1
    for (int i = 0; i < 10; ++i) {
#pragma unroll
      for (int n = 0; n < 20; ++n) {
        const int ks = n / 5, e = n % 5, nn = (n + 3) % 20;
        if (n == 17) { if (i < 9) BAR(); LSTAMP(i); sb = sb == 2 * STG ? 0 : sb + STG; setp(sb); SB0(); }
        const v4u wf = cat(fl[n & 3], fh[n & 3]);
        acc[e][0] = mfma32(wf, fx[ks & 1][0], acc[e][0]); SB0();
        fl[(n + 3) & 3] = ltr(pw + woff(nn));
        fh[(n + 3) & 3] = ltr(pw + woff(nn) + 640); SB0();
        acc[e][1] = mfma32(wf, fx[ks & 1][1], acc[e][1]); SB0();
        if (ks < 3 && e == 1) { fx[(ks + 1) & 1][0] = l128(px[ks + 1]); fx[(ks + 1) & 1][1] = l128(px[ks + 1] + 4096); }
        if (n == 17) { fx[0][0] = l128(px[0]); fx[0][1] = l128(px[0] + 4096); }
        if (n == 18) cf = lrd(L_CSF + (lane & 3) * 4);
        SB0();
      }
    }
    lwait(L_CSF + (lane & 3) * 4, cf, 1);
    STAMP(12);
    const __amdgpu_buffer_rsrc_t rh = mkrs(p.ws + WS_H + (unsigned)(4 * id.R + w) * (NKB * 4096), 54 * 4096);
    const unsigned so0 = (r >> 4) * 1024 + (hh * 16 + (r & 15)) * 16;
    float mu[2], r2[2];
#pragma unroll
    for (int u = 0; u < 2; ++u) {
      const int F = 4 * w + 2 * u + (r >> 4);
      const f32x2 s = *(LAS f32x2*)LP(L_STATS + (128 * (F >> 3) + 8 * (r & 15) + (F & 7)) * 8);
      mu[u] = s.x; r2[u] = s.y * s.y;
    }
    const lp pc = LP(L_CS + hh * 16);
#pragma unroll
    for (int e = 0; e < 5; ++e) {
      const f32x4 g0 = lf4(pc + e * 64) + lf4(pc + e * 64 + 640), g1 = lf4(pc + e * 64 + 32) + lf4(pc + e * 64 + 672);
      const f32x4 u0 = lf4(pc + e * 64 + 320) + lf4(pc + e * 64 + 960), u1 = lf4(pc + e * 64 + 352) + lf4(pc + e * 64 + 992);
      const int T = 5 * id.g + e;
      const unsigned so = so0 + (T >> 1) * 4096 + (T & 1) * 512;
#pragma unroll
      for (int u = 0; u < 2; ++u) {
        const f32x16 a = acc[e][u];
        const f32x4 nm = sp4(-mu[u]);
        const f32x4 h0 = fma4(nm, g0, f32x4{a[0], a[1], a[2], a[3]}) * fma4(nm, u0, f32x4{a[8], a[9], a[10], a[11]}) * r2[u];
        const f32x4 h1 = fma4(nm, g1, f32x4{a[4], a[5], a[6], a[7]}) * fma4(nm, u1, f32x4{a[12], a[13], a[14], a[15]}) * r2[u];
        st16c(v4u{pk2(h0.x, h0.y), pk2(h0.z, h0.w), pk2(h1.x, h1.y), pk2(h1.z, h1.w)}, rh, so + u * 2048);
      }
    }
  } else {
    const int c = lane & 15, q = lane >> 4, G = w >> 1, blk = w & 1;
    const unsigned xa = (128 * G + c) * 128 + ((q ^ (c >> 1)) * 16);
    const unsigned wa = 32768 + (32 * blk + c) * 128 + ((q ^ (c >> 1)) * 16);
    f32x4 acc[8][4] = {};
#pragma unroll
    for (int j = 0; j < 8; ++j)
#pragma unroll
      for (int t = 0; t < 4; ++t) PIN4(acc[j][t]);
    v4u fw[2][4], fx[4];
    lp px[2], pw[2];
    auto setp = [&](unsigned sb) { px[0] = LP(sb + xa); px[1] = LP(sb + (xa ^ 64)); pw[0] = LP(sb + wa); pw[1] = LP(sb + (wa ^ 64)); };
    auto woff = [&](int t) { return (t >> 1) * 8192 + (t & 1) * 2048; };
    setp(0);
    SB0();
    tag = row_stats(p, id, w, lane, rin);
    {
      const __amdgpu_buffer_rsrc_t r2 = mkrs(p.ws + WS_W2 + (unsigned)(id.R * NKB + 54 + 4 * id.g + w) * 5120, 5120);
#pragma unroll
      for (int i = 0; i < 5; ++i) {
        v4u o;
#pragma unroll
        for (int e = 0; e < 4; ++e) o[e] = __builtin_amdgcn_ds_bpermute((4 * c + q) * 4, rin.f[i][e]);
        st16c(o, r2, i * 1024 + lane * 16);
      }
    }
    STAMP(17);
    SB0();
    BAR();
    STAMP(1);
#pragma unroll
    for (int t = 0; t < 4; ++t) fw[0][t] = l128(pw[0] + woff(t));
#pragma unroll
    for (int n = 0; n < 3; ++n) fx[n] = l128(px[0] + n * 2048);
    unsigned sb = 0;
#pragma unroll 1
    for (int i = 0; i < 10; ++i) {
#pragma unroll
      for (int n = 0; n < 16; ++n) {
        const int kk = n >> 3, j = n & 7, nn = (n + 3) & 15;
        if (n == 13) { if (i == 2) WAITV(0); if (i < 9) BAR(); LSTAMP(i); sb = sb == 2 * STG ? 0 : sb + STG; setp(sb); SB0(); }
        const v4u xf = fx[n & 3];
        acc[j][0] = mfma16(fw[kk][0], xf, acc[j][0]); SB0();
        fx[(n + 3) & 3] = l128(px[nn >> 3] + (nn & 7) * 2048); SB0();
        acc[j][1] = mfma16(fw[kk][1], xf, acc[j][1]); SB0();
        if (n >= 2 && n < 6) fw[1][n - 2] = l128(pw[1] + woff(n - 2));
        if (n == 13) fw[0][0] = l128(pw[0] + woff(0));
        if (n == 14) fw[0][2] = l128(pw[0] + woff(2));
        SB0();
        acc[j][2] = mfma16(fw[kk][2], xf, acc[j][2]); SB0();
        if (n == 13) fw[0][1] = l128(pw[0] + woff(1));
        if (n == 14) fw[0][3] = l128(pw[0] + woff(3));
        SB0();
        acc[j][3] = mfma16(fw[kk][3], xf, acc[j][3]); SB0();
        if (n == 14) { cf = lrd(L_CSF + (lane & 3) * 4); SB0(); }
      }
    }
    lwait(L_CSF + (lane & 3) * 4, cf, 1);
    STAMP(12);
    const lp pe = LP(L_E + (16 * G + c) * ESTR + (32 * blk + 8 * q) * 4);
    const lp pk = LP(L_CST + (32 * blk + 8 * q) * 4);
    f32x4 lr[2], li[2], er[2], ei[2];
    {
      const lp ps = LP(L_STATS + (128 * G + 8 * c) * 8), pc = LP(L_CS + (32 * blk + 8 * q) * 4);
      f32x4 cs[4], st[4];
#pragma unroll
      for (int t = 0; t < 4; ++t) { cs[t] = lf4(pc + (t >> 1) * 320 + (t & 1) * 16) + lf4(pc + 640 + (t >> 1) * 320 + (t & 1) * 16); st[t] = lf4(ps + t * 16); }
#pragma unroll
      for (int ab = 0; ab < 2; ++ab) { lr[ab] = lf4(pk + ab * 16); li[ab] = lf4(pk + 256 + ab * 16); }
#pragma unroll
      for (int jj = 0; jj < 4; ++jj)
#pragma unroll
        for (int t = 0; t < 4; ++t) {
          acc[2 * jj][t] = fma4(sp4(-st[jj].x), cs[t], acc[2 * jj][t]) * st[jj].y;
          acc[2 * jj + 1][t] = fma4(sp4(-st[jj].z), cs[t], acc[2 * jj + 1][t]) * st[jj].w;
        }
    }
#pragma unroll
    for (int ab = 0; ab < 2; ++ab) { er[ab] = acc[0][ab]; ei[ab] = acc[0][2 + ab]; }
#pragma unroll
    for (int j = 1; j < 8; ++j)
#pragma unroll
      for (int ab = 0; ab < 2; ++ab) cmac(er[ab], ei[ab], lr[ab], li[ab], acc[j][ab], acc[j][2 + ab]);
#pragma unroll
    for (int ab = 0; ab < 2; ++ab) { sf4(pe + ab * 16, er[ab]); sf4(pe + 256 + ab * 16, ei[ab]); }
    SB0();
    BAR();
    STAMP(13);
#pragma unroll
    for (int ab = 0; ab < 2; ++ab) {
      const f32x4 kr = lf4(pk + 512 * 4 + ab * 16), ki = lf4(pk + 512 * 4 + 256 + ab * 16);
#pragma unroll
      for (int j = 0; j < 8; ++j) {
        const f32x4 ur = acc[j][ab], ui = acc[j][2 + ab];
        acc[j][ab] = fma4(-ki, ui, kr * ur); acc[j][2 + ab] = fma4(-ki, ur, -kr * ui);
        PIN4(acc[j][ab]); PIN4(acc[j][2 + ab]);
      }
      li[ab] = -li[ab];
    }
    const __amdgpu_buffer_rsrc_t rh = mkrs(p.ws + WS_H + (unsigned)(4 * id.R + 2 * G) * (NKB * 4096), 2 * NKB * 4096);
    unsigned vo[4];
#pragma unroll
    for (int z = 0; z < 4; ++z) { vo[z] = (54 + 2 * id.g + blk + 20 * (z & 1) + NKB * (z >> 1)) * 4096 + lane * 16; PIN4(vo[z]); }
    SB0();
    BAR();
    STAMP(14);
#pragma unroll
    for (int ab = 0; ab < 2; ++ab) { er[ab] = lf4(pe + ab * 16); ei[ab] = lf4(pe + 256 + ab * 16); }
#pragma unroll
    for (int j = 0; j < 8; ++j) {
#pragma unroll
      for (int ab = 0; ab < 2; ++ab) cmac(er[ab], ei[ab], lr[ab], li[ab], acc[j][ab], acc[j][2 + ab]);
      st16c(v4u{pk2(er[0].x, er[0].y), pk2(er[0].z, er[0].w), pk2(er[1].x, er[1].y), pk2(er[1].z, er[1].w)}, rh, vo[2 * (j >> 2)] + (j & 3) * 1024);
      st16c(v4u{pk2(ei[0].x, ei[0].y), pk2(ei[0].z, ei[0].w), pk2(ei[1].x, ei[1].y), pk2(ei[1].z, ei[1].w)}, rh, vo[2 * (j >> 2) + 1] + (j & 3) * 1024);
    }
  }
  SB0();
  STAMP(15);
  return tag;
}

#define HBAR() do { SB0(); BAR(); SB0(); } while (0)
struct HState { f32x4 acs[5]; unsigned pl, pr, pi; };
template <bool SSM>
DI unsigned helper_wave(const Par& p, const Ids& id, const int h, const int lane, HState& hs) {
  STAMP(0);
  constexpr int NWP = SSM ? 4 : 5, NP = 8 + NWP;
  constexpr unsigned WSTEP = SSM ? 128u : 64u * 3456u;
  const int c = lane & 15, q = lane >> 4;
  const __amdgpu_buffer_rsrc_t rctl = mkrs(p.ws, WS_CTL_BYTES);
  const __amdgpu_buffer_rsrc_t rx = mkrs(p.x, L_ * D_ * 2);
  const __amdgpu_buffer_rsrc_t rw = mkrs(SSM ? ((h >> 1) ? p.bim : p.bre) : ((h >> 1) ? p.wu : p.wg), SSM ? P_ * D_ * 2 : D_ * F_ * 2);
  const unsigned v0 = (8 * (lane >> 3)) * 1280 + (((lane & 7) ^ (lane >> 4)) * 16), v1 = v0 ^ 64;
#define ROT 0
  unsigned st = ROT == 0 ? 0 : ROT == 1 ? 5 * ((blockIdx.x >> 2) & 1) : ROT == 2 ? (5 * (blockIdx.x & 7)) >> 2 : 5 * (((blockIdx.x >> 2) ^ blockIdx.x) & 1);
  const unsigned sx = (256 * id.R + 128 * (h >> 1) + 4 * (h & 1)) * 1280;
  auto issue_x = [&](unsigned s, unsigned sb) {
    dma_x(MkSeq<8>::T{}, rx, sb + h * 8192, v0, v1, s);
  };
  asm volatile("s_nop 4");
  issue_x(sx + st * 128, 0);
  unsigned vw[NWP];
  if constexpr (SSM) {
    vw[0] = (8 * (lane >> 5) + ((lane >> 3) & 3)) * 1280 + (((lane & 7) ^ (lane >> 4)) * 16); vw[1] = vw[0] ^ 64;
  } else {
#pragma unroll
    for (int i = 0; i < 5; ++i) { const int lc = 64 * (5 * (h & 1) + i) + lane, pos = lc / 10; vw[i] = pos * 3456 + (lc - pos * 10) * 16; }
  }
  const unsigned sw = SSM ? (64 * id.g + 32 * (h & 1)) * 1280 : 160 * id.g;
  const unsigned dw = 32768 + (h >> 1) * (SSM ? 8192 : UPO) + (h & 1) * NWP * 1024;
  auto issue_w = [&](unsigned s, unsigned sb) {
    if constexpr (SSM) dma_b(MkSeq<4>::T{}, rw, sb + dw, vw, s); else dma_g(MkSeq<5>::T{}, rw, sb + dw, vw, s);
  };
  issue_w(sw + st * WSTEP, 0);
  unsigned sb = STG;
  auto step = [&]() {
    st = st == 9 ? 0 : st + 1;
    issue_x(sx + st * 128, sb); issue_w(sw + st * WSTEP, sb);
    sb = sb == 2 * STG ? 0 : sb + STG;
  };
  step();
  if (h == 3 && lane < 8) *(LAS unsigned*)LP(L_CNT + lane * 4) = 0u;
  f32x4 acs[NWP] = {};
  v4u fc[NWP];
  const unsigned ca = SSM ? 32768 + (h >> 1) * 8192 + c * 128 + (((4 * (h & 1) + q) ^ (c >> 1)) * 16)
                          : 32768 + (h >> 1) * UPO + (32 * (h & 1) + 4 * q + (c >> 2)) * 160 + (c & 3) * 8;
  const v4u ones = {0x3F803F80u, 0x3F803F80u, 0x3F803F80u, 0x3F803F80u};
  auto cs_rd = [&](unsigned s) {
    const lp a = LP(s + ca);
#pragma unroll
    for (int t = 0; t < NWP; ++t) { if constexpr (SSM) fc[t] = l128(a + t * 2048); else fc[t] = cat(ltr(a + t * 32), ltr(a + t * 32 + 2560)); }
    SB0();
  };
  auto cs_mm = [&]() {
    SB0();
    __builtin_amdgcn_s_setprio(2);
#pragma unroll
    for (int t = 0; t < NWP; ++t) acs[t] = mfma16(fc[t], ones, acs[t]);
    __builtin_amdgcn_s_setprio(0);
    SB0();
  };
  const int tsk = (id.g * 8 + id.R) * 4 + h, kb = tsk / 10, dq = tsk - 10 * kb;
  v4u f[4];
  auto rt_load = [&]() {
    const __amdgpu_buffer_rsrc_t r = mkrs(p.wd, F_ * D_ * 2);
    const int cc = lane >> 2, qq = lane & 3;
    const unsigned vo = (32 * kb + 16 * (cc >> 3) + 4 * ((cc >> 2) & 1) + (cc & 3)) * 1280 + dq * 128 + qq * 16;
    asm volatile("buffer_load_dwordx4 %0, %4, %5, 0 offen\n\tbuffer_load_dwordx4 %2, %4, %5, 0 offen offset:64\n\tbuffer_load_dwordx4 %1, %4, %5, %6 offen\n\tbuffer_load_dwordx4 %3, %4, %5, %6 offen offset:64"
                 : "=&v"(f[0]), "=&v"(f[1]), "=&v"(f[2]), "=&v"(f[3]) : "v"(vo), "s"(r), "s"(8 * 1280));
  };
  auto rt_store = [&]() {
#pragma unroll
    for (int i = 0; i < 4; ++i)
#pragma unroll
      for (int e = 0; e < 4; ++e) f[i][e] = __builtin_amdgcn_ds_bpermute((4 * c + q) * 4, f[i][e]);
    v4u sel[2];
#pragma unroll
    for (int hf = 0; hf < 2; ++hf)
#pragma unroll
      for (int e = 0; e < 4; ++e) sel[hf][e] = (8 * q + 2 * e == 16 * hf + c ? 0x3F80u : 0u) | (8 * q + 2 * e + 1 == 16 * hf + c ? 0x3F800000u : 0u);
    const __amdgpu_buffer_rsrc_t r2 = mkrs(p.ws + WS_W2, tsk < 540 ? WS_W2_BYTES : 0);
    __builtin_amdgcn_s_setprio(2);
#pragma unroll
    for (int i = 0; i < 4; ++i) {
      const f32x4 a = mfma16(f[i & ~1], sel[i & 1], f32x4{0.f, 0.f, 0.f, 0.f}), b = mfma16(f[i | 1], sel[i & 1], f32x4{0.f, 0.f, 0.f, 0.f});
      const int dt = 4 * dq + i, ct = dt / 5;
      st16c(v4u{pk2(a.x, a.y), pk2(a.z, a.w), pk2(b.x, b.y), pk2(b.z, b.w)}, r2, ((ct * NKB + kb) * 5 + dt - 5 * ct) * 1024 + lane * 16);
    }
    __builtin_amdgcn_s_setprio(0);
    SB0();
  };
  unsigned epoch, pl = 0, pr = 0, pi = 0;
  asm volatile("buffer_load_dword %0, off, %1, 0 sc1" : "=v"(epoch) : "s"(rctl));
  if constexpr (SSM) {
    const unsigned po = (64 * id.g + lane) * 2;
    asm volatile("buffer_load_ushort %0, %3, %4, 0 offen\n\tbuffer_load_ushort %1, %3, %5, 0 offen\n\tbuffer_load_ushort %2, %3, %6, 0 offen"
                 : "=&v"(pl), "=&v"(pr), "=&v"(pi) : "v"(po), "s"(mkrs(p.ldt, P_ * 2)), "s"(mkrs(p.lre, P_ * 2)), "s"(mkrs(p.lim, P_ * 2)));
  }
  WAITV(NP + (SSM ? 4 : 1)); BAR();
  cs_rd(0);
  step();
  if constexpr (!SSM) rt_load();
  cs_mm();
  asm volatile("s_waitcnt vmcnt(%4)" : "+v"(epoch), "+v"(pl), "+v"(pr), "+v"(pi) : "i"(NP + (SSM ? 0 : 4)) : "memory");
  BAR();
  cs_rd(STG); step(); cs_mm();
  if constexpr (SSM) WAITV(NP);
  else asm volatile("s_waitcnt vmcnt(%4)" : "+v"(f[0]), "+v"(f[1]), "+v"(f[2]), "+v"(f[3]) : "i"(NP) : "memory");
  BAR();
  cs_rd(2 * STG);
  step();
  const unsigned tag = mktag(epoch);
  cs_mm();
  if constexpr (!SSM) rt_store();
  WAITV(NP + (SSM ? 0 : 4)); BAR();
  cs_rd(0);
  step(); cs_mm();
  WAITV(NP); BAR();
  cs_rd(STG);
  st4c(tag, rctl, (h == 3 && lane == 0) ? WS_FLW + blockIdx.x * 4 : OOB);
  SB0();
  step(); cs_mm();
  WAITV(NP + 1); BAR();
  cs_rd(2 * STG);
  v4u sv;
  asm volatile("buffer_load_dwordx4 %0, %1, %2, 0 offen sc1" : "=v"(sv) : "v"(WS_STATS + (256 * id.R + 64 * h + lane) * 16), "s"(rctl));
  step(); cs_mm();
  asm volatile("s_waitcnt vmcnt(%1)" : "+v"(sv) : "i"(NP) : "memory");
  if (!__all((sv.y == tag) & (sv.w == tag))) {
    _Pragma("unroll 1") for (int spin = 0; spin < SPIN; ++spin) { FENCE();
      sv = ld16c(rctl, WS_STATS + (256 * id.R + 64 * h + lane) * 16, 0);
      if (__all((sv.y == tag) & (sv.w == tag))) break;
      __builtin_amdgcn_s_sleep(4);
    }
  }
  *(LAS v2u*)LP(L_STATS + (64 * h + lane) * 8) = v2u{sv.x, sv.z};
  BAR();
  cs_rd(0); step(); cs_mm();
  WAITV(NP); BAR();
  cs_rd(STG);
  auto w2_check = [&](v4u fl) {
    if (h == 3) {
      if (!__all((fl.x == tag) & (fl.y == tag) & (fl.z == tag) & (fl.w == tag))) {
        _Pragma("unroll 1") for (int spin = 0; spin < SPIN; ++spin) { FENCE();
          const v4u f2 = ld16c(rctl, WS_FLW + lane * 16, 0);
          if (__all((f2.x == tag) & (f2.y == tag) & (f2.z == tag) & (f2.w == tag))) break;
          __builtin_amdgcn_s_sleep(4);
        }
      }
      st4c(tag, rctl, (lane == 0 && blockIdx.x == 255) ? WS_EPOCH : OOB);
    }
  };
  if constexpr (!SSM) {
    v4u fl;
    asm volatile("buffer_load_dwordx4 %0, %1, %2, 0 offen sc1" : "=v"(fl) : "v"(h == 3 ? WS_FLW + lane * 16 : OOB), "s"(rctl));
    step(); cs_mm();
    asm volatile("s_waitcnt vmcnt(%1)" : "+v"(fl) : "i"(NP) : "memory");
    w2_check(fl);
  } else {
    step(); cs_mm();
    WAITV(NP);
  }
  BAR();
#pragma unroll
  for (int t = 0; t < 5; ++t) hs.acs[t] = acs[t < NWP ? t : 0];
  hs.pl = pl; hs.pr = pr; hs.pi = pi;
  return tag;
}
template <bool SSM>
DI void helper_tail(const Par& p, const Ids& id, const int h, const int lane, const unsigned tag, HState& hs) {
  constexpr int NWP = SSM ? 4 : 5;
  const int c = lane & 15, q = lane >> 4;
  const __amdgpu_buffer_rsrc_t rctl = mkrs(p.ws, WS_CTL_BYTES);
  f32x4* acs = hs.acs;
  const unsigned pl = hs.pl, pr = hs.pr, pi = hs.pi;
  v4u fc[NWP];
  const unsigned ca = SSM ? 32768 + (h >> 1) * 8192 + c * 128 + (((4 * (h & 1) + q) ^ (c >> 1)) * 16)
                          : 32768 + (h >> 1) * UPO + (32 * (h & 1) + 4 * q + (c >> 2)) * 160 + (c & 3) * 8;
  const v4u ones = {0x3F803F80u, 0x3F803F80u, 0x3F803F80u, 0x3F803F80u};
  auto cs_rd = [&](unsigned s) {
    const lp a = LP(s + ca);
#pragma unroll
    for (int t = 0; t < NWP; ++t) { if constexpr (SSM) fc[t] = l128(a + t * 2048); else fc[t] = cat(ltr(a + t * 32), ltr(a + t * 32 + 2560)); }
    SB0();
  };
  auto cs_mm = [&]() {
    SB0();
    __builtin_amdgcn_s_setprio(2);
#pragma unroll
    for (int t = 0; t < NWP; ++t) acs[t] = mfma16(fc[t], ones, acs[t]);
    __builtin_amdgcn_s_setprio(0);
    SB0();
  };
  cs_rd(2 * STG);
  if (SSM) {
    if (h == 3) {
      _Pragma("unroll 1") for (int spin = 0; spin < SPIN; ++spin) { FENCE();
        const v4u f2 = ld16c(rctl, WS_FLW + lane * 16, 0);
        if (__all((f2.x == tag) & (f2.y == tag) & (f2.z == tag) & (f2.w == tag))) break;
        __builtin_amdgcn_s_sleep(4);
      }
    }
  }
  float br = 0.f, bi = 0.f;
  if (SSM && h == 2) {
    const float dt = rbf(expf(bf2f(pl))), ex = expf(dt * bf2f(pr));
    float sn, cn;
    sincosf(dt * bf2f(pi), &sn, &cn);
    br = ex * cn; bi = ex * sn;
  }
  cs_mm();
  WAITV(SSM ? 0 : 15); BAR();
  cs_rd(0);
  if (SSM && h == 2) {
    const float lr = bf2f(pr), li = bf2f(pi), nr = br - 1.f, ni = bi, den = lr * lr + li * li;
    LAS float* cst = (LAS float*)LP(L_CST);
    cst[4 * 128 + lane] = 2.f * (nr * lr + ni * li) / den; cst[4 * 128 + 64 + lane] = 2.f * (ni * lr - nr * li) / den;
    cst[lane] = br; cst[64 + lane] = bi;
#pragma unroll
    for (int s = 1; s <= 8; ++s) {
      const float a = br * br - bi * bi, b = 2.f * br * bi;
      br = a; bi = b;
      const int k = s == 3 ? 1 : s == 6 ? 2 : s == 8 ? 3 : 0;
      if (k) { cst[k * 128 + lane] = br; cst[k * 128 + 64 + lane] = bi; }
    }
  }
  cs_mm();
  if (c == 0) {
#pragma unroll
    for (int t = 0; t < NWP; ++t) {
      const int col = SSM ? 32 * (t >> 1) + 8 * q + 4 * (t & 1) : 16 * t + 4 * q;
      sf4(LP(L_CS + ((h & 1) * 160 + (h >> 1) * 80 + col) * 4), acs[t]);
    }
  }
  sig(L_CSF + h * 4, 1);
}

DI void carries(const Par& p, const Ids& id, const int h, const int lane, const unsigned tag, WRing& wr) {
  const __amdgpu_buffer_rsrc_t rctl = mkrs(p.ws, WS_CTL_BYTES);
  const int c = lane & 15, q = lane >> 4;
  {
    const int ch = 16 * h + c;
    lwait<2>(L_CSF + 8, 0, 1);
    LAS float* cst = (LAS float*)LP(L_CST);
    const float l8r = cst[128 + ch], l8i = cst[192 + ch], l64r = cst[256 + ch], l64i = cst[320 + ch], lwr = cst[384 + ch], lwi = cst[448 + ch];
    const float kr = cst[512 + ch], ki = cst[576 + ch];
    float w0r = 1.f, w0i = 0.f, w1r = 1.f, w1i = 0.f, qr = kr, qi = ki;
#pragma unroll
    for (int s = 0; s < 6; ++s) {
      float a = w0r, b = w0i; cm1(a, b, lwr, lwi, 0.f, 0.f);
      if (s < id.R - 1 - q) { w0r = a; w0i = b; }
      if (s < id.R - 5 - q) { w1r = a; w1i = b; }
    }
#pragma unroll
    for (int s = 0; s < 3; ++s) { float a = qr, b = qi; cm1(a, b, l64r, l64i, 0.f, 0.f); if (s < q) { qr = a; qi = b; } }
    const __amdgpu_buffer_rsrc_t rag = mkrs(p.ws + WS_AGG + id.R * (8 * 640 * 16), id.R * 640 * 16);
    LAS float* pe = (LAS float*)LP(L_E + 8 * q * ESTR + ch * 4);
    HBAR();
    float er[8], ei[8];
#pragma unroll
    for (int s = 0; s < 8; ++s) { er[s] = pe[s * (ESTR / 4)]; ei[s] = pe[s * (ESTR / 4) + 64]; }
    float a = er[0], b = ei[0];
#pragma unroll
    for (int s = 1; s < 8; ++s) cm1(a, b, l8r, l8i, er[s], ei[s]);
    float ta[4], tb[4];
    gather4(a, ta); gather4(b, tb);
    float pa = 0.f, pb = 0.f;
    a = ta[0]; b = tb[0];
#pragma unroll
    for (int s = 1; s < 4; ++s) {
      if (q == s) { pa = a; pb = b; }
      cm1(a, b, l64r, l64i, ta[s], tb[s]);
    }
#pragma unroll
    for (int z = 0; z < 2; ++z)
      st16c(v4u{__float_as_uint(a), tag, __float_as_uint(b), tag}, rctl, q + 4 * z > id.R ? WS_AGG + (((q + 4 * z) * 8 + id.R) * 640 + 64 * id.g + ch) * 16 : OOB);
    SB0();
    wr.dma01();
    v4u p0 = ld16c(rag, (q * 640 + 64 * id.g + ch) * 16, 0), p1 = ld16c(rag, ((q + 4) * 640 + 64 * id.g + ch) * 16, 0);
    SB0();
    cm1(pa, pb, kr, ki, 0.f, 0.f);
#pragma unroll
    for (int s = 0; s < 7; ++s) cm1(er[s], ei[s], kr, ki, 0.f, 0.f);
    _Pragma("unroll 1") for (int spin = 0; spin < SPIN; ++spin) { FENCE();
      const bool ok = ((q >= id.R) | ((p0.y == tag) & (p0.w == tag))) & ((q + 4 >= id.R) | ((p1.y == tag) & (p1.w == tag)));
      if (__all(ok)) break;
      p0 = ld16c(rag, (q * 640 + 64 * id.g + ch) * 16, 0); p1 = ld16c(rag, ((q + 4) * 640 + 64 * id.g + ch) * 16, 0);
    }
    float ca = __uint_as_float(p1.x), cb = __uint_as_float(p1.z);
    cm1(ca, cb, w1r, w1i, 0.f, 0.f);
    { float x = __uint_as_float(p0.x), y = __uint_as_float(p0.z); cm1(x, y, w0r, w0i, ca, cb); ca = x; cb = y; }
    gather4(ca, ta); gather4(cb, tb);
    ca = (ta[0] + ta[1]) + (ta[2] + ta[3]); cb = (tb[0] + tb[1]) + (tb[2] + tb[3]);
    cm1(ca, cb, qr, qi, pa, pb);
#pragma unroll
    for (int s = 0; s < 8; ++s) {
      pe[s * (ESTR / 4)] = ca; pe[s * (ESTR / 4) + 64] = -cb;
      if (s < 7) cm1(ca, cb, l8r, l8i, er[s], ei[s]);
    }
    HBAR();
  }
}

__global__ __launch_bounds__(512, 2) void fused(Par p) {
  const int bid = blockIdx.x, xcd = bid & 7, idx = bid >> 3, a = xcd >> 2, b = xcd & 3, s = idx >> 2;
  Ids id;
  const bool ssm = idx < 10, part = idx >= 8 && idx < 12;
  id.R = 4 * a + (part ? 2 * (b & 1) + (idx & 1) : (idx & 3));
  id.g = ssm ? (part ? 8 + (b >> 1) : 2 * b + s) : (part ? 20 + (b >> 1) : 5 * b + s - 3);
  id.cg = ssm ? id.g : 10 + id.g;
  id.rt = 16 * a + 8 * (b >> 1) + (idx >> 2);
  id.ct = 4 * (b & 1) + (idx & 3);
  const int wv = rfl(threadIdx.x >> 6), lane = threadIdx.x & 63;
  unsigned cpf = 0;
  if (wv == 3) {
    const unsigned long long pc = __builtin_amdgcn_s_getpc() & ~127ull;
    cpf = __builtin_amdgcn_raw_buffer_load_b32(mkrs((const void*)pc, CODEPF), lane < 12 ? (idx * 12 + lane) * 128 : OOB, 0, 0);
  }
  const __amdgpu_buffer_rsrc_t rctl = mkrs(p.ws, WS_CTL_BYTES);
  const __amdgpu_buffer_rsrc_t rw = mkrs(p.ws + WS_W2 + (unsigned)id.ct * (NKB * 5120), NKB * 5120);
  const unsigned vl = lane * 16;
  const unsigned fo = WS_FLH + (id.rt * 8 + id.ct) * 128 + (lane & 31) * 4;
  WRing wr;
  wr.rw = rw; wr.vl = vl; wr.wv = wv;
  unsigned tag;
  if (wv < 4) {
    tag = ssm ? compute_wave<true>(p, id, wv, lane) : compute_wave<false>(p, id, wv, lane);
    WAITV(0);
    unsigned old = 0;
    if (lane == 0) old = __hip_atomic_fetch_add((LAS unsigned*)LP(L_CNT), 1u, __ATOMIC_RELAXED, __HIP_MEMORY_SCOPE_WORKGROUP);
    st4c(tag, rctl, (lane < 32 && rfl(old) == 3) ? WS_FLH + (id.R * 32 + lane) * 128 + id.cg * 4 : OOB);
    asm volatile("" ::"v"(cpf));
    STAMP(16);
#pragma unroll
    for (int it = NL; it < NL + DW; ++it) wr.load(it);
  } else {
    HState hs;
    tag = ssm ? helper_wave<true>(p, id, wv - 4, lane, hs) : helper_wave<false>(p, id, wv - 4, lane, hs);
    if (ssm) helper_tail<true>(p, id, wv - 4, lane, tag, hs);
    wr.load(NL); wr.load(NL + 1); wr.load(NL + 2);
    if (!ssm) helper_tail<false>(p, id, wv - 4, lane, tag, hs);
#pragma unroll
    for (int it = NL + 3; it < NL + DW; ++it) wr.load(it);
    if (ssm) carries(p, id, wv - 4, lane, tag, wr); else wr.dma01();
    lwait<2>(L_CNT, 0, 4);
    STAMP(16);
    if (wv > 4) {
      asm volatile("s_nop 4");
      dma_lin(MkSeq<14>::T{}, rw, (wv - 5) * 13312, vl, 81920 + (wv - 5) * 13312);
    } else {
      _Pragma("unroll 1") for (int spin = 0; spin < SPIN; ++spin) { FENCE();
        if (__all((ld4c(rctl, fo) == tag) | ((lane & 31) < 10))) break;
        __builtin_amdgcn_s_sleep(2);
      }
      sig(L_GO, 1);
    }
  }

  const __amdgpu_buffer_rsrc_t rh = mkrs(p.ws + WS_H + (unsigned)id.rt * (NKB * 4096), NKB * 4096);
  v4u hb[DH][4];
  auto loadh = [&](int it) {
#pragma unroll
    for (int j = 0; j < 4; ++j) { hb[it % DH][j] = ld16c(rh, vl + j * 1024, (8 * it + wv) * 4096); SB0(); }
  };
  lwait<1>(L_GO, 0, 1);
  SB0();
  STAMP(20);
#pragma unroll
  for (int it = 0; it < DH; ++it) { loadh(it); SB0(); }
  f32x4 acc[4][5];
#pragma unroll
  for (int j = 0; j < 4; ++j)
#pragma unroll
    for (int i = 0; i < 5; ++i) acc[j][i] = f32x4{0.f, 0.f, 0.f, 0.f};
  const int tid = wv * 64 + lane;
  v2u xv[3], dv[3]; v4u sv[3]; int oo[3], ra[3];
  unsigned sfl = 0;
  const lp pw = LP(L_W + wv * 5120 + vl), pw2 = LP(wv * 5120 + vl);
  SB0();
  if (wv > 4) {
    WAITV(4 * DH);
    if (lane == 0) __hip_atomic_fetch_add((LAS unsigned*)LP(L_RDY), 1u, __ATOMIC_RELAXED, __HIP_MEMORY_SCOPE_WORKGROUP);
  }
  lwait<1>(L_RDY, 0, 3);
  v4u wl = l128(pw);
  SB0();
#pragma unroll
  for (int it = 0; it < NIT; ++it) {
    if (it < NL) {
#pragma unroll
      for (int i = 0; i < 5; ++i) {
        const v4u wf = wl;
        if (5 * it + i + 1 < 5 * NL) wl = l128((5 * it + i + 1) / 5 == 2 ? pw2 + ((5 * it + i + 1) % 5) * 1024 : pw + ((5 * it + i + 1) / 5) * 40960 + ((5 * it + i + 1) % 5) * 1024);
#pragma unroll
        for (int j = 0; j < 4; ++j) acc[j][i] = mfma16(wf, hb[it % DH][j], acc[j][i]);
        SB0();
      }
    } else {
#pragma unroll
      for (int j = 0; j < 4; ++j)
#pragma unroll
        for (int i = 0; i < 5; ++i) acc[j][i] = mfma16(wr.wb[it % DW][i], hb[it % DH][j], acc[j][i]);
    }
    SB0();
    if (it + DH == 6) {
      if (__builtin_expect(!__all(sfl == tag), 0)) {
        _Pragma("unroll 1") for (int spin = 0; spin < SPIN; ++spin) { FENCE();
          if (__all(ld4c(rctl, fo) == tag)) break;
          __builtin_amdgcn_s_sleep(2);
        }
      }
      FENCE();
      STAMP(18); STAMPC(28);
    }
    if (it == 0) sfl = ld4c(rctl, fo);
    if (it + DH < NIT) loadh(it + DH);
    if (it >= NL && it + DW < NIT) wr.load(it + DW);
    if (it == NIT - DH) {
#pragma unroll
      for (int r = 0; r < 3; ++r) {
        const int sl = min(tid + 512 * r, 1279), row = sl / 20, qd = sl - 20 * row;
        const int tok = 128 * (id.rt >> 1) + 8 * (row >> 2) + 4 * (id.rt & 1) + (row & 3), d = 80 * id.ct + 4 * qd;
        oo[r] = tok * D_ + d;
        ra[r] = ((row & 3) * 5 + (qd >> 2)) * 1024 + (qd & 3) * 256 + (((row >> 2) + qd) & 15) * 16;
        xv[r] = *(const v2u*)(p.x + oo[r]); dv[r] = *(const v2u*)(p.dsk + d);
        sv[r] = ld16c(rctl, WS_STATS + tok * 16, 0);
      }
    }
#define BARIT 2
    if (it == BARIT) BAR();
    SB0();
  }
  STAMP(21); STAMPC(29);
#pragma unroll
  for (int i = 0; i < 5; ++i) {
    const lp rb = LP(wv * 20480 + (lane >> 4) * 256 + (((lane & 15) + 4 * i + (lane >> 4)) & 15) * 16);
#pragma unroll
    for (int j = 0; j < 4; ++j) sf4(rb + (j * 5 + i) * 1024, acc[j][i]);
  }
  BAR();
  STAMP(19);
#pragma unroll
  for (int r = 0; r < 3; ++r) {
    if (r < 2 || wv < 4) {
      const lp q0 = LP(ra[r]), q1 = LP(ra[r] + 4 * 20480);
      f32x4 sum = ((lf4(q0) + lf4(q0 + 20480)) + (lf4(q0 + 40960) + lf4(q0 + 61440))) + ((lf4(q1) + lf4(q1 + 20480)) + (lf4(q1 + 40960) + lf4(q1 + 61440)));
      const f32x4 xf = {bflo(xv[r].x), bfhi(xv[r].x), bflo(xv[r].y), bfhi(xv[r].y)};
      const f32x4 df = {bflo(dv[r].x), bfhi(dv[r].x), bflo(dv[r].y), bfhi(dv[r].y)};
      sum += xf + (xf - __uint_as_float(sv[r].x)) * __uint_as_float(sv[r].z) * df;
#define OUTAUX 16
      __builtin_amdgcn_raw_buffer_store_b128(__builtin_bit_cast(v4u, sum), mkrs(p.out, L_ * D_ * 4), oo[r] * 4, 0, OUTAUX);
    }
  }
  STAMP(22);
}

extern "C" void kernel_launch(void* const* d_in, const int* in_sizes, int n_in, void* d_out, int out_size, void* d_ws, size_t ws_size, hipStream_t stream) {
  Par p;
  p.x = (const u16*)d_in[0]; p.lre = (const u16*)d_in[1]; p.lim = (const u16*)d_in[2];
  p.bre = (const u16*)d_in[3]; p.bim = (const u16*)d_in[4]; p.cre = (const u16*)d_in[5]; p.cim = (const u16*)d_in[6];
  p.ldt = (const u16*)d_in[7]; p.dsk = (const u16*)d_in[8];
  p.wg = (const u16*)d_in[10]; p.wu = (const u16*)d_in[11]; p.wd = (const u16*)d_in[12];
  p.ws = (unsigned char*)d_ws; p.out = (float*)d_out;
  (void)hipFuncSetAttribute((const void*)fused, hipFuncAttributeMaxDynamicSharedMemorySize, LDS1);
  fused<<<256, 512, LDS1, stream>>>(p);
}
```

```cpp
#define WSOFF 1
#define PDH 2
#define PDW 5
#include <hip/hip_runtime.h>
#include <stdint.h>

typedef unsigned short u16;
typedef unsigned v4u __attribute__((ext_vector_type(4)));
typedef unsigned v2u __attribute__((ext_vector_type(2)));
typedef float f32x4 __attribute__((ext_vector_type(4)));
typedef float f32x2 __attribute__((ext_vector_type(2)));
typedef float f32x16 __attribute__((ext_vector_type(16)));
typedef short bf16x8 __attribute__((ext_vector_type(8)));
typedef short s16x4 __attribute__((ext_vector_type(4)));
typedef __bf16 bf2 __attribute__((ext_vector_type(2)));
#define LAS __attribute__((address_space(3)))
#define DI __device__ __forceinline__
typedef LAS unsigned char* lp;

constexpr int L_ = 2048, D_ = 640, P_ = 640, F_ = 1728;
constexpr int NKB = 94;
constexpr unsigned WS_EPOCH = 0;
constexpr unsigned WS_FLW = 2048;
constexpr unsigned WS_FLH = 4096;
constexpr unsigned WS_AGG = WS_FLH + 32768;
constexpr unsigned WS_STATS = WS_AGG + 64 * 640 * 16;
constexpr unsigned WS_CTL_BYTES = WS_STATS + 32768;
constexpr unsigned WS_W2 = 1u << 20;
constexpr unsigned WS_W2_BYTES = 8 * NKB * 5120;
constexpr unsigned WS_H = 8u << 20;
constexpr unsigned WS_H_BYTES = 32 * NKB * 4096;

constexpr int UPO = 10368;
constexpr int STG = 32768 + 20480 + 128;
constexpr int LDS1 = 163840;
constexpr int L_STATS = 3 * STG;
constexpr int L_CS = L_STATS + 2048;
constexpr int L_CNT = L_CS + 1280, L_GO = L_CNT + 4, L_RDY = L_CNT + 8, L_CSF = L_CNT + 16;
constexpr int L_CST = 49152;
constexpr int L_W = STG;
constexpr int L_E = 136000, ESTR = 528;
constexpr int SPIN = 1 << 18;
constexpr unsigned OOB = 0x40000000u;

DI float bf2f(unsigned b) { return __uint_as_float(b << 16); }
DI float bflo(unsigned w) { return __uint_as_float(w << 16); }
DI float bfhi(unsigned w) { return __uint_as_float(w & 0xffff0000u); }
DI unsigned pk2(float a, float b) { f32x2 v = {a, b}; return __builtin_bit_cast(unsigned, __builtin_convertvector(v, bf2)); }
DI float rbf(float a) { return bflo(pk2(a, a) & 0xffffu); }
DI f32x4 mfma16(v4u a, v4u b, f32x4 c) { return __builtin_amdgcn_mfma_f32_16x16x32_bf16(__builtin_bit_cast(bf16x8, a), __builtin_bit_cast(bf16x8, b), c, 0, 0, 0); }
DI f32x16 mfma32(v4u a, v4u b, f32x16 c) { return __builtin_amdgcn_mfma_f32_32x32x16_bf16(__builtin_bit_cast(bf16x8, a), __builtin_bit_cast(bf16x8, b), c, 0, 0, 0); }
DI unsigned rfl(unsigned v) { return __builtin_amdgcn_readfirstlane(v); }
DI __amdgpu_buffer_rsrc_t mkrs(const void* p, unsigned bytes) { return __builtin_amdgcn_make_buffer_rsrc((void*)p, 0, bytes, 0x00020000); }
template <int LI, int SI> DI void glds(__amdgpu_buffer_rsrc_t rs, unsigned lb, unsigned voff, unsigned sb) {
  unsigned t;
  asm volatile("s_add_u32 m0, %1, %5\n\ts_add_u32 %0, %4, %6\n\tbuffer_load_dwordx4 %2, %3, %0 offen lds" : "=&s"(t) : "s"(lb), "v"(voff), "s"(rs), "s"(sb), "i"(LI), "i"(SI) : "scc");
}
template <int LI, int SI> DI void gldsc(__amdgpu_buffer_rsrc_t rs, unsigned lb, unsigned voff, unsigned sb) {
  unsigned t;
  asm volatile("s_add_u32 m0, %1, %5\n\ts_add_u32 %0, %4, %6\n\tbuffer_load_dwordx4 %2, %3, %0 offen sc1 lds" : "=&s"(t) : "s"(lb), "v"(voff), "s"(rs), "s"(sb), "i"(LI), "i"(SI) : "scc");
}
template <int... I> struct Seq {};
template <int N, int... I> struct MkSeq : MkSeq<N - 1, N - 1, I...> {};
template <int... I> struct MkSeq<0, I...> { typedef Seq<I...> T; };
template <int... I> DI void dma_lin(Seq<I...>, __amdgpu_buffer_rsrc_t rs, unsigned lb, unsigned vl, unsigned sb) { (gldsc<I * 1024, I * 1024>(rs, lb, vl, sb), ...); }
template <int... I> DI void dma_x(Seq<I...>, __amdgpu_buffer_rsrc_t rs, unsigned lb, unsigned v0, unsigned v1, unsigned sb) { (glds<I * 1024, (64 * (I & 1) + (I >> 1)) * 1280>(rs, lb, (I & 1) ? v1 : v0, sb), ...); }
template <int... I> DI void dma_b(Seq<I...>, __amdgpu_buffer_rsrc_t rs, unsigned lb, const unsigned* v, unsigned sb) { (glds<I * 1024, (16 * (I & 1) + 4 * (I >> 1)) * 1280>(rs, lb, v[I & 1], sb), ...); }
template <int... I> DI void dma_g(Seq<I...>, __amdgpu_buffer_rsrc_t rs, unsigned lb, const unsigned* v, unsigned sb) { (glds<I * 1024, 0>(rs, lb, v[I], sb), ...); }
DI lp LP(unsigned a) { return (lp)(uintptr_t)a; }
DI v4u l128(lp a) { return *(LAS v4u*)a; }
DI f32x4 lf4(lp a) { return *(LAS f32x4*)a; }
DI void sf4(lp a, f32x4 v) { *(LAS f32x4*)a = v; }
DI s16x4 ltr(lp a) { return __builtin_amdgcn_ds_read_tr16_b64_v4i16((LAS s16x4*)a); }
DI v4u cat(s16x4 lo, s16x4 hi) { return __builtin_bit_cast(v4u, __builtin_shufflevector(lo, hi, 0, 1, 2, 3, 4, 5, 6, 7)); }
DI v4u ld16(__amdgpu_buffer_rsrc_t r, unsigned vo, unsigned so) { return __builtin_amdgcn_raw_buffer_load_b128(r, vo, so, 0); }
DI v4u ld16c(__amdgpu_buffer_rsrc_t r, unsigned vo, unsigned so) { return __builtin_amdgcn_raw_buffer_load_b128(r, vo, so, 16); }
DI unsigned ld4c(__amdgpu_buffer_rsrc_t r, unsigned vo) { return __builtin_amdgcn_raw_buffer_load_b32(r, vo, 0, 16); }
DI void st16c(v4u v, __amdgpu_buffer_rsrc_t r, unsigned vo) { __builtin_amdgcn_raw_buffer_store_b128(v, r, vo, 0, 16); }
DI void st4c(unsigned v, __amdgpu_buffer_rsrc_t r, unsigned vo) { __builtin_amdgcn_raw_buffer_store_b32(v, r, vo, 0, 16); }
#define BAR() asm volatile("s_waitcnt lgkmcnt(0)\n\ts_barrier" ::: "memory")
#define SB0() __builtin_amdgcn_sched_barrier(0)
#define WAITV(n) asm volatile("s_waitcnt vmcnt(%0)" ::"i"(n) : "memory")
#define FENCE() asm volatile("" ::: "memory")
template <int C> DI float dppf(float v) { return __int_as_float(__builtin_amdgcn_update_dpp(0, __float_as_int(v), C, 0xf, 0xf, true)); }
template <int N> DI f32x4 shr4(f32x4 v) { return f32x4{dppf<0x110 + N>(v.x), dppf<0x110 + N>(v.y), dppf<0x110 + N>(v.z), dppf<0x110 + N>(v.w)}; }
DI float sum8(float v) {
  v += dppf<0xB1>(v); v += dppf<0x4E>(v); v += dppf<0x141>(v);
  return v;
}
DI float dot2(unsigned a, unsigned b, float c) { return __builtin_amdgcn_fdot2_f32_bf16(__builtin_bit_cast(bf2, a), __builtin_bit_cast(bf2, b), c, false); }
DI void gather4(float x, float (&t)[4]) {
  const auto a = __builtin_amdgcn_permlane16_swap(__float_as_uint(x), __float_as_uint(x), false, false);
  const auto e = __builtin_amdgcn_permlane32_swap(a[0], a[0], false, false), o = __builtin_amdgcn_permlane32_swap(a[1], a[1], false, false);
  t[0] = __uint_as_float(e[0]); t[2] = __uint_as_float(e[1]); t[1] = __uint_as_float(o[0]); t[3] = __uint_as_float(o[1]);
}
DI void cm1(float& a, float& b, float lr, float li, float ur, float ui) {
  const float na = fmaf(lr, a, fmaf(-li, b, ur)), nb = fmaf(lr, b, fmaf(li, a, ui));
  a = na; b = nb;
}
DI f32x4 fma4(f32x4 a, f32x4 b, f32x4 c) { return __builtin_elementwise_fma(a, b, c); }
DI f32x4 sp4(float a) { return f32x4{a, a, a, a}; }
DI void cmac(f32x4& a, f32x4& b, f32x4 lr, f32x4 li, f32x4 ur, f32x4 ui) {
  const f32x4 na = fma4(lr, a, fma4(-li, b, ur)), nb = fma4(lr, b, fma4(li, a, ui));
  a = na; b = nb;
}

#define STAMP(i)
#define STAMPC(i)

#define LSTAMP(i)
struct Par {
  const u16 *x, *lre, *lim, *bre, *bim, *cre, *cim, *ldt, *dsk, *wg, *wu, *wd;
  unsigned char* ws;
  float* out;
};
struct Ids { int R, g, cg, rt, ct; };
#define CODEPF 46080

constexpr int DW = PDW, DH = PDH, NIT = 12, NL = 3;
#define PIN4(x) asm volatile("" : "+v"(x))
DI void sig(unsigned addr, unsigned v) {
  asm volatile("s_mov_b64 exec, 1\n\tds_write_b32 %0, %1\n\ts_mov_b64 exec, -1" ::"v"(addr), "v"(v) : "memory");
}
DI unsigned lrd(unsigned addr) { return *(volatile LAS unsigned*)LP(addr); }
#define LSLEEP 1
template <int SL = 1> DI void lwait(unsigned addr, unsigned v, unsigned need) {
  if (__builtin_expect(!__all(v >= need), 0)) {
    _Pragma("unroll 1") for (int spin = 0; spin < 64 * SPIN; ++spin) { FENCE();
      if (__all(lrd(addr) >= need)) break;
      __builtin_amdgcn_s_sleep(SL);
    }
  }
  FENCE();
}

struct WRing {
  v4u wb[DW][5];
  __amdgpu_buffer_rsrc_t rw;
  unsigned vl, wv;
  DI void load(int it) {
#pragma unroll
    for (int i = 0; i < 5; ++i) { wb[it % DW][i] = ld16c(rw, vl + (i & 3) * 1024, (8 * it + wv) * 5120 + (i >> 2) * 4096); SB0(); }
  }
  DI void dma01() {
    asm volatile("s_nop 4");
    dma_lin(MkSeq<20>::T{}, rw, L_W + (wv - 4) * 20480, vl, (wv - 4) * 20480);
  }
};

DI float wsum(float v) {
  v = sum8(v); v += dppf<0x128>(v);
  float t[4]; gather4(v, t);
  return (t[0] + t[1]) + (t[2] + t[3]);
}
DI unsigned mktag(unsigned epoch) { return rfl(epoch) * 0x9E3779B1u + 0x7F4A7C15u; }
struct RowIn { v4u xa[2], f[5]; unsigned xb[2], epoch; };
template <bool SSM> DI void row_load(const Par& p, const Ids& id, const int w, const int lane, RowIn& in) {
  const int row = 256 * id.R + 8 * id.cg + 2 * w;
#pragma unroll
  for (int z = 0; z < 2; ++z) { in.xa[z] = *(const v4u*)(p.x + (row + z) * D_ + lane * 8); in.xb[z] = *(const unsigned*)(p.x + (row + z) * D_ + 512 + lane * 2); }
  in.epoch = ld4c(mkrs(p.ws, WS_CTL_BYTES), WS_EPOCH);
  if constexpr (SSM) {
    const int kb = 4 * id.g + w;
    const __amdgpu_buffer_rsrc_t r = mkrs(kb < 20 ? p.cre : p.cim, D_ * P_ * 2);
#pragma unroll
    for (int i = 0; i < 5; ++i) in.f[i] = ld16(r, (80 * id.R + (lane >> 2)) * 1280 + (kb < 20 ? kb : kb - 20) * 64 + (lane & 3) * 16, i * (16 * 1280));
  }
  SB0();
}
DI unsigned row_stats(const Par& p, const Ids& id, const int w, const int lane, const RowIn& in) {
  const __amdgpu_buffer_rsrc_t rctl = mkrs(p.ws, WS_CTL_BYTES);
  const int row = 256 * id.R + 8 * id.cg + 2 * w;
  const v4u* xa = in.xa; const unsigned* xb = in.xb; const unsigned epoch = in.epoch;
  float v[2][10], mean[2], s2[2];
#pragma unroll
  for (int z = 0; z < 2; ++z) {
    const float t[10] = {bflo(xa[z].x), bfhi(xa[z].x), bflo(xa[z].y), bfhi(xa[z].y), bflo(xa[z].z), bfhi(xa[z].z), bflo(xa[z].w), bfhi(xa[z].w), bflo(xb[z]), bfhi(xb[z])};
#pragma unroll
    for (int e = 0; e < 10; ++e) v[z][e] = t[e];
    mean[z] = wsum(((t[0] + t[1]) + (t[2] + t[3])) + ((t[4] + t[5]) + (t[6] + t[7])) + (t[8] + t[9])) * (1.f / D_);
  }
#pragma unroll
  for (int z = 0; z < 2; ++z) {
    float a = 0.f, b = 0.f;
#pragma unroll
    for (int e = 0; e < 5; ++e) { a = fmaf(v[z][e] - mean[z], v[z][e] - mean[z], a); b = fmaf(v[z][5 + e] - mean[z], v[z][5 + e] - mean[z], b); }
    s2[z] = wsum(a + b);
  }
  const unsigned tag = mktag(epoch);
  const float m = rbf((lane & 1) ? mean[1] : mean[0]), rs = 1.f / rbf(sqrtf(rbf(rbf(((lane & 1) ? s2[1] : s2[0]) * (1.f / D_)) + 1e-5f)));
  st16c(v4u{__float_as_uint(m), tag, __float_as_uint(rs), tag}, rctl, lane < 2 ? WS_STATS + (row + lane) * 16 : OOB);
  return tag;
}

template <bool SSM>
DI unsigned compute_wave(const Par& p, const Ids& id, const int w, const int lane) {
  STAMP(0);
  unsigned tag, cf = 0;
  RowIn rin;
  row_load<SSM>(p, id, w, lane, rin);
  if constexpr (!SSM) {
    const int r = lane & 31, hh = lane >> 5;
    const unsigned xa = (64 * w + r) * 128 + ((hh ^ ((r >> 1) & 7)) * 16);
    const unsigned wa = 32768 + ((lane >> 4) & 1) * UPO + (8 * hh + ((lane & 15) >> 2)) * 160 + (lane & 3) * 8;
    f32x16 acc[5][2] = {};
#pragma unroll
    for (int e = 0; e < 5; ++e) { PIN4(acc[e][0]); PIN4(acc[e][1]); }
    v4u fx[2][2]; s16x4 fl[4], fh[4];
    lp px[4], pw;
    auto setp = [&](unsigned sb) {
#pragma unroll
      for (int s = 0; s < 4; ++s) px[s] = LP(sb + (xa ^ (32 * s)));
      pw = LP(sb + wa);
    };
    auto woff = [&](int n) { return (n / 5) * 2560 + (n % 5) * 32; };
    setp(0);
    SB0();
    tag = row_stats(p, id, w, lane, rin);
    STAMP(17);
    SB0();
    BAR();
    STAMP(1);
    fx[0][0] = l128(px[0]); fx[0][1] = l128(px[0] + 4096);
#pragma unroll
    for (int n = 0; n < 3; ++n) { fl[n] = ltr(pw + woff(n)); fh[n] = ltr(pw + woff(n) + 640); }
    unsigned sb = 0;
#pragma unroll 1
    for (int i = 0; i < 10; ++i) {
#pragma unroll
      for (int n = 0; n < 20; ++n) {
        const int ks = n / 5, e = n % 5, nn = (n + 3) % 20;
        if (n == 17) { if (i < 9) BAR(); LSTAMP(i); sb = sb == 2 * STG ? 0 : sb + STG; setp(sb); SB0(); }
        const v4u wf = cat(fl[n & 3], fh[n & 3]);
        acc[e][0] = mfma32(wf, fx[ks & 1][0], acc[e][0]); SB0();
        fl[(n + 3) & 3] = ltr(pw + woff(nn));
        fh[(n + 3) & 3] = ltr(pw + woff(nn) + 640); SB0();
        acc[e][1] = mfma32(wf, fx[ks & 1][1], acc[e][1]); SB0();
        if (ks < 3 && e == 1) { fx[(ks + 1) & 1][0] = l128(px[ks + 1]); fx[(ks + 1) & 1][1] = l128(px[ks + 1] + 4096); }
        if (n == 17) { fx[0][0] = l128(px[0]); fx[0][1] = l128(px[0] + 4096); }
        if (n == 18) cf = lrd(L_CSF + (lane & 3) * 4);
        SB0();
      }
    }
    lwait(L_CSF + (lane & 3) * 4, cf, 1);
    STAMP(12);
    const __amdgpu_buffer_rsrc_t rh = mkrs(p.ws + WS_H + (unsigned)(4 * id.R + w) * (NKB * 4096), 54 * 4096);
    const unsigned so0 = (r >> 4) * 1024 + (hh * 16 + (r & 15)) * 16;
    float mu[2], r2[2];
#pragma unroll
    for (int u = 0; u < 2; ++u) {
      const int F = 4 * w + 2 * u + (r >> 4);
      const f32x2 s = *(LAS f32x2*)LP(L_STATS + (128 * (F >> 3) + 8 * (r & 15) + (F & 7)) * 8);
      mu[u] = s.x; r2[u] = s.y * s.y;
    }
    const lp pc = LP(L_CS + hh * 16);
#pragma unroll
    for (int e = 0; e < 5; ++e) {
      const f32x4 g0 = lf4(pc + e * 64) + lf4(pc + e * 64 + 640), g1 = lf4(pc + e * 64 + 32) + lf4(pc + e * 64 + 672);
      const f32x4 u0 = lf4(pc + e * 64 + 320) + lf4(pc + e * 64 + 960), u1 = lf4(pc + e * 64 + 352) + lf4(pc + e * 64 + 992);
      const int T = 5 * id.g + e;
      const unsigned so = so0 + (T >> 1) * 4096 + (T & 1) * 512;
#pragma unroll
      for (int u = 0; u < 2; ++u) {
        const f32x16 a = acc[e][u];
        const f32x4 nm = sp4(-mu[u]);
        const f32x4 h0 = fma4(nm, g0, f32x4{a[0], a[1], a[2], a[3]}) * fma4(nm, u0, f32x4{a[8], a[9], a[10], a[11]}) * r2[u];
        const f32x4 h1 = fma4(nm, g1, f32x4{a[4], a[5], a[6], a[7]}) * fma4(nm, u1, f32x4{a[12], a[13], a[14], a[15]}) * r2[u];
        st16c(v4u{pk2(h0.x, h0.y), pk2(h0.z, h0.w), pk2(h1.x, h1.y), pk2(h1.z, h1.w)}, rh, so + u * 2048);
      }
    }
  } else {
    const int c = lane & 15, q = lane >> 4, G = w >> 1, blk = w & 1;
    const unsigned xa = (128 * G + c) * 128 + ((q ^ (c >> 1)) * 16);
    const unsigned wa = 32768 + (32 * blk + c) * 128 + ((q ^ (c >> 1)) * 16);
    f32x4 acc[8][4] = {};
#pragma unroll
    for (int j = 0; j < 8; ++j)
#pragma unroll
      for (int t = 0; t < 4; ++t) PIN4(acc[j][t]);
    v4u fw[2][4], fx[4];
    lp px[2], pw[2];
    auto setp = [&](unsigned sb) { px[0] = LP(sb + xa); px[1] = LP(sb + (xa ^ 64)); pw[0] = LP(sb + wa); pw[1] = LP(sb + (wa ^ 64)); };
    auto woff = [&](int t) { return (t >> 1) * 8192 + (t & 1) * 2048; };
    setp(0);
    SB0();
    tag = row_stats(p, id, w, lane, rin);
    {
      const __amdgpu_buffer_rsrc_t r2 = mkrs(p.ws + WS_W2 + (unsigned)(id.R * NKB + 54 + 4 * id.g + w) * 5120, 5120);
#pragma unroll
      for (int i = 0; i < 5; ++i) {
        v4u o;
#pragma unroll
        for (int e = 0; e < 4; ++e) o[e] = __builtin_amdgcn_ds_bpermute((4 * c + q) * 4, rin.f[i][e]);
        st16c(o, r2, i * 1024 + lane * 16);
      }
    }
    STAMP(17);
    SB0();
    BAR();
    STAMP(1);
#pragma unroll
    for (int t = 0; t < 4; ++t) fw[0][t] = l128(pw[0] + woff(t));
#pragma unroll
    for (int n = 0; n < 3; ++n) fx[n] = l128(px[0] + n * 2048);
    unsigned sb = 0;
#pragma unroll 1
    for (int i = 0; i < 10; ++i) {
#pragma unroll
      for (int n = 0; n < 16; ++n) {
        const int kk = n >> 3, j = n & 7, nn = (n + 3) & 15;
        if (n == 13) { if (i == 2) WAITV(0); if (i < 9) BAR(); LSTAMP(i); sb = sb == 2 * STG ? 0 : sb + STG; setp(sb); SB0(); }
        const v4u xf = fx[n & 3];
        acc[j][0] = mfma16(fw[kk][0], xf, acc[j][0]); SB0();
        fx[(n + 3) & 3] = l128(px[nn >> 3] + (nn & 7) * 2048); SB0();
        acc[j][1] = mfma16(fw[kk][1], xf, acc[j][1]); SB0();
        if (n >= 2 && n < 6) fw[1][n - 2] = l128(pw[1] + woff(n - 2));
        if (n == 13) fw[0][0] = l128(pw[0] + woff(0));
        if (n == 14) fw[0][2] = l128(pw[0] + woff(2));
        SB0();
        acc[j][2] = mfma16(fw[kk][2], xf, acc[j][2]); SB0();
        if (n == 13) fw[0][1] = l128(pw[0] + woff(1));
        if (n == 14) fw[0][3] = l128(pw[0] + woff(3));
        SB0();
        acc[j][3] = mfma16(fw[kk][3], xf, acc[j][3]); SB0();
        if (n == 14) { cf = lrd(L_CSF + (lane & 3) * 4); SB0(); }
      }
    }
    lwait(L_CSF + (lane & 3) * 4, cf, 1);
    STAMP(12);
    const lp pe = LP(L_E + (16 * G + c) * ESTR + (32 * blk + 8 * q) * 4);
    const lp pk = LP(L_CST + (32 * blk + 8 * q) * 4);
    f32x4 lr[2], li[2], er[2], ei[2];
    {
      const lp ps = LP(L_STATS + (128 * G + 8 * c) * 8), pc = LP(L_CS + (32 * blk + 8 * q) * 4);
      f32x4 cs[4], st[4];
#pragma unroll
      for (int t = 0; t < 4; ++t) { cs[t] = lf4(pc + (t >> 1) * 320 + (t & 1) * 16) + lf4(pc + 640 + (t >> 1) * 320 + (t & 1) * 16); st[t] = lf4(ps + t * 16); }
#pragma unroll
      for (int ab = 0; ab < 2; ++ab) { lr[ab] = lf4(pk + ab * 16); li[ab] = lf4(pk + 256 + ab * 16); }
#pragma unroll
      for (int jj = 0; jj < 4; ++jj)
#pragma unroll
        for (int t = 0; t < 4; ++t) {
          acc[2 * jj][t] = fma4(sp4(-st[jj].x), cs[t], acc[2 * jj][t]) * st[jj].y;
          acc[2 * jj + 1][t] = fma4(sp4(-st[jj].z), cs[t], acc[2 * jj + 1][t]) * st[jj].w;
        }
    }
#pragma unroll
    for (int ab = 0; ab < 2; ++ab) { er[ab] = acc[0][ab]; ei[ab] = acc[0][2 + ab]; }
#pragma unroll
    for (int j = 1; j < 8; ++j)
#pragma unroll
      for (int ab = 0; ab < 2; ++ab) cmac(er[ab], ei[ab], lr[ab], li[ab], acc[j][ab], acc[j][2 + ab]);
#pragma unroll
    for (int ab = 0; ab < 2; ++ab) { sf4(pe + ab * 16, er[ab]); sf4(pe + 256 + ab * 16, ei[ab]); }
    SB0();
    BAR();
    STAMP(13);
#pragma unroll
    for (int ab = 0; ab < 2; ++ab) {
      const f32x4 kr = lf4(pk + 512 * 4 + ab * 16), ki = lf4(pk + 512 * 4 + 256 + ab * 16);
#pragma unroll
      for (int j = 0; j < 8; ++j) {
        const f32x4 ur = acc[j][ab], ui = acc[j][2 + ab];
        acc[j][ab] = fma4(-ki, ui, kr * ur); acc[j][2 + ab] = fma4(-ki, ur, -kr * ui);
        PIN4(acc[j][ab]); PIN4(acc[j][2 + ab]);
      }
      li[ab] = -li[ab];
    }
    const __amdgpu_buffer_rsrc_t rh = mkrs(p.ws + WS_H + (unsigned)(4 * id.R + 2 * G) * (NKB * 4096), 2 * NKB * 4096);
    unsigned vo[4];
#pragma unroll
    for (int z = 0; z < 4; ++z) { vo[z] = (54 + 2 * id.g + blk + 20 * (z & 1) + NKB * (z >> 1)) * 4096 + lane * 16; PIN4(vo[z]); }
    SB0();
    BAR();
    STAMP(14);
#pragma unroll
    for (int ab = 0; ab < 2; ++ab) { er[ab] = lf4(pe + ab * 16); ei[ab] = lf4(pe + 256 + ab * 16); }
#pragma unroll
    for (int j = 0; j < 8; ++j) {
#pragma unroll
      for (int ab = 0; ab < 2; ++ab) cmac(er[ab], ei[ab], lr[ab], li[ab], acc[j][ab], acc[j][2 + ab]);
      st16c(v4u{pk2(er[0].x, er[0].y), pk2(er[0].z, er[0].w), pk2(er[1].x, er[1].y), pk2(er[1].z, er[1].w)}, rh, vo[2 * (j >> 2)] + (j & 3) * 1024);
      st16c(v4u{pk2(ei[0].x, ei[0].y), pk2(ei[0].z, ei[0].w), pk2(ei[1].x, ei[1].y), pk2(ei[1].z, ei[1].w)}, rh, vo[2 * (j >> 2) + 1] + (j & 3) * 1024);
    }
  }
  SB0();
  STAMP(15);
  return tag;
}

#define HBAR() do { SB0(); BAR(); SB0(); } while (0)
struct HState { f32x4 acs[5]; unsigned pl, pr, pi; };
template <bool SSM>
DI unsigned helper_wave(const Par& p, const Ids& id, const int h, const int lane, HState& hs) {
  STAMP(0);
  constexpr int NWP = SSM ? 4 : 5, NP = 8 + NWP;
  constexpr unsigned WSTEP = SSM ? 128u : 64u * 3456u;
  const int c = lane & 15, q = lane >> 4;
  const __amdgpu_buffer_rsrc_t rctl = mkrs(p.ws, WS_CTL_BYTES);
  const __amdgpu_buffer_rsrc_t rx = mkrs(p.x, L_ * D_ * 2);
  const __amdgpu_buffer_rsrc_t rw = mkrs(SSM ? ((h >> 1) ? p.bim : p.bre) : ((h >> 1) ? p.wu : p.wg), SSM ? P_ * D_ * 2 : D_ * F_ * 2);
  const unsigned v0 = (8 * (lane >> 3)) * 1280 + (((lane & 7) ^ (lane >> 4)) * 16), v1 = v0 ^ 64;
#define ROT 0
  unsigned st = ROT == 0 ? 0 : ROT == 1 ? 5 * ((blockIdx.x >> 2) & 1) : ROT == 2 ? (5 * (blockIdx.x & 7)) >> 2 : 5 * (((blockIdx.x >> 2) ^ blockIdx.x) & 1);
  const unsigned sx = (256 * id.R + 128 * (h >> 1) + 4 * (h & 1)) * 1280;
  auto issue_x = [&](unsigned s, unsigned sb) {
    dma_x(MkSeq<8>::T{}, rx, sb + h * 8192, v0, v1, s);
  };
  asm volatile("s_nop 4");
  issue_x(sx + st * 128, 0);
  unsigned vw[NWP];
  if constexpr (SSM) {
    vw[0] = (8 * (lane >> 5) + ((lane >> 3) & 3)) * 1280 + (((lane & 7) ^ (lane >> 4)) * 16); vw[1] = vw[0] ^ 64;
  } else {
#pragma unroll
    for (int i = 0; i < 5; ++i) { const int lc = 64 * (5 * (h & 1) + i) + lane, pos = lc / 10; vw[i] = pos * 3456 + (lc - pos * 10) * 16; }
  }
  const unsigned sw = SSM ? (64 * id.g + 32 * (h & 1)) * 1280 : 160 * id.g;
  const unsigned dw = 32768 + (h >> 1) * (SSM ? 8192 : UPO) + (h & 1) * NWP * 1024;
  auto issue_w = [&](unsigned s, unsigned sb) {
    if constexpr (SSM) dma_b(MkSeq<4>::T{}, rw, sb + dw, vw, s); else dma_g(MkSeq<5>::T{}, rw, sb + dw, vw, s);
  };
  issue_w(sw + st * WSTEP, 0);
  unsigned sb = STG;
  auto step = [&]() {
    st = st == 9 ? 0 : st + 1;
    issue_x(sx + st * 128, sb); issue_w(sw + st * WSTEP, sb);
    sb = sb == 2 * STG ? 0 : sb + STG;
  };
  step();
  if (h == 3 && lane < 8) *(LAS unsigned*)LP(L_CNT + lane * 4) = 0u;
  f32x4 acs[NWP] = {};
  v4u fc[NWP];
  const unsigned ca = SSM ? 32768 + (h >> 1) * 8192 + c * 128 + (((4 * (h & 1) + q) ^ (c >> 1)) * 16)
                          : 32768 + (h >> 1) * UPO + (32 * (h & 1) + 4 * q + (c >> 2)) * 160 + (c & 3) * 8;
  const v4u ones = {0x3F803F80u, 0x3F803F80u, 0x3F803F80u, 0x3F803F80u};
  auto cs_rd = [&](unsigned s) {
    const lp a = LP(s + ca);
#pragma unroll
    for (int t = 0; t < NWP; ++t) { if constexpr (SSM) fc[t] = l128(a + t * 2048); else fc[t] = cat(ltr(a + t * 32), ltr(a + t * 32 + 2560)); }
    SB0();
  };
  auto cs_mm = [&]() {
    SB0();
    __builtin_amdgcn_s_setprio(2);
#pragma unroll
    for (int t = 0; t < NWP; ++t) acs[t] = mfma16(fc[t], ones, acs[t]);
    __builtin_amdgcn_s_setprio(0);
    SB0();
  };
  const int tsk = (id.g * 8 + id.R) * 4 + h, kb = tsk / 10, dq = tsk - 10 * kb;
  v4u f[4];
  auto rt_load = [&]() {
    const __amdgpu_buffer_rsrc_t r = mkrs(p.wd, F_ * D_ * 2);
    const int cc = lane >> 2, qq = lane & 3;
    const unsigned vo = (32 * kb + 16 * (cc >> 3) + 4 * ((cc >> 2) & 1) + (cc & 3)) * 1280 + dq * 128 + qq * 16;
    asm volatile("buffer_load_dwordx4 %0, %4, %5, 0 offen\n\tbuffer_load_dwordx4 %2, %4, %5, 0 offen offset:64\n\tbuffer_load_dwordx4 %1, %4, %5, %6 offen\n\tbuffer_load_dwordx4 %3, %4, %5, %6 offen offset:64"
                 : "=&v"(f[0]), "=&v"(f[1]), "=&v"(f[2]), "=&v"(f[3]) : "v"(vo), "s"(r), "s"(8 * 1280));
  };
  auto rt_store = [&]() {
#pragma unroll
    for (int i = 0; i < 4; ++i)
#pragma unroll
      for (int e = 0; e < 4; ++e) f[i][e] = __builtin_amdgcn_ds_bpermute((4 * c + q) * 4, f[i][e]);
    v4u sel[2];
#pragma unroll
    for (int hf = 0; hf < 2; ++hf)
#pragma unroll
      for (int e = 0; e < 4; ++e) sel[hf][e] = (8 * q + 2 * e == 16 * hf + c ? 0x3F80u : 0u) | (8 * q + 2 * e + 1 == 16 * hf + c ? 0x3F800000u : 0u);
    const __amdgpu_buffer_rsrc_t r2 = mkrs(p.ws + WS_W2, tsk < 540 ? WS_W2_BYTES : 0);
    __builtin_amdgcn_s_setprio(2);
#pragma unroll
    for (int i = 0; i < 4; ++i) {
      const f32x4 a = mfma16(f[i & ~1], sel[i & 1], f32x4{0.f, 0.f, 0.f, 0.f}), b = mfma16(f[i | 1], sel[i & 1], f32x4{0.f, 0.f, 0.f, 0.f});
      const int dt = 4 * dq + i, ct = dt / 5;
      st16c(v4u{pk2(a.x, a.y), pk2(a.z, a.w), pk2(b.x, b.y), pk2(b.z, b.w)}, r2, ((ct * NKB + kb) * 5 + dt - 5 * ct) * 1024 + lane * 16);
    }
    __builtin_amdgcn_s_setprio(0);
    SB0();
  };
  unsigned epoch, pl = 0, pr = 0, pi = 0;
  asm volatile("buffer_load_dword %0, off, %1, 0 sc1" : "=v"(epoch) : "s"(rctl));
  if constexpr (SSM) {
    const unsigned po = (64 * id.g + lane) * 2;
    asm volatile("buffer_load_ushort %0, %3, %4, 0 offen\n\tbuffer_load_ushort %1, %3, %5, 0 offen\n\tbuffer_load_ushort %2, %3, %6, 0 offen"
                 : "=&v"(pl), "=&v"(pr), "=&v"(pi) : "v"(po), "s"(mkrs(p.ldt, P_ * 2)), "s"(mkrs(p.lre, P_ * 2)), "s"(mkrs(p.lim, P_ * 2)));
  }
  WAITV(NP + (SSM ? 4 : 1)); BAR();
  cs_rd(0);
  step();
  if constexpr (!SSM) rt_load();
  cs_mm();
  asm volatile("s_waitcnt vmcnt(%4)" : "+v"(epoch), "+v"(pl), "+v"(pr), "+v"(pi) : "i"(NP + (SSM ? 0 : 4)) : "memory");
  BAR();
  cs_rd(STG); step(); cs_mm();
  if constexpr (SSM) WAITV(NP);
  else asm volatile("s_waitcnt vmcnt(%4)" : "+v"(f[0]), "+v"(f[1]), "+v"(f[2]), "+v"(f[3]) : "i"(NP) : "memory");
  BAR();
  cs_rd(2 * STG);
  step();
  const unsigned tag = mktag(epoch);
  cs_mm();
  if constexpr (!SSM) rt_store();
  WAITV(NP + (SSM ? 0 : 4)); BAR();
  cs_rd(0);
  step(); cs_mm();
  WAITV(NP); BAR();
  cs_rd(STG);
  st4c(tag, rctl, (h == 3 && lane == 0) ? WS_FLW + blockIdx.x * 4 : OOB);
  SB0();
  step(); cs_mm();
  WAITV(NP + 1); BAR();
  cs_rd(2 * STG);
  v4u sv;
  asm volatile("buffer_load_dwordx4 %0, %1, %2, 0 offen sc1" : "=v"(sv) : "v"(WS_STATS + (256 * id.R + 64 * h + lane) * 16), "s"(rctl));
  step(); cs_mm();
  asm volatile("s_waitcnt vmcnt(%1)" : "+v"(sv) : "i"(NP) : "memory");
  if (!__all((sv.y == tag) & (sv.w == tag))) {
    _Pragma("unroll 1") for (int spin = 0; spin < SPIN; ++spin) { FENCE();
      sv = ld16c(rctl, WS_STATS + (256 * id.R + 64 * h + lane) * 16, 0);
      if (__all((sv.y == tag) & (sv.w == tag))) break;
      __builtin_amdgcn_s_sleep(4);
    }
  }
  *(LAS v2u*)LP(L_STATS + (64 * h + lane) * 8) = v2u{sv.x, sv.z};
  BAR();
  cs_rd(0); step(); cs_mm();
  WAITV(NP); BAR();
  cs_rd(STG);
  auto w2_check = [&](v4u fl) {
    if (h == 3) {
      if (!__all((fl.x == tag) & (fl.y == tag) & (fl.z == tag) & (fl.w == tag))) {
        _Pragma("unroll 1") for (int spin = 0; spin < SPIN; ++spin) { FENCE();
          const v4u f2 = ld16c(rctl, WS_FLW + lane * 16, 0);
          if (__all((f2.x == tag) & (f2.y == tag) & (f2.z == tag) & (f2.w == tag))) break;
          __builtin_amdgcn_s_sleep(4);
        }
      }
      st4c(tag, rctl, (lane == 0 && blockIdx.x == 255) ? WS_EPOCH : OOB);
    }
  };
  if constexpr (!SSM) {
    v4u fl;
    asm volatile("buffer_load_dwordx4 %0, %1, %2, 0 offen sc1" : "=v"(fl) : "v"(h == 3 ? WS_FLW + lane * 16 : OOB), "s"(rctl));
    step(); cs_mm();
    asm volatile("s_waitcnt vmcnt(%1)" : "+v"(fl) : "i"(NP) : "memory");
    w2_check(fl);
  } else {
    step(); cs_mm();
    WAITV(NP);
  }
  BAR();
#pragma unroll
  for (int t = 0; t < 5; ++t) hs.acs[t] = acs[t < NWP ? t : 0];
  hs.pl = pl; hs.pr = pr; hs.pi = pi;
  return tag;
}
template <bool SSM>
DI void helper_tail(const Par& p, const Ids& id, const int h, const int lane, const unsigned tag, HState& hs) {
  constexpr int NWP = SSM ? 4 : 5;
  const int c = lane & 15, q = lane >> 4;
  const __amdgpu_buffer_rsrc_t rctl = mkrs(p.ws, WS_CTL_BYTES);
  f32x4* acs = hs.acs;
  const unsigned pl = hs.pl, pr = hs.pr, pi = hs.pi;
  v4u fc[NWP];
  const unsigned ca = SSM ? 32768 + (h >> 1) * 8192 + c * 128 + (((4 * (h & 1) + q) ^ (c >> 1)) * 16)
                          : 32768 + (h >> 1) * UPO + (32 * (h & 1) + 4 * q + (c >> 2)) * 160 + (c & 3) * 8;
  const v4u ones = {0x3F803F80u, 0x3F803F80u, 0x3F803F80u, 0x3F803F80u};
  auto cs_rd = [&](unsigned s) {
    const lp a = LP(s + ca);
#pragma unroll
    for (int t = 0; t < NWP; ++t) { if constexpr (SSM) fc[t] = l128(a + t * 2048); else fc[t] = cat(ltr(a + t * 32), ltr(a + t * 32 + 2560)); }
    SB0();
  };
  auto cs_mm = [&]() {
    SB0();
    __builtin_amdgcn_s_setprio(2);
#pragma unroll
    for (int t = 0; t < NWP; ++t) acs[t] = mfma16(fc[t], ones, acs[t]);
    __builtin_amdgcn_s_setprio(0);
    SB0();
  };
  cs_rd(2 * STG);
  if (SSM) {
    if (h == 3) {
      _Pragma("unroll 1") for (int spin = 0; spin < SPIN; ++spin) { FENCE();
        const v4u f2 = ld16c(rctl, WS_FLW + lane * 16, 0);
        if (__all((f2.x == tag) & (f2.y == tag) & (f2.z == tag) & (f2.w == tag))) break;
        __builtin_amdgcn_s_sleep(4);
      }
    }
  }
  float br = 0.f, bi = 0.f;
  if (SSM && h == 2) {
    const float dt = rbf(expf(bf2f(pl))), ex = expf(dt * bf2f(pr));
    float sn, cn;
    sincosf(dt * bf2f(pi), &sn, &cn);
    br = ex * cn; bi = ex * sn;
  }
  cs_mm();
  WAITV(SSM ? 0 : 15); BAR();
  cs_rd(0);
  if (SSM && h == 2) {
    const float lr = bf2f(pr), li = bf2f(pi), nr = br - 1.f, ni = bi, den = lr * lr + li * li;
    LAS float* cst = (LAS float*)LP(L_CST);
    cst[4 * 128 + lane] = 2.f * (nr * lr + ni * li) / den; cst[4 * 128 + 64 + lane] = 2.f * (ni * lr - nr * li) / den;
    cst[lane] = br; cst[64 + lane] = bi;
#pragma unroll
    for (int s = 1; s <= 8; ++s) {
      const float a = br * br - bi * bi, b = 2.f * br * bi;
      br = a; bi = b;
      const int k = s == 3 ? 1 : s == 6 ? 2 : s == 8 ? 3 : 0;
      if (k) { cst[k * 128 + lane] = br; cst[k * 128 + 64 + lane] = bi; }
    }
  }
  cs_mm();
  if (c == 0) {
#pragma unroll
    for (int t = 0; t < NWP; ++t) {
      const int col = SSM ? 32 * (t >> 1) + 8 * q + 4 * (t & 1) : 16 * t + 4 * q;
      sf4(LP(L_CS + ((h & 1) * 160 + (h >> 1) * 80 + col) * 4), acs[t]);
    }
  }
  sig(L_CSF + h * 4, 1);
}

DI void carries(const Par& p, const Ids& id, const int h, const int lane, const unsigned tag, WRing& wr) {
  const __amdgpu_buffer_rsrc_t rctl = mkrs(p.ws, WS_CTL_BYTES);
  const int c = lane & 15, q = lane >> 4;
  {
    const int ch = 16 * h + c;
    lwait<2>(L_CSF + 8, 0, 1);
    LAS float* cst = (LAS float*)LP(L_CST);
    const float l8r = cst[128 + ch], l8i = cst[192 + ch], l64r = cst[256 + ch], l64i = cst[320 + ch], lwr = cst[384 + ch], lwi = cst[448 + ch];
    const float kr = cst[512 + ch], ki = cst[576 + ch];
    float w0r = 1.f, w0i = 0.f, w1r = 1.f, w1i = 0.f, qr = kr, qi = ki;
#pragma unroll
    for (int s = 0; s < 6; ++s) {
      float a = w0r, b = w0i; cm1(a, b, lwr, lwi, 0.f, 0.f);
      if (s < id.R - 1 - q) { w0r = a; w0i = b; }
      if (s < id.R - 5 - q) { w1r = a; w1i = b; }
    }
#pragma unroll
    for (int s = 0; s < 3; ++s) { float a = qr, b = qi; cm1(a, b, l64r, l64i, 0.f, 0.f); if (s < q) { qr = a; qi = b; } }
    const __amdgpu_buffer_rsrc_t rag = mkrs(p.ws + WS_AGG + id.R * (8 * 640 * 16), id.R * 640 * 16);
    LAS float* pe = (LAS float*)LP(L_E + 8 * q * ESTR + ch * 4);
    asm volatile("" : "+v"(w0r), "+v"(w0i), "+v"(w1r), "+v"(w1i), "+v"(qr), "+v"(qi));
    unsigned ao[2], po = (q * 640 + 64 * id.g + ch) * 16;
#pragma unroll
    for (int z = 0; z < 2; ++z) ao[z] = q + 4 * z > id.R ? WS_AGG + (((q + 4 * z) * 8 + id.R) * 640 + 64 * id.g + ch) * 16 : OOB;
    asm volatile("" : "+v"(ao[0]), "+v"(ao[1]), "+v"(po));
    __builtin_amdgcn_s_setprio(3);
    HBAR();
    float er[8], ei[8];
#pragma unroll
    for (int s = 0; s < 8; ++s) { er[s] = pe[s * (ESTR / 4)]; ei[s] = pe[s * (ESTR / 4) + 64]; }
    float a = er[0], b = ei[0];
#pragma unroll
    for (int s = 1; s < 8; ++s) cm1(a, b, l8r, l8i, er[s], ei[s]);
    float ta[4], tb[4];
    gather4(a, ta); gather4(b, tb);
    float pa = 0.f, pb = 0.f;
    a = ta[0]; b = tb[0];
#pragma unroll
    for (int s = 1; s < 4; ++s) {
      if (q == s) { pa = a; pb = b; }
      cm1(a, b, l64r, l64i, ta[s], tb[s]);
    }
#pragma unroll
    for (int z = 0; z < 2; ++z)
      st16c(v4u{__float_as_uint(a), tag, __float_as_uint(b), tag}, rctl, ao[z]);
    SB0();
    wr.dma01();
    v4u p0 = ld16c(rag, po, 0), p1 = ld16c(rag, po, 4 * 640 * 16);
    SB0();
    cm1(pa, pb, kr, ki, 0.f, 0.f);
#pragma unroll
    for (int s = 0; s < 7; ++s) cm1(er[s], ei[s], kr, ki, 0.f, 0.f);
    _Pragma("unroll 1") for (int spin = 0; spin < SPIN; ++spin) { FENCE();
      const bool ok = ((q >= id.R) | ((p0.y == tag) & (p0.w == tag))) & ((q + 4 >= id.R) | ((p1.y == tag) & (p1.w == tag)));
      if (__all(ok)) break;
      p0 = ld16c(rag, po, 0); p1 = ld16c(rag, po, 4 * 640 * 16);
    }
    float ca = __uint_as_float(p1.x), cb = __uint_as_float(p1.z);
    cm1(ca, cb, w1r, w1i, 0.f, 0.f);
    { float x = __uint_as_float(p0.x), y = __uint_as_float(p0.z); cm1(x, y, w0r, w0i, ca, cb); ca = x; cb = y; }
    gather4(ca, ta); gather4(cb, tb);
    ca = (ta[0] + ta[1]) + (ta[2] + ta[3]); cb = (tb[0] + tb[1]) + (tb[2] + tb[3]);
    cm1(ca, cb, qr, qi, pa, pb);
#pragma unroll
    for (int s = 0; s < 8; ++s) {
      pe[s * (ESTR / 4)] = ca; pe[s * (ESTR / 4) + 64] = -cb;
      if (s < 7) cm1(ca, cb, l8r, l8i, er[s], ei[s]);
    }
    __builtin_amdgcn_s_setprio(0);
    HBAR();
  }
}

__global__ __launch_bounds__(512, 2) void fused(Par p) {
  const int bid = blockIdx.x, xcd = bid & 7, idx = bid >> 3, a = xcd >> 2, b = xcd & 3, s = idx >> 2;
  Ids id;
  const bool ssm = idx < 10, part = idx >= 8 && idx < 12;
  id.R = 4 * a + (part ? 2 * (b & 1) + (idx & 1) : (idx & 3));
  id.g = ssm ? (part ? 8 + (b >> 1) : 2 * b + s) : (part ? 20 + (b >> 1) : 5 * b + s - 3);
  id.cg = ssm ? id.g : 10 + id.g;
  id.rt = 16 * a + 8 * (b >> 1) + (idx >> 2);
  id.ct = 4 * (b & 1) + (idx & 3);
  const int wv = rfl(threadIdx.x >> 6), lane = threadIdx.x & 63;
  unsigned cpf = 0;
  if (wv == 3) {
    const unsigned long long pc = __builtin_amdgcn_s_getpc() & ~127ull;
    cpf = __builtin_amdgcn_raw_buffer_load_b32(mkrs((const void*)pc, CODEPF), lane < 12 ? (idx * 12 + lane) * 128 : OOB, 0, 0);
  }
  const __amdgpu_buffer_rsrc_t rctl = mkrs(p.ws, WS_CTL_BYTES);
  const __amdgpu_buffer_rsrc_t rw = mkrs(p.ws + WS_W2 + (unsigned)id.ct * (NKB * 5120), NKB * 5120);
  const unsigned vl = lane * 16;
  const unsigned fo = WS_FLH + (id.rt * 8 + id.ct) * 128 + (lane & 31) * 4;
  WRing wr;
  wr.rw = rw; wr.vl = vl; wr.wv = wv;
  unsigned tag;
  if (wv < 4) {
    tag = ssm ? compute_wave<true>(p, id, wv, lane) : compute_wave<false>(p, id, wv, lane);
    WAITV(0);
    unsigned old = 0;
    if (lane == 0) old = __hip_atomic_fetch_add((LAS unsigned*)LP(L_CNT), 1u, __ATOMIC_RELAXED, __HIP_MEMORY_SCOPE_WORKGROUP);
    st4c(tag, rctl, (lane < 32 && rfl(old) == 3) ? WS_FLH + (id.R * 32 + lane) * 128 + id.cg * 4 : OOB);
    asm volatile("" ::"v"(cpf));
    STAMP(16);
#pragma unroll
    for (int it = NL; it < NL + DW; ++it) wr.load(it);
  } else {
    HState hs;
    tag = ssm ? helper_wave<true>(p, id, wv - 4, lane, hs) : helper_wave<false>(p, id, wv - 4, lane, hs);
    if (ssm) helper_tail<true>(p, id, wv - 4, lane, tag, hs);
    wr.load(NL); wr.load(NL + 1); wr.load(NL + 2);
    if (!ssm) helper_tail<false>(p, id, wv - 4, lane, tag, hs);
#pragma unroll
    for (int it = NL + 3; it < NL + DW; ++it) wr.load(it);
    if (ssm) carries(p, id, wv - 4, lane, tag, wr); else wr.dma01();
    lwait<2>(L_CNT, 0, 4);
    STAMP(16);
    if (wv > 4) {
      asm volatile("s_nop 4");
      dma_lin(MkSeq<14>::T{}, rw, (wv - 5) * 13312, vl, 81920 + (wv - 5) * 13312);
    } else {
      _Pragma("unroll 1") for (int spin = 0; spin < SPIN; ++spin) { FENCE();
        if (__all((ld4c(rctl, fo) == tag) | ((lane & 31) < 10))) break;
        __builtin_amdgcn_s_sleep(2);
      }
      sig(L_GO, 1);
    }
  }

  const __amdgpu_buffer_rsrc_t rh = mkrs(p.ws + WS_H + (unsigned)id.rt * (NKB * 4096), NKB * 4096);
  v4u hb[DH][4];
  auto loadh = [&](int it) {
#pragma unroll
    for (int j = 0; j < 4; ++j) { hb[it % DH][j] = ld16c(rh, vl + j * 1024, (8 * it + wv) * 4096); SB0(); }
  };
  lwait<1>(L_GO, 0, 1);
  SB0();
  STAMP(20);
#pragma unroll
  for (int it = 0; it < DH; ++it) { loadh(it); SB0(); }
  f32x4 acc[4][5];
#pragma unroll
  for (int j = 0; j < 4; ++j)
#pragma unroll
    for (int i = 0; i < 5; ++i) acc[j][i] = f32x4{0.f, 0.f, 0.f, 0.f};
  const int tid = wv * 64 + lane;
  v2u xv[3], dv[3]; v4u sv[3]; int oo[3], ra[3];
  unsigned sfl = 0;
  const lp pw = LP(L_W + wv * 5120 + vl), pw2 = LP(wv * 5120 + vl);
  SB0();
  if (wv > 4) {
    WAITV(4 * DH);
    if (lane == 0) __hip_atomic_fetch_add((LAS unsigned*)LP(L_RDY), 1u, __ATOMIC_RELAXED, __HIP_MEMORY_SCOPE_WORKGROUP);
  }
  lwait<1>(L_RDY, 0, 3);
  v4u wl = l128(pw);
  SB0();
#pragma unroll
  for (int it = 0; it < NIT; ++it) {
    if (it < NL) {
#pragma unroll
      for (int i = 0; i < 5; ++i) {
        const v4u wf = wl;
        if (5 * it + i + 1 < 5 * NL) wl = l128((5 * it + i + 1) / 5 == 2 ? pw2 + ((5 * it + i + 1) % 5) * 1024 : pw + ((5 * it + i + 1) / 5) * 40960 + ((5 * it + i + 1) % 5) * 1024);
#pragma unroll
        for (int j = 0; j < 4; ++j) acc[j][i] = mfma16(wf, hb[it % DH][j], acc[j][i]);
        SB0();
      }
    } else {
#pragma unroll
      for (int j = 0; j < 4; ++j)
#pragma unroll
        for (int i = 0; i < 5; ++i) acc[j][i] = mfma16(wr.wb[it % DW][i], hb[it % DH][j], acc[j][i]);
    }
    SB0();
    if (it + DH == 6) {
      if (__builtin_expect(!__all(sfl == tag), 0)) {
        _Pragma("unroll 1") for (int spin = 0; spin < SPIN; ++spin) { FENCE();
          if (__all(ld4c(rctl, fo) == tag)) break;
          __builtin_amdgcn_s_sleep(2);
        }
      }
      FENCE();
      STAMP(18); STAMPC(28);
    }
    if (it == 0) sfl = ld4c(rctl, fo);
    if (it + DH < NIT) loadh(it + DH);
    if (it >= NL && it + DW < NIT) wr.load(it + DW);
    if (it == NIT - DH) {
#pragma unroll
      for (int r = 0; r < 3; ++r) {
        const int sl = min(tid + 512 * r, 1279), row = sl / 20, qd = sl - 20 * row;
        const int tok = 128 * (id.rt >> 1) + 8 * (row >> 2) + 4 * (id.rt & 1) + (row & 3), d = 80 * id.ct + 4 * qd;
        oo[r] = tok * D_ + d;
        ra[r] = ((row & 3) * 5 + (qd >> 2)) * 1024 + (qd & 3) * 256 + (((row >> 2) + qd) & 15) * 16;
        xv[r] = *(const v2u*)(p.x + oo[r]); dv[r] = *(const v2u*)(p.dsk + d);
        sv[r] = ld16c(rctl, WS_STATS + tok * 16, 0);
      }
    }
#define BARIT 2
    if (it == BARIT) BAR();
    SB0();
  }
  STAMP(21); STAMPC(29);
#pragma unroll
  for (int i = 0; i < 5; ++i) {
    const lp rb = LP(wv * 20480 + (lane >> 4) * 256 + (((lane & 15) + 4 * i + (lane >> 4)) & 15) * 16);
#pragma unroll
    for (int j = 0; j < 4; ++j) sf4(rb + (j * 5 + i) * 1024, acc[j][i]);
  }
  BAR();
  STAMP(19);
#pragma unroll
  for (int r = 0; r < 3; ++r) {
    if (r < 2 || wv < 4) {
      const lp q0 = LP(ra[r]), q1 = LP(ra[r] + 4 * 20480);
      f32x4 sum = ((lf4(q0) + lf4(q0 + 20480)) + (lf4(q0 + 40960) + lf4(q0 + 61440))) + ((lf4(q1) + lf4(q1 + 20480)) + (lf4(q1 + 40960) + lf4(q1 + 61440)));
      const f32x4 xf = {bflo(xv[r].x), bfhi(xv[r].x), bflo(xv[r].y), bfhi(xv[r].y)};
      const f32x4 df = {bflo(dv[r].x), bfhi(dv[r].x), bflo(dv[r].y), bfhi(dv[r].y)};
      sum += xf + (xf - __uint_as_float(sv[r].x)) * __uint_as_float(sv[r].z) * df;
#define OUTAUX 16
      __builtin_amdgcn_raw_buffer_store_b128(__builtin_bit_cast(v4u, sum), mkrs(p.out, L_ * D_ * 4), oo[r] * 4, 0, OUTAUX);
    }
  }
  STAMP(22);
}

extern "C" void kernel_launch(void* const* d_in, const int* in_sizes, int n_in, void* d_out, int out_size, void* d_ws, size_t ws_size, hipStream_t stream) {
  Par p;
  p.x = (const u16*)d_in[0]; p.lre = (const u16*)d_in[1]; p.lim = (const u16*)d_in[2];
  p.bre = (const u16*)d_in[3]; p.bim = (const u16*)d_in[4]; p.cre = (const u16*)d_in[5]; p.cim = (const u16*)d_in[6];
  p.ldt = (const u16*)d_in[7]; p.dsk = (const u16*)d_in[8];
  p.wg = (const u16*)d_in[10]; p.wu = (const u16*)d_in[11]; p.wd = (const u16*)d_in[12];
  p.ws = (unsigned char*)d_ws + (WSOFF == 1 ? ((ws_size - (21u << 20)) & ~(size_t)0xFFFFF) : WSOFF == 2 ? (ws_size / 2) : 0); p.out = (float*)d_out;
  (void)hipFuncSetAttribute((const void*)fused, hipFuncAttributeMaxDynamicSharedMemorySize, LDS1);
  fused<<<256, 512, LDS1, stream>>>(p);
}
```

```cpp
#include <hip/hip_runtime.h>
#include <stdint.h>

typedef unsigned short u16;
typedef unsigned v4u __attribute__((ext_vector_type(4)));
typedef unsigned v2u __attribute__((ext_vector_type(2)));
typedef float f32x4 __attribute__((ext_vector_type(4)));
typedef float f32x2 __attribute__((ext_vector_type(2)));
typedef float f32x16 __attribute__((ext_vector_type(16)));
typedef short bf16x8 __attribute__((ext_vector_type(8)));
typedef short s16x4 __attribute__((ext_vector_type(4)));
typedef __bf16 bf2 __attribute__((ext_vector_type(2)));
#define LAS __attribute__((address_space(3)))
#define DI __device__ __forceinline__
typedef LAS unsigned char* lp;

constexpr int L_ = 2048, D_ = 640, P_ = 640, F_ = 1728;
constexpr int NKB = 94;
constexpr unsigned WS_EPOCH = 0;
constexpr unsigned WS_FLW = 2048;
constexpr unsigned WS_FLH = 4096;
constexpr unsigned WS_AGG = WS_FLH + 32768;
constexpr unsigned WS_STATS = WS_AGG + 64 * 640 * 16;
constexpr unsigned WS_CTL_BYTES = WS_STATS + 32768;
constexpr unsigned WS_W2 = 1u << 20;
constexpr unsigned WS_W2_BYTES = 8 * NKB * 5120;
constexpr unsigned WS_H = 8u << 20;
constexpr unsigned WS_H_BYTES = 32 * NKB * 4096;

constexpr int UPO = 10368;
constexpr int STG = 32768 + 20480 + 128;
constexpr int LDS1 = 163840;
constexpr int L_STATS = 3 * STG;
constexpr int L_CS = L_STATS + 2048;
constexpr int L_CNT = L_CS + 1280, L_GO = L_CNT + 4, L_RDY = L_CNT + 8, L_CSF = L_CNT + 16;
constexpr int L_CST = 49152;
constexpr int L_W = STG;
constexpr int L_E = 136000, ESTR = 528;
constexpr int SPIN = 1 << 18;
constexpr unsigned OOB = 0x40000000u;

DI float bf2f(unsigned b) { return __uint_as_float(b << 16); }
DI float bflo(unsigned w) { return __uint_as_float(w << 16); }
DI float bfhi(unsigned w) { return __uint_as_float(w & 0xffff0000u); }
DI unsigned pk2(float a, float b) { f32x2 v = {a, b}; return __builtin_bit_cast(unsigned, __builtin_convertvector(v, bf2)); }
DI float rbf(float a) { return bflo(pk2(a, a) & 0xffffu); }
DI f32x4 mfma16(v4u a, v4u b, f32x4 c) { return __builtin_amdgcn_mfma_f32_16x16x32_bf16(__builtin_bit_cast(bf16x8, a), __builtin_bit_cast(bf16x8, b), c, 0, 0, 0); }
DI f32x16 mfma32(v4u a, v4u b, f32x16 c) { return __builtin_amdgcn_mfma_f32_32x32x16_bf16(__builtin_bit_cast(bf16x8, a), __builtin_bit_cast(bf16x8, b), c, 0, 0, 0); }
DI unsigned rfl(unsigned v) { return __builtin_amdgcn_readfirstlane(v); }
DI __amdgpu_buffer_rsrc_t mkrs(const void* p, unsigned bytes) { return __builtin_amdgcn_make_buffer_rsrc((void*)p, 0, bytes, 0x00020000); }
template <int LI, int SI> DI void glds(__amdgpu_buffer_rsrc_t rs, unsigned lb, unsigned voff, unsigned sb) {
  unsigned t;
  asm volatile("s_add_u32 m0, %1, %5\n\ts_add_u32 %0, %4, %6\n\tbuffer_load_dwordx4 %2, %3, %0 offen lds" : "=&s"(t) : "s"(lb), "v"(voff), "s"(rs), "s"(sb), "i"(LI), "i"(SI) : "scc");
}
template <int LI, int SI> DI void gldsc(__amdgpu_buffer_rsrc_t rs, unsigned lb, unsigned voff, unsigned sb) {
  unsigned t;
  asm volatile("s_add_u32 m0, %1, %5\n\ts_add_u32 %0, %4, %6\n\tbuffer_load_dwordx4 %2, %3, %0 offen sc1 lds" : "=&s"(t) : "s"(lb), "v"(voff), "s"(rs), "s"(sb), "i"(LI), "i"(SI) : "scc");
}
template <int... I> struct Seq {};
template <int N, int... I> struct MkSeq : MkSeq<N - 1, N - 1, I...> {};
template <int... I> struct MkSeq<0, I...> { typedef Seq<I...> T; };
template <int... I> DI void dma_lin(Seq<I...>, __amdgpu_buffer_rsrc_t rs, unsigned lb, unsigned vl, unsigned sb) { (gldsc<I * 1024, I * 1024>(rs, lb, vl, sb), ...); }
template <int... I> DI void dma_x(Seq<I...>, __amdgpu_buffer_rsrc_t rs, unsigned lb, unsigned v0, unsigned v1, unsigned sb) { (glds<I * 1024, (64 * (I & 1) + (I >> 1)) * 1280>(rs, lb, (I & 1) ? v1 : v0, sb), ...); }
template <int... I> DI void dma_b(Seq<I...>, __amdgpu_buffer_rsrc_t rs, unsigned lb, const unsigned* v, unsigned sb) { (glds<I * 1024, (16 * (I & 1) + 4 * (I >> 1)) * 1280>(rs, lb, v[I & 1], sb), ...); }
template <int... I> DI void dma_g(Seq<I...>, __amdgpu_buffer_rsrc_t rs, unsigned lb, const unsigned* v, unsigned sb) { (glds<I * 1024, 0>(rs, lb, v[I], sb), ...); }
DI lp LP(unsigned a) { return (lp)(uintptr_t)a; }
DI v4u l128(lp a) { return *(LAS v4u*)a; }
DI f32x4 lf4(lp a) { return *(LAS f32x4*)a; }
DI void sf4(lp a, f32x4 v) { *(LAS f32x4*)a = v; }
DI s16x4 ltr(lp a) { return __builtin_amdgcn_ds_read_tr16_b64_v4i16((LAS s16x4*)a); }
DI v4u cat(s16x4 lo, s16x4 hi) { return __builtin_bit_cast(v4u, __builtin_shufflevector(lo, hi, 0, 1, 2, 3, 4, 5, 6, 7)); }
DI v4u ld16(__amdgpu_buffer_rsrc_t r, unsigned vo, unsigned so) { return __builtin_amdgcn_raw_buffer_load_b128(r, vo, so, 0); }
DI v4u ld16c(__amdgpu_buffer_rsrc_t r, unsigned vo, unsigned so) { return __builtin_amdgcn_raw_buffer_load_b128(r, vo, so, 16); }
DI unsigned ld4c(__amdgpu_buffer_rsrc_t r, unsigned vo) { return __builtin_amdgcn_raw_buffer_load_b32(r, vo, 0, 16); }
DI void st16c(v4u v, __amdgpu_buffer_rsrc_t r, unsigned vo) { __builtin_amdgcn_raw_buffer_store_b128(v, r, vo, 0, 16); }
DI void st4c(unsigned v, __amdgpu_buffer_rsrc_t r, unsigned vo) { __builtin_amdgcn_raw_buffer_store_b32(v, r, vo, 0, 16); }
#define BAR() asm volatile("s_waitcnt lgkmcnt(0)\n\ts_barrier" ::: "memory")
#define SB0() __builtin_amdgcn_sched_barrier(0)
#define WAITV(n) asm volatile("s_waitcnt vmcnt(%0)" ::"i"(n) : "memory")
#define FENCE() asm volatile("" ::: "memory")
template <int C> DI float dppf(float v) { return __int_as_float(__builtin_amdgcn_update_dpp(0, __float_as_int(v), C, 0xf, 0xf, true)); }
template <int N> DI f32x4 shr4(f32x4 v) { return f32x4{dppf<0x110 + N>(v.x), dppf<0x110 + N>(v.y), dppf<0x110 + N>(v.z), dppf<0x110 + N>(v.w)}; }
DI float sum8(float v) {
  v += dppf<0xB1>(v); v += dppf<0x4E>(v); v += dppf<0x141>(v);
  return v;
}
DI float dot2(unsigned a, unsigned b, float c) { return __builtin_amdgcn_fdot2_f32_bf16(__builtin_bit_cast(bf2, a), __builtin_bit_cast(bf2, b), c, false); }
DI void gather4(float x, float (&t)[4]) {
  const auto a = __builtin_amdgcn_permlane16_swap(__float_as_uint(x), __float_as_uint(x), false, false);
  const auto e = __builtin_amdgcn_permlane32_swap(a[0], a[0], false, false), o = __builtin_amdgcn_permlane32_swap(a[1], a[1], false, false);
  t[0] = __uint_as_float(e[0]); t[2] = __uint_as_float(e[1]); t[1] = __uint_as_float(o[0]); t[3] = __uint_as_float(o[1]);
}
DI void cm1(float& a, float& b, float lr, float li, float ur, float ui) {
  const float na = fmaf(lr, a, fmaf(-li, b, ur)), nb = fmaf(lr, b, fmaf(li, a, ui));
  a = na; b = nb;
}
DI f32x4 fma4(f32x4 a, f32x4 b, f32x4 c) { return __builtin_elementwise_fma(a, b, c); }
DI f32x4 sp4(float a) { return f32x4{a, a, a, a}; }
DI void cmac(f32x4& a, f32x4& b, f32x4 lr, f32x4 li, f32x4 ur, f32x4 ui) {
  const f32x4 na = fma4(lr, a, fma4(-li, b, ur)), nb = fma4(lr, b, fma4(li, a, ui));
  a = na; b = nb;
}

#define STAMP(i)
#define STAMPC(i)

#define LSTAMP(i)
struct Par {
  const u16 *x, *lre, *lim, *bre, *bim, *cre, *cim, *ldt, *dsk, *wg, *wu, *wd;
  unsigned char* ws;
  float* out;
};
struct Ids { int R, g, cg, rt, ct; };
#define CODEPF 45056

#define PDW 5
#define PDH 2
constexpr int DW = PDW, DH = PDH, NIT = 12, NL = 3;
#define PIN4(x) asm volatile("" : "+v"(x))
DI void sig(unsigned addr, unsigned v) {
  asm volatile("s_mov_b64 exec, 1\n\tds_write_b32 %0, %1\n\ts_mov_b64 exec, -1" ::"v"(addr), "v"(v) : "memory");
}
DI unsigned lrd(unsigned addr) { return *(volatile LAS unsigned*)LP(addr); }
#define LSLEEP 1
template <int SL = 1> DI void lwait(unsigned addr, unsigned v, unsigned need) {
  if (__builtin_expect(!__all(v >= need), 0)) {
    _Pragma("unroll 1") for (int spin = 0; spin < 64 * SPIN; ++spin) { FENCE();
      if (__all(lrd(addr) >= need)) break;
      __builtin_amdgcn_s_sleep(SL);
    }
  }
  FENCE();
}

struct WRing {
  v4u wb[DW][5];
  __amdgpu_buffer_rsrc_t rw;
  unsigned vl, wv;
  DI void load(int it) {
#pragma unroll
    for (int i = 0; i < 5; ++i) { wb[it % DW][i] = ld16c(rw, vl + (i & 3) * 1024, (8 * it + wv) * 5120 + (i >> 2) * 4096); SB0(); }
  }
  DI void dma01() {
    asm volatile("s_nop 4");
    dma_lin(MkSeq<20>::T{}, rw, L_W + (wv - 4) * 20480, vl, (wv - 4) * 20480);
  }
};

DI float wsum(float v) {
  v = sum8(v); v += dppf<0x128>(v);
  float t[4]; gather4(v, t);
  return (t[0] + t[1]) + (t[2] + t[3]);
}
DI unsigned mktag(unsigned epoch) { return rfl(epoch) * 0x9E3779B1u + 0x7F4A7C15u; }
struct RowIn { v4u xa[2]; unsigned xb[2], epoch; };
template <bool SSM> DI void row_load(const Par& p, const Ids& id, const int w, const int lane, RowIn& in) {
  const int row = 256 * id.R + 8 * id.cg + 2 * w;
#pragma unroll
  for (int z = 0; z < 2; ++z) { in.xa[z] = *(const v4u*)(p.x + (row + z) * D_ + lane * 8); in.xb[z] = *(const unsigned*)(p.x + (row + z) * D_ + 512 + lane * 2); }
  in.epoch = ld4c(mkrs(p.ws, WS_CTL_BYTES), WS_EPOCH);
  SB0();
}
DI unsigned row_stats(const Par& p, const Ids& id, const int w, const int lane, const RowIn& in) {
  const __amdgpu_buffer_rsrc_t rctl = mkrs(p.ws, WS_CTL_BYTES);
  const int row = 256 * id.R + 8 * id.cg + 2 * w;
  const v4u* xa = in.xa; const unsigned* xb = in.xb; const unsigned epoch = in.epoch;
  float v[2][10], mean[2], s2[2];
#pragma unroll
  for (int z = 0; z < 2; ++z) {
    const float t[10] = {bflo(xa[z].x), bfhi(xa[z].x), bflo(xa[z].y), bfhi(xa[z].y), bflo(xa[z].z), bfhi(xa[z].z), bflo(xa[z].w), bfhi(xa[z].w), bflo(xb[z]), bfhi(xb[z])};
#pragma unroll
    for (int e = 0; e < 10; ++e) v[z][e] = t[e];
    mean[z] = wsum(((t[0] + t[1]) + (t[2] + t[3])) + ((t[4] + t[5]) + (t[6] + t[7])) + (t[8] + t[9])) * (1.f / D_);
  }
#pragma unroll
  for (int z = 0; z < 2; ++z) {
    float a = 0.f, b = 0.f;
#pragma unroll
    for (int e = 0; e < 5; ++e) { a = fmaf(v[z][e] - mean[z], v[z][e] - mean[z], a); b = fmaf(v[z][5 + e] - mean[z], v[z][5 + e] - mean[z], b); }
    s2[z] = wsum(a + b);
  }
  const unsigned tag = mktag(epoch);
  const float m = rbf((lane & 1) ? mean[1] : mean[0]), rs = 1.f / rbf(sqrtf(rbf(rbf(((lane & 1) ? s2[1] : s2[0]) * (1.f / D_)) + 1e-5f)));
  st16c(v4u{__float_as_uint(m), tag, __float_as_uint(rs), tag}, rctl, lane < 2 ? WS_STATS + (row + lane) * 16 : OOB);
  return tag;
}

template <bool SSM>
DI unsigned compute_wave(const Par& p, const Ids& id, const int w, const int lane) {
  STAMP(0);
  unsigned tag, cf = 0;
  RowIn rin;
  row_load<SSM>(p, id, w, lane, rin);
  if constexpr (!SSM) {
    const int r = lane & 31, hh = lane >> 5;
    const unsigned xa = (64 * w + r) * 128 + ((hh ^ ((r >> 1) & 7)) * 16);
    const unsigned wa = 32768 + ((lane >> 4) & 1) * UPO + (8 * hh + ((lane & 15) >> 2)) * 160 + (lane & 3) * 8;
    f32x16 acc[5][2] = {};
#pragma unroll
    for (int e = 0; e < 5; ++e) { PIN4(acc[e][0]); PIN4(acc[e][1]); }
    v4u fx[2][2]; s16x4 fl[4], fh[4];
    lp px[4], pw;
    auto setp = [&](unsigned sb) {
#pragma unroll
      for (int s = 0; s < 4; ++s) px[s] = LP(sb + (xa ^ (32 * s)));
      pw = LP(sb + wa);
    };
    auto woff = [&](int n) { return (n / 5) * 2560 + (n % 5) * 32; };
    setp(0);
    SB0();
    tag = row_stats(p, id, w, lane, rin);
    STAMP(17);
    SB0();
    BAR();
    STAMP(1);
    fx[0][0] = l128(px[0]); fx[0][1] = l128(px[0] + 4096);
#pragma unroll
    for (int n = 0; n < 3; ++n) { fl[n] = ltr(pw + woff(n)); fh[n] = ltr(pw + woff(n) + 640); }
    unsigned sb = 0;
#pragma unroll 1
    for (int i = 0; i < 10; ++i) {
#pragma unroll
      for (int n = 0; n < 20; ++n) {
        const int ks = n / 5, e = n % 5, nn = (n + 3) % 20;
        if (n == 17) { if (i < 9) BAR(); LSTAMP(i); sb = sb == 2 * STG ? 0 : sb + STG; setp(sb); SB0(); }
        const v4u wf = cat(fl[n & 3], fh[n & 3]);
        acc[e][0] = mfma32(wf, fx[ks & 1][0], acc[e][0]); SB0();
        fl[(n + 3) & 3] = ltr(pw + woff(nn));
        fh[(n + 3) & 3] = ltr(pw + woff(nn) + 640); SB0();
        acc[e][1] = mfma32(wf, fx[ks & 1][1], acc[e][1]); SB0();
        if (ks < 3 && e == 1) { fx[(ks + 1) & 1][0] = l128(px[ks + 1]); fx[(ks + 1) & 1][1] = l128(px[ks + 1] + 4096); }
        if (n == 17) { fx[0][0] = l128(px[0]); fx[0][1] = l128(px[0] + 4096); }
        if (n == 18) cf = lrd(L_CSF + (lane & 3) * 4);
        SB0();
      }
    }
    lwait(L_CSF + (lane & 3) * 4, cf, 1);
    STAMP(12);
    const __amdgpu_buffer_rsrc_t rh = mkrs(p.ws + WS_H + (unsigned)(4 * id.R + w) * (NKB * 4096), 54 * 4096);
    const unsigned so0 = (r >> 4) * 1024 + (hh * 16 + (r & 15)) * 16;
    float mu[2], r2[2];
#pragma unroll
    for (int u = 0; u < 2; ++u) {
      const int F = 4 * w + 2 * u + (r >> 4);
      const f32x2 s = *(LAS f32x2*)LP(L_STATS + (128 * (F >> 3) + 8 * (r & 15) + (F & 7)) * 8);
      mu[u] = s.x; r2[u] = s.y * s.y;
    }
    const lp pc = LP(L_CS + hh * 16);
#pragma unroll
    for (int e = 0; e < 5; ++e) {
      const f32x4 g0 = lf4(pc + e * 64) + lf4(pc + e * 64 + 640), g1 = lf4(pc + e * 64 + 32) + lf4(pc + e * 64 + 672);
      const f32x4 u0 = lf4(pc + e * 64 + 320) + lf4(pc + e * 64 + 960), u1 = lf4(pc + e * 64 + 352) + lf4(pc + e * 64 + 992);
      const int T = 5 * id.g + e;
      const unsigned so = so0 + (T >> 1) * 4096 + (T & 1) * 512;
#pragma unroll
      for (int u = 0; u < 2; ++u) {
        const f32x16 a = acc[e][u];
        const f32x4 nm = sp4(-mu[u]);
        const f32x4 h0 = fma4(nm, g0, f32x4{a[0], a[1], a[2], a[3]}) * fma4(nm, u0, f32x4{a[8], a[9], a[10], a[11]}) * r2[u];
        const f32x4 h1 = fma4(nm, g1, f32x4{a[4], a[5], a[6], a[7]}) * fma4(nm, u1, f32x4{a[12], a[13], a[14], a[15]}) * r2[u];
        st16c(v4u{pk2(h0.x, h0.y), pk2(h0.z, h0.w), pk2(h1.x, h1.y), pk2(h1.z, h1.w)}, rh, so + u * 2048);
      }
    }
  } else {
    const int c = lane & 15, q = lane >> 4, G = w >> 1, blk = w & 1;
    const unsigned xa = (128 * G + c) * 128 + ((q ^ (c >> 1)) * 16);
    const unsigned wa = 32768 + (32 * blk + c) * 128 + ((q ^ (c >> 1)) * 16);
    f32x4 acc[8][4] = {};
#pragma unroll
    for (int j = 0; j < 8; ++j)
#pragma unroll
      for (int t = 0; t < 4; ++t) PIN4(acc[j][t]);
    v4u fw[2][4], fx[4];
    lp px[2], pw[2];
    auto setp = [&](unsigned sb) { px[0] = LP(sb + xa); px[1] = LP(sb + (xa ^ 64)); pw[0] = LP(sb + wa); pw[1] = LP(sb + (wa ^ 64)); };
    auto woff = [&](int t) { return (t >> 1) * 8192 + (t & 1) * 2048; };
    setp(0);
    SB0();
    tag = row_stats(p, id, w, lane, rin);
    STAMP(17);
    SB0();
    BAR();
    STAMP(1);
#pragma unroll
    for (int t = 0; t < 4; ++t) fw[0][t] = l128(pw[0] + woff(t));
#pragma unroll
    for (int n = 0; n < 3; ++n) fx[n] = l128(px[0] + n * 2048);
    unsigned sb = 0;
#pragma unroll 1
    for (int i = 0; i < 10; ++i) {
#pragma unroll
      for (int n = 0; n < 16; ++n) {
        const int kk = n >> 3, j = n & 7, nn = (n + 3) & 15;
        if (n == 13) { if (i < 9) BAR(); LSTAMP(i); sb = sb == 2 * STG ? 0 : sb + STG; setp(sb); SB0(); }
        const v4u xf = fx[n & 3];
        acc[j][0] = mfma16(fw[kk][0], xf, acc[j][0]); SB0();
        fx[(n + 3) & 3] = l128(px[nn >> 3] + (nn & 7) * 2048); SB0();
        acc[j][1] = mfma16(fw[kk][1], xf, acc[j][1]); SB0();
        if (n >= 2 && n < 6) fw[1][n - 2] = l128(pw[1] + woff(n - 2));
        if (n == 13) fw[0][0] = l128(pw[0] + woff(0));
        if (n == 14) fw[0][2] = l128(pw[0] + woff(2));
        SB0();
        acc[j][2] = mfma16(fw[kk][2], xf, acc[j][2]); SB0();
        if (n == 13) fw[0][1] = l128(pw[0] + woff(1));
        if (n == 14) fw[0][3] = l128(pw[0] + woff(3));
        SB0();
        acc[j][3] = mfma16(fw[kk][3], xf, acc[j][3]); SB0();
        if (n == 14) { cf = lrd(L_CSF + (lane & 3) * 4); SB0(); }
      }
    }
    lwait(L_CSF + (lane & 3) * 4, cf, 1);
    STAMP(12);
    const lp pe = LP(L_E + (16 * G + c) * ESTR + (32 * blk + 8 * q) * 4);
    const lp pk = LP(L_CST + (32 * blk + 8 * q) * 4);
    f32x4 lr[2], li[2], er[2], ei[2];
    {
      const lp ps = LP(L_STATS + (128 * G + 8 * c) * 8), pc = LP(L_CS + (32 * blk + 8 * q) * 4);
      f32x4 cs[4], st[4];
#pragma unroll
      for (int t = 0; t < 4; ++t) { cs[t] = lf4(pc + (t >> 1) * 320 + (t & 1) * 16) + lf4(pc + 640 + (t >> 1) * 320 + (t & 1) * 16); st[t] = lf4(ps + t * 16); }
#pragma unroll
      for (int ab = 0; ab < 2; ++ab) { lr[ab] = lf4(pk + ab * 16); li[ab] = lf4(pk + 256 + ab * 16); }
#pragma unroll
      for (int jj = 0; jj < 4; ++jj)
#pragma unroll
        for (int t = 0; t < 4; ++t) {
          acc[2 * jj][t] = fma4(sp4(-st[jj].x), cs[t], acc[2 * jj][t]) * st[jj].y;
          acc[2 * jj + 1][t] = fma4(sp4(-st[jj].z), cs[t], acc[2 * jj + 1][t]) * st[jj].w;
        }
    }
#pragma unroll
    for (int ab = 0; ab < 2; ++ab) { er[ab] = acc[0][ab]; ei[ab] = acc[0][2 + ab]; }
#pragma unroll
    for (int j = 1; j < 8; ++j)
#pragma unroll
      for (int ab = 0; ab < 2; ++ab) cmac(er[ab], ei[ab], lr[ab], li[ab], acc[j][ab], acc[j][2 + ab]);
#pragma unroll
    for (int ab = 0; ab < 2; ++ab) { sf4(pe + ab * 16, er[ab]); sf4(pe + 256 + ab * 16, ei[ab]); }
    SB0();
    BAR();
    STAMP(13);
#pragma unroll
    for (int ab = 0; ab < 2; ++ab) {
      const f32x4 kr = lf4(pk + 512 * 4 + ab * 16), ki = lf4(pk + 512 * 4 + 256 + ab * 16);
#pragma unroll
      for (int j = 0; j < 8; ++j) {
        const f32x4 ur = acc[j][ab], ui = acc[j][2 + ab];
        acc[j][ab] = fma4(-ki, ui, kr * ur); acc[j][2 + ab] = fma4(-ki, ur, -kr * ui);
        PIN4(acc[j][ab]); PIN4(acc[j][2 + ab]);
      }
      li[ab] = -li[ab];
    }
    const __amdgpu_buffer_rsrc_t rh = mkrs(p.ws + WS_H + (unsigned)(4 * id.R + 2 * G) * (NKB * 4096), 2 * NKB * 4096);
    unsigned vo[4];
#pragma unroll
    for (int z = 0; z < 4; ++z) { vo[z] = (54 + 2 * id.g + blk + 20 * (z & 1) + NKB * (z >> 1)) * 4096 + lane * 16; PIN4(vo[z]); }
    SB0();
    BAR();
    STAMP(14);
#pragma unroll
    for (int ab = 0; ab < 2; ++ab) { er[ab] = lf4(pe + ab * 16); ei[ab] = lf4(pe + 256 + ab * 16); }
#pragma unroll
    for (int j = 0; j < 8; ++j) {
#pragma unroll
      for (int ab = 0; ab < 2; ++ab) cmac(er[ab], ei[ab], lr[ab], li[ab], acc[j][ab], acc[j][2 + ab]);
      st16c(v4u{pk2(er[0].x, er[0].y), pk2(er[0].z, er[0].w), pk2(er[1].x, er[1].y), pk2(er[1].z, er[1].w)}, rh, vo[2 * (j >> 2)] + (j & 3) * 1024);
      st16c(v4u{pk2(ei[0].x, ei[0].y), pk2(ei[0].z, ei[0].w), pk2(ei[1].x, ei[1].y), pk2(ei[1].z, ei[1].w)}, rh, vo[2 * (j >> 2) + 1] + (j & 3) * 1024);
    }
  }
  SB0();
  STAMP(15);
  return tag;
}

#define HBAR() do { SB0(); BAR(); SB0(); } while (0)
struct HState { f32x4 acs[5]; unsigned pl, pr, pi; };
template <bool SSM>
DI unsigned helper_wave(const Par& p, const Ids& id, const int h, const int lane, HState& hs) {
  STAMP(0);
  constexpr int NWP = SSM ? 4 : 5, NP = 8 + NWP;
  constexpr unsigned WSTEP = SSM ? 128u : 64u * 3456u;
  const int c = lane & 15, q = lane >> 4;
  const __amdgpu_buffer_rsrc_t rctl = mkrs(p.ws, WS_CTL_BYTES);
  const __amdgpu_buffer_rsrc_t rx = mkrs(p.x, L_ * D_ * 2);
  const __amdgpu_buffer_rsrc_t rw = mkrs(SSM ? ((h >> 1) ? p.bim : p.bre) : ((h >> 1) ? p.wu : p.wg), SSM ? P_ * D_ * 2 : D_ * F_ * 2);
  const unsigned v0 = (8 * (lane >> 3)) * 1280 + (((lane & 7) ^ (lane >> 4)) * 16), v1 = v0 ^ 64;
#define ROT 0
  unsigned st = ROT == 0 ? 0 : ROT == 1 ? 5 * ((blockIdx.x >> 2) & 1) : ROT == 2 ? (5 * (blockIdx.x & 7)) >> 2 : 5 * (((blockIdx.x >> 2) ^ blockIdx.x) & 1);
  const unsigned sx = (256 * id.R + 128 * (h >> 1) + 4 * (h & 1)) * 1280;
  auto issue_x = [&](unsigned s, unsigned sb) {
    dma_x(MkSeq<8>::T{}, rx, sb + h * 8192, v0, v1, s);
  };
  asm volatile("s_nop 4");
  issue_x(sx + st * 128, 0);
  unsigned vw[NWP];
  if constexpr (SSM) {
    vw[0] = (8 * (lane >> 5) + ((lane >> 3) & 3)) * 1280 + (((lane & 7) ^ (lane >> 4)) * 16); vw[1] = vw[0] ^ 64;
  } else {
#pragma unroll
    for (int i = 0; i < 5; ++i) { const int lc = 64 * (5 * (h & 1) + i) + lane, pos = lc / 10; vw[i] = pos * 3456 + (lc - pos * 10) * 16; }
  }
  const unsigned sw = SSM ? (64 * id.g + 32 * (h & 1)) * 1280 : 160 * id.g;
  const unsigned dw = 32768 + (h >> 1) * (SSM ? 8192 : UPO) + (h & 1) * NWP * 1024;
  auto issue_w = [&](unsigned s, unsigned sb) {
    if constexpr (SSM) dma_b(MkSeq<4>::T{}, rw, sb + dw, vw, s); else dma_g(MkSeq<5>::T{}, rw, sb + dw, vw, s);
  };
  issue_w(sw + st * WSTEP, 0);
  unsigned sb = STG;
  auto step = [&]() {
    st = st == 9 ? 0 : st + 1;
    issue_x(sx + st * 128, sb); issue_w(sw + st * WSTEP, sb);
    sb = sb == 2 * STG ? 0 : sb + STG;
  };
  step();
  if (h == 3 && lane < 8) *(LAS unsigned*)LP(L_CNT + lane * 4) = 0u;
  f32x4 acs[NWP] = {};
  v4u fc[NWP];
  const unsigned ca = SSM ? 32768 + (h >> 1) * 8192 + c * 128 + (((4 * (h & 1) + q) ^ (c >> 1)) * 16)
                          : 32768 + (h >> 1) * UPO + (32 * (h & 1) + 4 * q + (c >> 2)) * 160 + (c & 3) * 8;
  const v4u ones = {0x3F803F80u, 0x3F803F80u, 0x3F803F80u, 0x3F803F80u};
  auto cs_rd = [&](unsigned s) {
    const lp a = LP(s + ca);
#pragma unroll
    for (int t = 0; t < NWP; ++t) { if constexpr (SSM) fc[t] = l128(a + t * 2048); else fc[t] = cat(ltr(a + t * 32), ltr(a + t * 32 + 2560)); }
    SB0();
  };
  auto cs_mm = [&]() {
    SB0();
    __builtin_amdgcn_s_setprio(2);
#pragma unroll
    for (int t = 0; t < NWP; ++t) acs[t] = mfma16(fc[t], ones, acs[t]);
    __builtin_amdgcn_s_setprio(0);
    SB0();
  };
  constexpr int NF = SSM ? 5 : 4;
#define JL 1
  const int tsk = (id.g * 8 + id.R) * 4 + h, kb = SSM ? 4 * id.g + h : tsk / 10, dq = tsk - 10 * (tsk / 10);
  v4u f[5] = {}, sel[2], sv = {}, fl = {};
  unsigned fvo, fso[NF];
  const __amdgpu_buffer_rsrc_t rf = mkrs(SSM ? (kb < 20 ? p.cre : p.cim) : p.wd, SSM ? D_ * P_ * 2 : F_ * D_ * 2);
  const __amdgpu_buffer_rsrc_t r2 = mkrs(p.ws + WS_W2, (SSM || tsk < 540) ? WS_W2_BYTES : 0);
  {
    const int cc = lane >> 2, qq = lane & 3;
    fvo = SSM ? (80 * id.R + cc) * 1280 + (kb < 20 ? kb : kb - 20) * 64 + qq * 16
              : (32 * kb + 16 * (cc >> 3) + 4 * ((cc >> 2) & 1) + (cc & 3)) * 1280 + dq * 128 + qq * 16;
#pragma unroll
    for (int i = 0; i < NF; ++i) {
      const int dt = 4 * dq + i, ct = dt / 5;
      fso[i] = (SSM ? (id.R * NKB + 54 + kb) * 5 + i : (ct * NKB + kb) * 5 + dt - 5 * ct) * 1024 + lane * 16;
      PIN4(fso[i]);
    }
#pragma unroll
    for (int hf = 0; hf < 2; ++hf) {
#pragma unroll
      for (int e = 0; e < 4; ++e) sel[hf][e] = (8 * q + 2 * e == 16 * hf + c ? 0x3F80u : 0u) | (8 * q + 2 * e + 1 == 16 * hf + c ? 0x3F800000u : 0u);
      if constexpr (!SSM) PIN4(sel[hf]);
    }
    PIN4(fvo);
  }
  unsigned epoch, pl = 0, pr = 0, pi = 0, tag = 0;
  unsigned so = WS_STATS + (256 * id.R + 64 * h + lane) * 16, flo = h == 3 ? WS_FLW + lane * 16 : OOB, fwo = (h == 3 && lane == 0) ? WS_FLW + blockIdx.x * 4 : OOB;
  PIN4(so); PIN4(flo); PIN4(fwo);
  asm volatile("buffer_load_dword %0, off, %1, 0 sc1" : "=v"(epoch) : "s"(rctl));
  if constexpr (SSM) {
    const unsigned po = (64 * id.g + lane) * 2;
    asm volatile("buffer_load_ushort %0, %3, %4, 0 offen\n\tbuffer_load_ushort %1, %3, %5, 0 offen\n\tbuffer_load_ushort %2, %3, %6, 0 offen"
                 : "=&v"(pl), "=&v"(pr), "=&v"(pi) : "v"(po), "s"(mkrs(p.ldt, P_ * 2)), "s"(mkrs(p.lre, P_ * 2)), "s"(mkrs(p.lim, P_ * 2)));
  }
  WAITV(NP + (SSM ? 4 : 1)); BAR();
#pragma unroll
  for (int j = 0; j < 8; ++j) {
    cs_rd((j % 3) * STG);
    if (j == JL + 4) { tag = mktag(epoch); st4c(tag, rctl, fwo); }
    if (j == 6) asm volatile("buffer_load_dwordx4 %0, %1, %2, 0 offen sc1" : "=v"(sv) : "v"(so), "s"(rctl));
    if (!SSM && j == 7) asm volatile("buffer_load_dwordx4 %0, %1, %2, 0 offen sc1" : "=v"(fl) : "v"(flo), "s"(rctl));
    SB0();
    step();
    if (j == JL) {
      if constexpr (SSM)
        asm volatile("buffer_load_dwordx4 %0, %5, %6, 0 offen\n\tbuffer_load_dwordx4 %1, %5, %6, %7 offen\n\tbuffer_load_dwordx4 %2, %5, %6, %8 offen\n\tbuffer_load_dwordx4 %3, %5, %6, %9 offen\n\tbuffer_load_dwordx4 %4, %5, %6, %10 offen"
                     : "=&v"(f[0]), "=&v"(f[1]), "=&v"(f[2]), "=&v"(f[3]), "=&v"(f[4]) : "v"(fvo), "s"(rf), "s"(16 * 1280), "s"(32 * 1280), "s"(48 * 1280), "s"(64 * 1280));
      else
        asm volatile("buffer_load_dwordx4 %0, %4, %5, 0 offen\n\tbuffer_load_dwordx4 %2, %4, %5, 0 offen offset:64\n\tbuffer_load_dwordx4 %1, %4, %5, %6 offen\n\tbuffer_load_dwordx4 %3, %4, %5, %6 offen offset:64"
                     : "=&v"(f[0]), "=&v"(f[1]), "=&v"(f[2]), "=&v"(f[3]) : "v"(fvo), "s"(rf), "s"(8 * 1280));
    }
    cs_mm();
    if (j == JL + 2) {
      v4u g[NF];
#pragma unroll
      for (int i = 0; i < NF; ++i)
#pragma unroll
        for (int e = 0; e < 4; ++e) g[i][e] = __builtin_amdgcn_ds_bpermute((4 * c + q) * 4, f[i][e]);
      __builtin_amdgcn_s_setprio(2);
#pragma unroll
      for (int i = 0; i < NF; ++i) {
        if constexpr (SSM) st16c(g[i], r2, fso[i]);
        else {
          const f32x4 a = mfma16(g[i & ~1], sel[i & 1], f32x4{0.f, 0.f, 0.f, 0.f}), b = mfma16(g[i | 1], sel[i & 1], f32x4{0.f, 0.f, 0.f, 0.f});
          st16c(v4u{pk2(a.x, a.y), pk2(a.z, a.w), pk2(b.x, b.y), pk2(b.z, b.w)}, r2, fso[i]);
        }
      }
      __builtin_amdgcn_s_setprio(0);
      SB0();
    }
    if (j == JL || j == JL + 2) WAITV(NP + NF);
    else if (j == JL + 1) asm volatile("s_waitcnt vmcnt(%9)" : "+v"(f[0]), "+v"(f[1]), "+v"(f[2]), "+v"(f[3]), "+v"(f[NF - 1 + (SSM ? 0 : 1)]), "+v"(epoch), "+v"(pl), "+v"(pr), "+v"(pi) : "i"(NP) : "memory");
    else if (j == 6) asm volatile("s_waitcnt vmcnt(%1)" : "+v"(sv) : "i"(NP) : "memory");
    else if (j == 7) asm volatile("s_waitcnt vmcnt(%1)" : "+v"(fl) : "i"(NP) : "memory");
    else if (j == JL + 4) WAITV(NP + 1);
    else WAITV(NP);
    if (j == 6) {
      v2u o = {sv.x, sv.z};
      if (!__all((sv.y == tag) & (sv.w == tag))) {
        _Pragma("unroll 1") for (int spin = 0; spin < SPIN; ++spin) { FENCE();
          const v4u s2 = ld16c(rctl, so, 0);
          o = v2u{s2.x, s2.z};
          if (__all((s2.y == tag) & (s2.w == tag))) break;
          __builtin_amdgcn_s_sleep(4);
        }
      }
      *(LAS v2u*)LP(L_STATS + (64 * h + lane) * 8) = o;
    }
    if (!SSM && j == 7 && h == 3) {
      if (!__all((fl.x == tag) & (fl.y == tag) & (fl.z == tag) & (fl.w == tag))) {
        _Pragma("unroll 1") for (int spin = 0; spin < SPIN; ++spin) { FENCE();
          const v4u f2 = ld16c(rctl, flo, 0);
          if (__all((f2.x == tag) & (f2.y == tag) & (f2.z == tag) & (f2.w == tag))) break;
          __builtin_amdgcn_s_sleep(4);
        }
      }
      st4c(tag, rctl, (lane == 0 && blockIdx.x == 255) ? WS_EPOCH : OOB);
    }
    BAR();
  }
#pragma unroll
  for (int t = 0; t < 5; ++t) hs.acs[t] = acs[t < NWP ? t : 0];
  hs.pl = pl; hs.pr = pr; hs.pi = pi;
  return tag;
}
template <bool SSM>
DI void helper_tail(const Par& p, const Ids& id, const int h, const int lane, const unsigned tag, HState& hs) {
  constexpr int NWP = SSM ? 4 : 5;
  const int c = lane & 15, q = lane >> 4;
  const __amdgpu_buffer_rsrc_t rctl = mkrs(p.ws, WS_CTL_BYTES);
  f32x4* acs = hs.acs;
  const unsigned pl = hs.pl, pr = hs.pr, pi = hs.pi;
  v4u fc[NWP];
  const unsigned ca = SSM ? 32768 + (h >> 1) * 8192 + c * 128 + (((4 * (h & 1) + q) ^ (c >> 1)) * 16)
                          : 32768 + (h >> 1) * UPO + (32 * (h & 1) + 4 * q + (c >> 2)) * 160 + (c & 3) * 8;
  const v4u ones = {0x3F803F80u, 0x3F803F80u, 0x3F803F80u, 0x3F803F80u};
  auto cs_rd = [&](unsigned s) {
    const lp a = LP(s + ca);
#pragma unroll
    for (int t = 0; t < NWP; ++t) { if constexpr (SSM) fc[t] = l128(a + t * 2048); else fc[t] = cat(ltr(a + t * 32), ltr(a + t * 32 + 2560)); }
    SB0();
  };
  auto cs_mm = [&]() {
    SB0();
    __builtin_amdgcn_s_setprio(2);
#pragma unroll
    for (int t = 0; t < NWP; ++t) acs[t] = mfma16(fc[t], ones, acs[t]);
    __builtin_amdgcn_s_setprio(0);
    SB0();
  };
  cs_rd(2 * STG);
  if (SSM) {
    if (h == 3) {
      _Pragma("unroll 1") for (int spin = 0; spin < SPIN; ++spin) { FENCE();
        const v4u f2 = ld16c(rctl, WS_FLW + lane * 16, 0);
        if (__all((f2.x == tag) & (f2.y == tag) & (f2.z == tag) & (f2.w == tag))) break;
        __builtin_amdgcn_s_sleep(4);
      }
    }
  }
  float br = 0.f, bi = 0.f;
  if (SSM && h == 2) {
    const float dt = rbf(expf(bf2f(pl))), ex = expf(dt * bf2f(pr));
    float sn, cn;
    sincosf(dt * bf2f(pi), &sn, &cn);
    br = ex * cn; bi = ex * sn;
  }
  cs_mm();
  WAITV(SSM ? 0 : 15); BAR();
  cs_rd(0);
  if (SSM && h == 2) {
    const float lr = bf2f(pr), li = bf2f(pi), nr = br - 1.f, ni = bi, den = lr * lr + li * li;
    LAS float* cst = (LAS float*)LP(L_CST);
    cst[4 * 128 + lane] = 2.f * (nr * lr + ni * li) / den; cst[4 * 128 + 64 + lane] = 2.f * (ni * lr - nr * li) / den;
    cst[lane] = br; cst[64 + lane] = bi;
#pragma unroll
    for (int s = 1; s <= 8; ++s) {
      const float a = br * br - bi * bi, b = 2.f * br * bi;
      br = a; bi = b;
      const int k = s == 3 ? 1 : s == 6 ? 2 : s == 8 ? 3 : 0;
      if (k) { cst[k * 128 + lane] = br; cst[k * 128 + 64 + lane] = bi; }
    }
  }
  cs_mm();
  if (c == 0) {
#pragma unroll
    for (int t = 0; t < NWP; ++t) {
      const int col = SSM ? 32 * (t >> 1) + 8 * q + 4 * (t & 1) : 16 * t + 4 * q;
      sf4(LP(L_CS + ((h & 1) * 160 + (h >> 1) * 80 + col) * 4), acs[t]);
    }
  }
  sig(L_CSF + h * 4, 1);
}

DI void carries(const Par& p, const Ids& id, const int h, const int lane, const unsigned tag, WRing& wr) {
  const __amdgpu_buffer_rsrc_t rctl = mkrs(p.ws, WS_CTL_BYTES);
  const int c = lane & 15, q = lane >> 4;
  {
    const int ch = 16 * h + c;
    lwait<2>(L_CSF + 8, 0, 1);
    LAS float* cst = (LAS float*)LP(L_CST);
    const float l8r = cst[128 + ch], l8i = cst[192 + ch], l64r = cst[256 + ch], l64i = cst[320 + ch], lwr = cst[384 + ch], lwi = cst[448 + ch];
    const float kr = cst[512 + ch], ki = cst[576 + ch];
    float w0r = 1.f, w0i = 0.f, w1r = 1.f, w1i = 0.f, qr = kr, qi = ki;
#pragma unroll
    for (int s = 0; s < 6; ++s) {
      float a = w0r, b = w0i; cm1(a, b, lwr, lwi, 0.f, 0.f);
      if (s < id.R - 1 - q) { w0r = a; w0i = b; }
      if (s < id.R - 5 - q) { w1r = a; w1i = b; }
    }
#pragma unroll
    for (int s = 0; s < 3; ++s) { float a = qr, b = qi; cm1(a, b, l64r, l64i, 0.f, 0.f); if (s < q) { qr = a; qi = b; } }
    const __amdgpu_buffer_rsrc_t rag = mkrs(p.ws + WS_AGG + id.R * (8 * 640 * 16), id.R * 640 * 16);
    LAS float* pe = (LAS float*)LP(L_E + 8 * q * ESTR + ch * 4);
    asm volatile("" : "+v"(w0r), "+v"(w0i), "+v"(w1r), "+v"(w1i), "+v"(qr), "+v"(qi));
    unsigned ao[2], po = (q * 640 + 64 * id.g + ch) * 16;
#pragma unroll
    for (int z = 0; z < 2; ++z) ao[z] = q + 4 * z > id.R ? WS_AGG + (((q + 4 * z) * 8 + id.R) * 640 + 64 * id.g + ch) * 16 : OOB;
    asm volatile("" : "+v"(ao[0]), "+v"(ao[1]), "+v"(po));
    __builtin_amdgcn_s_setprio(3);
    HBAR();
    float er[8], ei[8];
#pragma unroll
    for (int s = 0; s < 8; ++s) { er[s] = pe[s * (ESTR / 4)]; ei[s] = pe[s * (ESTR / 4) + 64]; }
    float a = er[0], b = ei[0];
#pragma unroll
    for (int s = 1; s < 8; ++s) cm1(a, b, l8r, l8i, er[s], ei[s]);
    float ta[4], tb[4];
    gather4(a, ta); gather4(b, tb);
    float pa = 0.f, pb = 0.f;
    a = ta[0]; b = tb[0];
#pragma unroll
    for (int s = 1; s < 4; ++s) {
      if (q == s) { pa = a; pb = b; }
      cm1(a, b, l64r, l64i, ta[s], tb[s]);
    }
#pragma unroll
    for (int z = 0; z < 2; ++z)
      st16c(v4u{__float_as_uint(a), tag, __float_as_uint(b), tag}, rctl, ao[z]);
    SB0();
    wr.dma01();
    v4u p0 = ld16c(rag, po, 0), p1 = ld16c(rag, po, 4 * 640 * 16);
    SB0();
    cm1(pa, pb, kr, ki, 0.f, 0.f);
#pragma unroll
    for (int s = 0; s < 7; ++s) cm1(er[s], ei[s], kr, ki, 0.f, 0.f);
    _Pragma("unroll 1") for (int spin = 0; spin < SPIN; ++spin) { FENCE();
      const bool ok = ((q >= id.R) | ((p0.y == tag) & (p0.w == tag))) & ((q + 4 >= id.R) | ((p1.y == tag) & (p1.w == tag)));
      if (__all(ok)) break;
      p0 = ld16c(rag, po, 0); p1 = ld16c(rag, po, 4 * 640 * 16);
    }
    float ca = __uint_as_float(p1.x), cb = __uint_as_float(p1.z);
    cm1(ca, cb, w1r, w1i, 0.f, 0.f);
    { float x = __uint_as_float(p0.x), y = __uint_as_float(p0.z); cm1(x, y, w0r, w0i, ca, cb); ca = x; cb = y; }
    gather4(ca, ta); gather4(cb, tb);
    ca = (ta[0] + ta[1]) + (ta[2] + ta[3]); cb = (tb[0] + tb[1]) + (tb[2] + tb[3]);
    cm1(ca, cb, qr, qi, pa, pb);
#pragma unroll
    for (int s = 0; s < 8; ++s) {
      pe[s * (ESTR / 4)] = ca; pe[s * (ESTR / 4) + 64] = -cb;
      if (s < 7) cm1(ca, cb, l8r, l8i, er[s], ei[s]);
    }
    __builtin_amdgcn_s_setprio(0);
    HBAR();
  }
}

__global__ __launch_bounds__(512, 2) void fused(Par p) {
  const int bid = blockIdx.x, xcd = bid & 7, idx = bid >> 3, a = xcd >> 2, b = xcd & 3, s = idx >> 2;
  Ids id;
  const bool ssm = idx < 10, part = idx >= 8 && idx < 12;
  id.R = 4 * a + (part ? 2 * (b & 1) + (idx & 1) : (idx & 3));
  id.g = ssm ? (part ? 8 + (b >> 1) : 2 * b + s) : (part ? 20 + (b >> 1) : 5 * b + s - 3);
  id.cg = ssm ? id.g : 10 + id.g;
  id.rt = 16 * a + 8 * (b >> 1) + (idx >> 2);
  id.ct = 4 * (b & 1) + (idx & 3);
  const int wv = rfl(threadIdx.x >> 6), lane = threadIdx.x & 63;
  unsigned cpf = 0;
  if (wv == 3) {
    const unsigned long long pc = __builtin_amdgcn_s_getpc() & ~127ull;
    cpf = __builtin_amdgcn_raw_buffer_load_b32(mkrs((const void*)pc, CODEPF), lane < 12 ? (idx * 12 + lane) * 128 : OOB, 0, 0);
  }
  const __amdgpu_buffer_rsrc_t rctl = mkrs(p.ws, WS_CTL_BYTES);
  const __amdgpu_buffer_rsrc_t rw = mkrs(p.ws + WS_W2 + (unsigned)id.ct * (NKB * 5120), NKB * 5120);
  const unsigned vl = lane * 16;
  const unsigned fo = WS_FLH + (id.rt * 8 + id.ct) * 128 + (lane & 31) * 4;
  if (wv == 2) cpf = ld4c(rctl, lane < 32 ? fo : lane < 40 ? WS_FLW + (lane - 32) * 128 : OOB);
  WRing wr;
  wr.rw = rw; wr.vl = vl; wr.wv = wv;
  unsigned tag;
  if (wv < 4) {
    tag = ssm ? compute_wave<true>(p, id, wv, lane) : compute_wave<false>(p, id, wv, lane);
    WAITV(0);
    unsigned old = 0;
    if (lane == 0) old = __hip_atomic_fetch_add((LAS unsigned*)LP(L_CNT), 1u, __ATOMIC_RELAXED, __HIP_MEMORY_SCOPE_WORKGROUP);
    st4c(tag, rctl, (lane < 32 && rfl(old) == 3) ? WS_FLH + (id.R * 32 + lane) * 128 + id.cg * 4 : OOB);
    asm volatile("" ::"v"(cpf));
    STAMP(16);
#pragma unroll
    for (int it = NL; it < NL + DW; ++it) wr.load(it);
  } else {
    HState hs;
    tag = ssm ? helper_wave<true>(p, id, wv - 4, lane, hs) : helper_wave<false>(p, id, wv - 4, lane, hs);
    if (ssm) helper_tail<true>(p, id, wv - 4, lane, tag, hs);
    wr.load(NL); wr.load(NL + 1); wr.load(NL + 2);
    if (!ssm) helper_tail<false>(p, id, wv - 4, lane, tag, hs);
#pragma unroll
    for (int it = NL + 3; it < NL + DW; ++it) wr.load(it);
    if (ssm) carries(p, id, wv - 4, lane, tag, wr); else wr.dma01();
    lwait<2>(L_CNT, 0, 4);
    STAMP(16);
    if (wv > 4) {
      asm volatile("s_nop 4");
      dma_lin(MkSeq<14>::T{}, rw, (wv - 5) * 13312, vl, 81920 + (wv - 5) * 13312);
    } else {
      _Pragma("unroll 1") for (int spin = 0; spin < SPIN; ++spin) { FENCE();
        if (__all((ld4c(rctl, fo) == tag) | ((lane & 31) < 10))) break;
        __builtin_amdgcn_s_sleep(2);
      }
      sig(L_GO, 1);
    }
  }

  const __amdgpu_buffer_rsrc_t rh = mkrs(p.ws + WS_H + (unsigned)id.rt * (NKB * 4096), NKB * 4096);
  v4u hb[DH][4];
  auto loadh = [&](int it) {
#pragma unroll
    for (int j = 0; j < 4; ++j) { hb[it % DH][j] = ld16c(rh, vl + j * 1024, (8 * it + wv) * 4096); SB0(); }
  };
  lwait<1>(L_GO, 0, 1);
  SB0();
  STAMP(20);
#pragma unroll
  for (int it = 0; it < DH; ++it) { loadh(it); SB0(); }
  f32x4 acc[4][5];
#pragma unroll
  for (int j = 0; j < 4; ++j)
#pragma unroll
    for (int i = 0; i < 5; ++i) acc[j][i] = f32x4{0.f, 0.f, 0.f, 0.f};
  const int tid = wv * 64 + lane;
  v2u xv[3], dv[3]; v4u sv[3]; int oo[3], ra[3];
  unsigned sfl = 0;
  const lp pw = LP(L_W + wv * 5120 + vl), pw2 = LP(wv * 5120 + vl);
  SB0();
  if (wv > 4) {
    WAITV(4 * DH);
    if (lane == 0) __hip_atomic_fetch_add((LAS unsigned*)LP(L_RDY), 1u, __ATOMIC_RELAXED, __HIP_MEMORY_SCOPE_WORKGROUP);
  }
  lwait<1>(L_RDY, 0, 3);
  v4u wl = l128(pw);
  SB0();
#pragma unroll
  for (int it = 0; it < NIT; ++it) {
    if (it < NL) {
#pragma unroll
      for (int i = 0; i < 5; ++i) {
        const v4u wf = wl;
        if (5 * it + i + 1 < 5 * NL) wl = l128((5 * it + i + 1) / 5 == 2 ? pw2 + ((5 * it + i + 1) % 5) * 1024 : pw + ((5 * it + i + 1) / 5) * 40960 + ((5 * it + i + 1) % 5) * 1024);
#pragma unroll
        for (int j = 0; j < 4; ++j) acc[j][i] = mfma16(wf, hb[it % DH][j], acc[j][i]);
        SB0();
      }
    } else {
#pragma unroll
      for (int j = 0; j < 4; ++j)
#pragma unroll
        for (int i = 0; i < 5; ++i) acc[j][i] = mfma16(wr.wb[it % DW][i], hb[it % DH][j], acc[j][i]);
    }
    SB0();
    if (it + DH == 6) {
      if (__builtin_expect(!__all(sfl == tag), 0)) {
        _Pragma("unroll 1") for (int spin = 0; spin < SPIN; ++spin) { FENCE();
          if (__all(ld4c(rctl, fo) == tag)) break;
          __builtin_amdgcn_s_sleep(2);
        }
      }
      FENCE();
      STAMP(18); STAMPC(28);
    }
    if (it == 0) sfl = ld4c(rctl, fo);
    if (it + DH < NIT) loadh(it + DH);
    if (it >= NL && it + DW < NIT) wr.load(it + DW);
    if (it == NIT - DH) {
#pragma unroll
      for (int r = 0; r < 3; ++r) {
        const int sl = min(tid + 512 * r, 1279), row = sl / 20, qd = sl - 20 * row;
        const int tok = 128 * (id.rt >> 1) + 8 * (row >> 2) + 4 * (id.rt & 1) + (row & 3), d = 80 * id.ct + 4 * qd;
        oo[r] = tok * D_ + d;
        ra[r] = ((row & 3) * 5 + (qd >> 2)) * 1024 + (qd & 3) * 256 + (((row >> 2) + qd) & 15) * 16;
        xv[r] = *(const v2u*)(p.x + oo[r]); dv[r] = *(const v2u*)(p.dsk + d);
        sv[r] = ld16c(rctl, WS_STATS + tok * 16, 0);
      }
    }
#define BARIT 2
    if (it == BARIT) BAR();
    SB0();
  }
  STAMP(21); STAMPC(29);
#pragma unroll
  for (int i = 0; i < 5; ++i) {
    const lp rb = LP(wv * 20480 + (lane >> 4) * 256 + (((lane & 15) + 4 * i + (lane >> 4)) & 15) * 16);
#pragma unroll
    for (int j = 0; j < 4; ++j) sf4(rb + (j * 5 + i) * 1024, acc[j][i]);
  }
  BAR();
  STAMP(19);
#pragma unroll
  for (int r = 0; r < 3; ++r) {
    if (r < 2 || wv < 4) {
      const lp q0 = LP(ra[r]), q1 = LP(ra[r] + 4 * 20480);
      f32x4 sum = ((lf4(q0) + lf4(q0 + 20480)) + (lf4(q0 + 40960) + lf4(q0 + 61440))) + ((lf4(q1) + lf4(q1 + 20480)) + (lf4(q1 + 40960) + lf4(q1 + 61440)));
      const f32x4 xf = {bflo(xv[r].x), bfhi(xv[r].x), bflo(xv[r].y), bfhi(xv[r].y)};
      const f32x4 df = {bflo(dv[r].x), bfhi(dv[r].x), bflo(dv[r].y), bfhi(dv[r].y)};
      sum += xf + (xf - __uint_as_float(sv[r].x)) * __uint_as_float(sv[r].z) * df;
#define OUTAUX 16
      __builtin_amdgcn_raw_buffer_store_b128(__builtin_bit_cast(v4u, sum), mkrs(p.out, L_ * D_ * 4), oo[r] * 4, 0, OUTAUX);
    }
  }
  STAMP(22);
}

extern "C" void kernel_launch(void* const* d_in, const int* in_sizes, int n_in, void* d_out, int out_size, void* d_ws, size_t ws_size, hipStream_t stream) {
  Par p;
  p.x = (const u16*)d_in[0]; p.lre = (const u16*)d_in[1]; p.lim = (const u16*)d_in[2];
  p.bre = (const u16*)d_in[3]; p.bim = (const u16*)d_in[4]; p.cre = (const u16*)d_in[5]; p.cim = (const u16*)d_in[6];
  p.ldt = (const u16*)d_in[7]; p.dsk = (const u16*)d_in[8];
  p.wg = (const u16*)d_in[10]; p.wu = (const u16*)d_in[11]; p.wd = (const u16*)d_in[12];
#define WSOFF 0
  p.ws = (unsigned char*)d_ws + (WSOFF == 1 ? ((ws_size - (21u << 20)) & ~(size_t)0xFFFFF) : WSOFF == 2 ? (ws_size / 2) : 0); p.out = (float*)d_out;
  (void)hipFuncSetAttribute((const void*)fused, hipFuncAttributeMaxDynamicSharedMemorySize, LDS1);
  fused<<<256, 512, LDS1, stream>>>(p);
}
```

```cpp
#include <hip/hip_runtime.h>
#include <stdint.h>

typedef unsigned short u16;
typedef unsigned v4u __attribute__((ext_vector_type(4)));
typedef unsigned v2u __attribute__((ext_vector_type(2)));
typedef float f32x4 __attribute__((ext_vector_type(4)));
typedef float f32x2 __attribute__((ext_vector_type(2)));
typedef float f32x16 __attribute__((ext_vector_type(16)));
typedef short bf16x8 __attribute__((ext_vector_type(8)));
typedef short s16x4 __attribute__((ext_vector_type(4)));
typedef __bf16 bf2 __attribute__((ext_vector_type(2)));
#define LAS __attribute__((address_space(3)))
#define DI __device__ __forceinline__
typedef LAS unsigned char* lp;

constexpr int L_ = 2048, D_ = 640, P_ = 640, F_ = 1728;
constexpr int NKB = 94;
constexpr unsigned WS_EPOCH = 0;
constexpr unsigned WS_FLW = 2048;
constexpr unsigned WS_FLH = 4096;
constexpr unsigned WS_AGG = WS_FLH + 32768;
constexpr unsigned WS_STATS = WS_AGG + 64 * 640 * 16;
constexpr unsigned WS_CTL_BYTES = WS_STATS + 32768;
constexpr unsigned WS_W2 = 1u << 20;
constexpr unsigned WS_W2_BYTES = 8 * NKB * 5120;
constexpr unsigned WS_H = 8u << 20;
constexpr unsigned WS_H_BYTES = 32 * NKB * 4096;

constexpr int UPO = 10368;
constexpr int STG = 32768 + 20480 + 128;
constexpr int LDS1 = 163840;
constexpr int L_STATS = 3 * STG;
constexpr int L_CS = L_STATS + 2048;
constexpr int L_CNT = L_CS + 1280, L_GO = L_CNT + 4, L_RDY = L_CNT + 8, L_CSF = L_CNT + 16;
constexpr int L_CST = 49152;
constexpr int L_W = STG;
constexpr int L_E = 136000, ESTR = 528;
constexpr int SPIN = 1 << 18;
constexpr unsigned OOB = 0x40000000u;

DI float bf2f(unsigned b) { return __uint_as_float(b << 16); }
DI float bflo(unsigned w) { return __uint_as_float(w << 16); }
DI float bfhi(unsigned w) { return __uint_as_float(w & 0xffff0000u); }
DI unsigned pk2(float a, float b) { f32x2 v = {a, b}; return __builtin_bit_cast(unsigned, __builtin_convertvector(v, bf2)); }
DI float rbf(float a) { return bflo(pk2(a, a) & 0xffffu); }
DI f32x4 mfma16(v4u a, v4u b, f32x4 c) { return __builtin_amdgcn_mfma_f32_16x16x32_bf16(__builtin_bit_cast(bf16x8, a), __builtin_bit_cast(bf16x8, b), c, 0, 0, 0); }
DI f32x16 mfma32(v4u a, v4u b, f32x16 c) { return __builtin_amdgcn_mfma_f32_32x32x16_bf16(__builtin_bit_cast(bf16x8, a), __builtin_bit_cast(bf16x8, b), c, 0, 0, 0); }
DI unsigned rfl(unsigned v) { return __builtin_amdgcn_readfirstlane(v); }
DI __amdgpu_buffer_rsrc_t mkrs(const void* p, unsigned bytes) { return __builtin_amdgcn_make_buffer_rsrc((void*)p, 0, bytes, 0x00020000); }
template <int LI, int SI> DI void glds(__amdgpu_buffer_rsrc_t rs, unsigned lb, unsigned voff, unsigned sb) {
  unsigned t;
  asm volatile("s_add_u32 m0, %1, %5\n\ts_add_u32 %0, %4, %6\n\tbuffer_load_dwordx4 %2, %3, %0 offen lds" : "=&s"(t) : "s"(lb), "v"(voff), "s"(rs), "s"(sb), "i"(LI), "i"(SI) : "scc");
}
template <int LI, int SI> DI void gldsc(__amdgpu_buffer_rsrc_t rs, unsigned lb, unsigned voff, unsigned sb) {
  unsigned t;
  asm volatile("s_add_u32 m0, %1, %5\n\ts_add_u32 %0, %4, %6\n\tbuffer_load_dwordx4 %2, %3, %0 offen sc1 lds" : "=&s"(t) : "s"(lb), "v"(voff), "s"(rs), "s"(sb), "i"(LI), "i"(SI) : "scc");
}
template <int... I> struct Seq {};
template <int N, int... I> struct MkSeq : MkSeq<N - 1, N - 1, I...> {};
template <int... I> struct MkSeq<0, I...> { typedef Seq<I...> T; };
template <int... I> DI void dma_lin(Seq<I...>, __amdgpu_buffer_rsrc_t rs, unsigned lb, unsigned vl, unsigned sb) { (gldsc<I * 1024, I * 1024>(rs, lb, vl, sb), ...); }
template <int... I> DI void dma_x(Seq<I...>, __amdgpu_buffer_rsrc_t rs, unsigned lb, unsigned v0, unsigned v1, unsigned sb) { (glds<I * 1024, (64 * (I & 1) + (I >> 1)) * 1280>(rs, lb, (I & 1) ? v1 : v0, sb), ...); }
template <int... I> DI void dma_b(Seq<I...>, __amdgpu_buffer_rsrc_t rs, unsigned lb, const unsigned* v, unsigned sb) { (glds<I * 1024, (16 * (I & 1) + 4 * (I >> 1)) * 1280>(rs, lb, v[I & 1], sb), ...); }
template <int... I> DI void dma_g(Seq<I...>, __amdgpu_buffer_rsrc_t rs, unsigned lb, const unsigned* v, unsigned sb) { (glds<I * 1024, 0>(rs, lb, v[I], sb), ...); }
DI lp LP(unsigned a) { return (lp)(uintptr_t)a; }
DI v4u l128(lp a) { return *(LAS v4u*)a; }
DI f32x4 lf4(lp a) { return *(LAS f32x4*)a; }
DI void sf4(lp a, f32x4 v) { *(LAS f32x4*)a = v; }
DI s16x4 ltr(lp a) { return __builtin_amdgcn_ds_read_tr16_b64_v4i16((LAS s16x4*)a); }
DI v4u cat(s16x4 lo, s16x4 hi) { return __builtin_bit_cast(v4u, __builtin_shufflevector(lo, hi, 0, 1, 2, 3, 4, 5, 6, 7)); }
DI v4u ld16(__amdgpu_buffer_rsrc_t r, unsigned vo, unsigned so) { return __builtin_amdgcn_raw_buffer_load_b128(r, vo, so, 0); }
DI v4u ld16c(__amdgpu_buffer_rsrc_t r, unsigned vo, unsigned so) { return __builtin_amdgcn_raw_buffer_load_b128(r, vo, so, 16); }
DI unsigned ld4c(__amdgpu_buffer_rsrc_t r, unsigned vo) { return __builtin_amdgcn_raw_buffer_load_b32(r, vo, 0, 16); }
DI void st16c(v4u v, __amdgpu_buffer_rsrc_t r, unsigned vo) { __builtin_amdgcn_raw_buffer_store_b128(v, r, vo, 0, 16); }
DI void st4c(unsigned v, __amdgpu_buffer_rsrc_t r, unsigned vo) { __builtin_amdgcn_raw_buffer_store_b32(v, r, vo, 0, 16); }
#define BAR() asm volatile("s_waitcnt lgkmcnt(0)\n\ts_barrier" ::: "memory")
#define SB0() __builtin_amdgcn_sched_barrier(0)
#define WAITV(n) asm volatile("s_waitcnt vmcnt(%0)" ::"i"(n) : "memory")
#define FENCE() asm volatile("" ::: "memory")
template <int C> DI float dppf(float v) { return __int_as_float(__builtin_amdgcn_update_dpp(0, __float_as_int(v), C, 0xf, 0xf, true)); }
template <int N> DI f32x4 shr4(f32x4 v) { return f32x4{dppf<0x110 + N>(v.x), dppf<0x110 + N>(v.y), dppf<0x110 + N>(v.z), dppf<0x110 + N>(v.w)}; }
DI float sum8(float v) {
  v += dppf<0xB1>(v); v += dppf<0x4E>(v); v += dppf<0x141>(v);
  return v;
}
DI float dot2(unsigned a, unsigned b, float c) { return __builtin_amdgcn_fdot2_f32_bf16(__builtin_bit_cast(bf2, a), __builtin_bit_cast(bf2, b), c, false); }
DI void gather4(float x, float (&t)[4]) {
  const auto a = __builtin_amdgcn_permlane16_swap(__float_as_uint(x), __float_as_uint(x), false, false);
  const auto e = __builtin_amdgcn_permlane32_swap(a[0], a[0], false, false), o = __builtin_amdgcn_permlane32_swap(a[1], a[1], false, false);
  t[0] = __uint_as_float(e[0]); t[2] = __uint_as_float(e[1]); t[1] = __uint_as_float(o[0]); t[3] = __uint_as_float(o[1]);
}
DI void cm1(float& a, float& b, float lr, float li, float ur, float ui) {
  const float na = fmaf(lr, a, fmaf(-li, b, ur)), nb = fmaf(lr, b, fmaf(li, a, ui));
  a = na; b = nb;
}
DI f32x4 fma4(f32x4 a, f32x4 b, f32x4 c) { return __builtin_elementwise_fma(a, b, c); }
DI f32x4 sp4(float a) { return f32x4{a, a, a, a}; }
DI void cmac(f32x4& a, f32x4& b, f32x4 lr, f32x4 li, f32x4 ur, f32x4 ui) {
  const f32x4 na = fma4(lr, a, fma4(-li, b, ur)), nb = fma4(lr, b, fma4(li, a, ui));
  a = na; b = nb;
}

#define STAMP(i)
#define STAMPC(i)

#define LSTAMP(i)
struct Par {
  const u16 *x, *lre, *lim, *bre, *bim, *cre, *cim, *ldt, *dsk, *wg, *wu, *wd;
  unsigned char* ws;
  float* out;
};
struct Ids { int R, g, cg, rt, ct; };

#define PDW 5
#define PDH 2
constexpr int DW = PDW, DH = PDH, NIT = 12, NL = 3;
#define PIN4(x) asm volatile("" : "+v"(x))
DI void sig(unsigned addr, unsigned v) {
  asm volatile("s_mov_b64 exec, 1\n\tds_write_b32 %0, %1\n\ts_mov_b64 exec, -1" ::"v"(addr), "v"(v) : "memory");
}
DI unsigned lrd(unsigned addr) { return *(volatile LAS unsigned*)LP(addr); }
#define LSLEEP 1
template <int SL = 1> DI void lwait(unsigned addr, unsigned v, unsigned need) {
  if (__builtin_expect(!__all(v >= need), 0)) {
    _Pragma("unroll 1") for (int spin = 0; spin < 64 * SPIN; ++spin) { FENCE();
      if (__all(lrd(addr) >= need)) break;
      __builtin_amdgcn_s_sleep(SL);
    }
  }
  FENCE();
}

struct WRing {
  v4u wb[DW][5];
  __amdgpu_buffer_rsrc_t rw;
  unsigned vl, wv;
  DI void load(int it) {
#pragma unroll
    for (int i = 0; i < 5; ++i) { wb[it % DW][i] = ld16c(rw, vl + (i & 3) * 1024, (8 * it + wv) * 5120 + (i >> 2) * 4096); SB0(); }
  }
  DI void dma01() {
    asm volatile("s_nop 4");
    dma_lin(MkSeq<20>::T{}, rw, L_W + (wv - 4) * 20480, vl, (wv - 4) * 20480);
  }
};

DI float wsum(float v) {
  v = sum8(v); v += dppf<0x128>(v);
  float t[4]; gather4(v, t);
  return (t[0] + t[1]) + (t[2] + t[3]);
}
DI unsigned mktag(unsigned epoch) { return rfl(epoch) * 0x9E3779B1u + 0x7F4A7C15u; }
struct RowIn { v4u xa[2]; unsigned xb[2], epoch; };
template <bool SSM> DI void row_load(const Par& p, const Ids& id, const int w, const int lane, RowIn& in) {
  const int row = 256 * id.R + 8 * id.cg + 2 * w;
#pragma unroll
  for (int z = 0; z < 2; ++z) { in.xa[z] = *(const v4u*)(p.x + (row + z) * D_ + lane * 8); in.xb[z] = *(const unsigned*)(p.x + (row + z) * D_ + 512 + lane * 2); }
  in.epoch = ld4c(mkrs(p.ws, WS_CTL_BYTES), WS_EPOCH);
  SB0();
}
DI unsigned row_stats(const Par& p, const Ids& id, const int w, const int lane, const RowIn& in) {
  const __amdgpu_buffer_rsrc_t rctl = mkrs(p.ws, WS_CTL_BYTES);
  const int row = 256 * id.R + 8 * id.cg + 2 * w;
  const v4u* xa = in.xa; const unsigned* xb = in.xb; const unsigned epoch = in.epoch;
  float v[2][10], mean[2], s2[2];
#pragma unroll
  for (int z = 0; z < 2; ++z) {
    const float t[10] = {bflo(xa[z].x), bfhi(xa[z].x), bflo(xa[z].y), bfhi(xa[z].y), bflo(xa[z].z), bfhi(xa[z].z), bflo(xa[z].w), bfhi(xa[z].w), bflo(xb[z]), bfhi(xb[z])};
#pragma unroll
    for (int e = 0; e < 10; ++e) v[z][e] = t[e];
    mean[z] = wsum(((t[0] + t[1]) + (t[2] + t[3])) + ((t[4] + t[5]) + (t[6] + t[7])) + (t[8] + t[9])) * (1.f / D_);
  }
#pragma unroll
  for (int z = 0; z < 2; ++z) {
    float a = 0.f, b = 0.f;
#pragma unroll
    for (int e = 0; e < 5; ++e) { a = fmaf(v[z][e] - mean[z], v[z][e] - mean[z], a); b = fmaf(v[z][5 + e] - mean[z], v[z][5 + e] - mean[z], b); }
    s2[z] = wsum(a + b);
  }
  const unsigned tag = mktag(epoch);
  const float m = rbf((lane & 1) ? mean[1] : mean[0]), rs = 1.f / rbf(sqrtf(rbf(rbf(((lane & 1) ? s2[1] : s2[0]) * (1.f / D_)) + 1e-5f)));
  st16c(v4u{__float_as_uint(m), tag, __float_as_uint(rs), tag}, rctl, lane < 2 ? WS_STATS + (row + lane) * 16 : OOB);
  return tag;
}

template <bool SSM>
DI unsigned compute_wave(const Par& p, const Ids& id, const int w, const int lane) {
  STAMP(0);
  unsigned tag, cf = 0;
  RowIn rin;
  row_load<SSM>(p, id, w, lane, rin);
  if constexpr (!SSM) {
    const int r = lane & 31, hh = lane >> 5;
    const unsigned xa = (64 * w + r) * 128 + ((hh ^ ((r >> 1) & 7)) * 16);
    const unsigned wa = 32768 + ((lane >> 4) & 1) * UPO + (8 * hh + ((lane & 15) >> 2)) * 160 + (lane & 3) * 8;
    f32x16 acc[5][2] = {};
#pragma unroll
    for (int e = 0; e < 5; ++e) { PIN4(acc[e][0]); PIN4(acc[e][1]); }
    v4u fx[2][2]; s16x4 fl[4], fh[4];
    lp px[4], pw;
    auto setp = [&](unsigned sb) {
#pragma unroll
      for (int s = 0; s < 4; ++s) px[s] = LP(sb + (xa ^ (32 * s)));
      pw = LP(sb + wa);
    };
    auto woff = [&](int n) { return (n / 5) * 2560 + (n % 5) * 32; };
    setp(0);
    SB0();
    tag = row_stats(p, id, w, lane, rin);
    STAMP(17);
    SB0();
    BAR();
    STAMP(1);
    fx[0][0] = l128(px[0]); fx[0][1] = l128(px[0] + 4096);
#pragma unroll
    for (int n = 0; n < 3; ++n) { fl[n] = ltr(pw + woff(n)); fh[n] = ltr(pw + woff(n) + 640); }
    unsigned sb = 0;
#pragma unroll 1
    for (int i = 0; i < 10; ++i) {
#pragma unroll
      for (int n = 0; n < 20; ++n) {
        const int ks = n / 5, e = n % 5, nn = (n + 3) % 20;
        if (n == 17) { if (i < 9) BAR(); LSTAMP(i); sb = sb == 2 * STG ? 0 : sb + STG; setp(sb); SB0(); }
        const v4u wf = cat(fl[n & 3], fh[n & 3]);
        acc[e][0] = mfma32(wf, fx[ks & 1][0], acc[e][0]); SB0();
        fl[(n + 3) & 3] = ltr(pw + woff(nn));
        fh[(n + 3) & 3] = ltr(pw + woff(nn) + 640); SB0();
        acc[e][1] = mfma32(wf, fx[ks & 1][1], acc[e][1]); SB0();
        if (ks < 3 && e == 1) { fx[(ks + 1) & 1][0] = l128(px[ks + 1]); fx[(ks + 1) & 1][1] = l128(px[ks + 1] + 4096); }
        if (n == 17) { fx[0][0] = l128(px[0]); fx[0][1] = l128(px[0] + 4096); }
        if (n == 18) cf = lrd(L_CSF + (lane & 3) * 4);
        SB0();
      }
    }
    lwait(L_CSF + (lane & 3) * 4, cf, 1);
    STAMP(12);
    const __amdgpu_buffer_rsrc_t rh = mkrs(p.ws + WS_H + (unsigned)(4 * id.R + w) * (NKB * 4096), 54 * 4096);
    const unsigned so0 = (r >> 4) * 1024 + (hh * 16 + (r & 15)) * 16;
    float mu[2], r2[2];
#pragma unroll
    for (int u = 0; u < 2; ++u) {
      const int F = 4 * w + 2 * u + (r >> 4);
      const f32x2 s = *(LAS f32x2*)LP(L_STATS + (128 * (F >> 3) + 8 * (r & 15) + (F & 7)) * 8);
      mu[u] = s.x; r2[u] = s.y * s.y;
    }
    const lp pc = LP(L_CS + hh * 16);
#pragma unroll
    for (int e = 0; e < 5; ++e) {
      const f32x4 g0 = lf4(pc + e * 64) + lf4(pc + e * 64 + 640), g1 = lf4(pc + e * 64 + 32) + lf4(pc + e * 64 + 672);
      const f32x4 u0 = lf4(pc + e * 64 + 320) + lf4(pc + e * 64 + 960), u1 = lf4(pc + e * 64 + 352) + lf4(pc + e * 64 + 992);
      const int T = 5 * id.g + e;
      const unsigned so = so0 + (T >> 1) * 4096 + (T & 1) * 512;
#pragma unroll
      for (int u = 0; u < 2; ++u) {
        const f32x16 a = acc[e][u];
        const f32x4 nm = sp4(-mu[u]);
        const f32x4 h0 = fma4(nm, g0, f32x4{a[0], a[1], a[2], a[3]}) * fma4(nm, u0, f32x4{a[8], a[9], a[10], a[11]}) * r2[u];
        const f32x4 h1 = fma4(nm, g1, f32x4{a[4], a[5], a[6], a[7]}) * fma4(nm, u1, f32x4{a[12], a[13], a[14], a[15]}) * r2[u];
        st16c(v4u{pk2(h0.x, h0.y), pk2(h0.z, h0.w), pk2(h1.x, h1.y), pk2(h1.z, h1.w)}, rh, so + u * 2048);
      }
    }
  } else {
    const int c = lane & 15, q = lane >> 4, G = w >> 1, blk = w & 1;
    const unsigned xa = (128 * G + c) * 128 + ((q ^ (c >> 1)) * 16);
    const unsigned wa = 32768 + (32 * blk + c) * 128 + ((q ^ (c >> 1)) * 16);
    f32x4 acc[8][4] = {};
#pragma unroll
    for (int j = 0; j < 8; ++j)
#pragma unroll
      for (int t = 0; t < 4; ++t) PIN4(acc[j][t]);
    v4u fw[2][4], fx[4];
    lp px[2], pw[2];
    auto setp = [&](unsigned sb) { px[0] = LP(sb + xa); px[1] = LP(sb + (xa ^ 64)); pw[0] = LP(sb + wa); pw[1] = LP(sb + (wa ^ 64)); };
    auto woff = [&](int t) { return (t >> 1) * 8192 + (t & 1) * 2048; };
    setp(0);
    SB0();
    tag = row_stats(p, id, w, lane, rin);
    STAMP(17);
    SB0();
    BAR();
    STAMP(1);
#pragma unroll
    for (int t = 0; t < 4; ++t) fw[0][t] = l128(pw[0] + woff(t));
#pragma unroll
    for (int n = 0; n < 3; ++n) fx[n] = l128(px[0] + n * 2048);
    unsigned sb = 0;
#pragma unroll 1
    for (int i = 0; i < 10; ++i) {
#pragma unroll
      for (int n = 0; n < 16; ++n) {
        const int kk = n >> 3, j = n & 7, nn = (n + 3) & 15;
        if (n == 13) { if (i < 9) BAR(); LSTAMP(i); sb = sb == 2 * STG ? 0 : sb + STG; setp(sb); SB0(); }
        const v4u xf = fx[n & 3];
        acc[j][0] = mfma16(fw[kk][0], xf, acc[j][0]); SB0();
        fx[(n + 3) & 3] = l128(px[nn >> 3] + (nn & 7) * 2048); SB0();
        acc[j][1] = mfma16(fw[kk][1], xf, acc[j][1]); SB0();
        if (n >= 2 && n < 6) fw[1][n - 2] = l128(pw[1] + woff(n - 2));
        if (n == 13) fw[0][0] = l128(pw[0] + woff(0));
        if (n == 14) fw[0][2] = l128(pw[0] + woff(2));
        SB0();
        acc[j][2] = mfma16(fw[kk][2], xf, acc[j][2]); SB0();
        if (n == 13) fw[0][1] = l128(pw[0] + woff(1));
        if (n == 14) fw[0][3] = l128(pw[0] + woff(3));
        SB0();
        acc[j][3] = mfma16(fw[kk][3], xf, acc[j][3]); SB0();
        if (n == 14) { cf = lrd(L_CSF + (lane & 3) * 4); SB0(); }
      }
    }
    lwait(L_CSF + (lane & 3) * 4, cf, 1);
    STAMP(12);
    const lp pe = LP(L_E + (16 * G + c) * ESTR + (32 * blk + 8 * q) * 4);
    const lp pk = LP(L_CST + (32 * blk + 8 * q) * 4);
    f32x4 lr[2], li[2], er[2], ei[2];
    {
      const lp ps = LP(L_STATS + (128 * G + 8 * c) * 8), pc = LP(L_CS + (32 * blk + 8 * q) * 4);
      f32x4 cs[4], st[4];
#pragma unroll
      for (int t = 0; t < 4; ++t) { cs[t] = lf4(pc + (t >> 1) * 320 + (t & 1) * 16) + lf4(pc + 640 + (t >> 1) * 320 + (t & 1) * 16); st[t] = lf4(ps + t * 16); }
#pragma unroll
      for (int ab = 0; ab < 2; ++ab) { lr[ab] = lf4(pk + ab * 16); li[ab] = lf4(pk + 256 + ab * 16); }
#pragma unroll
      for (int jj = 0; jj < 4; ++jj)
#pragma unroll
        for (int t = 0; t < 4; ++t) {
          acc[2 * jj][t] = fma4(sp4(-st[jj].x), cs[t], acc[2 * jj][t]) * st[jj].y;
          acc[2 * jj + 1][t] = fma4(sp4(-st[jj].z), cs[t], acc[2 * jj + 1][t]) * st[jj].w;
        }
    }
#pragma unroll
    for (int ab = 0; ab < 2; ++ab) { er[ab] = acc[0][ab]; ei[ab] = acc[0][2 + ab]; }
#pragma unroll
    for (int j = 1; j < 8; ++j)
#pragma unroll
      for (int ab = 0; ab < 2; ++ab) cmac(er[ab], ei[ab], lr[ab], li[ab], acc[j][ab], acc[j][2 + ab]);
#pragma unroll
    for (int ab = 0; ab < 2; ++ab) { sf4(pe + ab * 16, er[ab]); sf4(pe + 256 + ab * 16, ei[ab]); }
    SB0();
    BAR();
    STAMP(13);
#pragma unroll
    for (int ab = 0; ab < 2; ++ab) {
      const f32x4 kr = lf4(pk + 512 * 4 + ab * 16), ki = lf4(pk + 512 * 4 + 256 + ab * 16);
#pragma unroll
      for (int j = 0; j < 8; ++j) {
        const f32x4 ur = acc[j][ab], ui = acc[j][2 + ab];
        acc[j][ab] = fma4(-ki, ui, kr * ur); acc[j][2 + ab] = fma4(-ki, ur, -kr * ui);
        PIN4(acc[j][ab]); PIN4(acc[j][2 + ab]);
      }
      li[ab] = -li[ab];
    }
    const __amdgpu_buffer_rsrc_t rh = mkrs(p.ws + WS_H + (unsigned)(4 * id.R + 2 * G) * (NKB * 4096), 2 * NKB * 4096);
    unsigned vo[4];
#pragma unroll
    for (int z = 0; z < 4; ++z) { vo[z] = (54 + 2 * id.g + blk + 20 * (z & 1) + NKB * (z >> 1)) * 4096 + lane * 16; PIN4(vo[z]); }
    SB0();
    BAR();
    STAMP(14);
#pragma unroll
    for (int ab = 0; ab < 2; ++ab) { er[ab] = lf4(pe + ab * 16); ei[ab] = lf4(pe + 256 + ab * 16); }
#pragma unroll
    for (int j = 0; j < 8; ++j) {
#pragma unroll
      for (int ab = 0; ab < 2; ++ab) cmac(er[ab], ei[ab], lr[ab], li[ab], acc[j][ab], acc[j][2 + ab]);
      st16c(v4u{pk2(er[0].x, er[0].y), pk2(er[0].z, er[0].w), pk2(er[1].x, er[1].y), pk2(er[1].z, er[1].w)}, rh, vo[2 * (j >> 2)] + (j & 3) * 1024);
      st16c(v4u{pk2(ei[0].x, ei[0].y), pk2(ei[0].z, ei[0].w), pk2(ei[1].x, ei[1].y), pk2(ei[1].z, ei[1].w)}, rh, vo[2 * (j >> 2) + 1] + (j & 3) * 1024);
    }
  }
  SB0();
  STAMP(15);
  return tag;
}

#define HBAR() do { SB0(); BAR(); SB0(); } while (0)
struct HState { f32x4 acs[5]; unsigned pl, pr, pi; };
template <bool SSM>
DI unsigned helper_wave(const Par& p, const Ids& id, const int h, const int lane, HState& hs) {
  STAMP(0);
  constexpr int NWP = SSM ? 4 : 5, NP = 8 + NWP;
  constexpr unsigned WSTEP = SSM ? 128u : 64u * 3456u;
  const int c = lane & 15, q = lane >> 4;
  const __amdgpu_buffer_rsrc_t rctl = mkrs(p.ws, WS_CTL_BYTES);
  const __amdgpu_buffer_rsrc_t rx = mkrs(p.x, L_ * D_ * 2);
  const __amdgpu_buffer_rsrc_t rw = mkrs(SSM ? ((h >> 1) ? p.bim : p.bre) : ((h >> 1) ? p.wu : p.wg), SSM ? P_ * D_ * 2 : D_ * F_ * 2);
  const unsigned v0 = (8 * (lane >> 3)) * 1280 + (((lane & 7) ^ (lane >> 4)) * 16), v1 = v0 ^ 64;
#define ROT 0
  unsigned st = ROT == 0 ? 0 : ROT == 1 ? 5 * ((blockIdx.x >> 2) & 1) : ROT == 2 ? (5 * (blockIdx.x & 7)) >> 2 : 5 * (((blockIdx.x >> 2) ^ blockIdx.x) & 1);
  const unsigned sx = (256 * id.R + 128 * (h >> 1) + 4 * (h & 1)) * 1280;
  auto issue_x = [&](unsigned s, unsigned sb) {
    dma_x(MkSeq<8>::T{}, rx, sb + h * 8192, v0, v1, s);
  };
  asm volatile("s_nop 4");
  issue_x(sx + st * 128, 0);
  unsigned vw[NWP];
  if constexpr (SSM) {
    vw[0] = (8 * (lane >> 5) + ((lane >> 3) & 3)) * 1280 + (((lane & 7) ^ (lane >> 4)) * 16); vw[1] = vw[0] ^ 64;
  } else {
#pragma unroll
    for (int i = 0; i < 5; ++i) { const int lc = 64 * (5 * (h & 1) + i) + lane, pos = lc / 10; vw[i] = pos * 3456 + (lc - pos * 10) * 16; }
  }
  const unsigned sw = SSM ? (64 * id.g + 32 * (h & 1)) * 1280 : 160 * id.g;
  const unsigned dw = 32768 + (h >> 1) * (SSM ? 8192 : UPO) + (h & 1) * NWP * 1024;
  auto issue_w = [&](unsigned s, unsigned sb) {
    if constexpr (SSM) dma_b(MkSeq<4>::T{}, rw, sb + dw, vw, s); else dma_g(MkSeq<5>::T{}, rw, sb + dw, vw, s);
  };
  issue_w(sw + st * WSTEP, 0);
  unsigned sb = STG;
  auto step = [&]() {
    st = st == 9 ? 0 : st + 1;
    issue_x(sx + st * 128, sb); issue_w(sw + st * WSTEP, sb);
    sb = sb == 2 * STG ? 0 : sb + STG;
  };
  step();
  if (h == 3 && lane < 8) *(LAS unsigned*)LP(L_CNT + lane * 4) = 0u;
  f32x4 acs[NWP] = {};
  v4u fc[NWP];
  const unsigned ca = SSM ? 32768 + (h >> 1) * 8192 + c * 128 + (((4 * (h & 1) + q) ^ (c >> 1)) * 16)
                          : 32768 + (h >> 1) * UPO + (32 * (h & 1) + 4 * q + (c >> 2)) * 160 + (c & 3) * 8;
  const v4u ones = {0x3F803F80u, 0x3F803F80u, 0x3F803F80u, 0x3F803F80u};
  auto cs_rd = [&](unsigned s) {
    const lp a = LP(s + ca);
#pragma unroll
    for (int t = 0; t < NWP; ++t) { if constexpr (SSM) fc[t] = l128(a + t * 2048); else fc[t] = cat(ltr(a + t * 32), ltr(a + t * 32 + 2560)); }
    SB0();
  };
  auto cs_mm = [&]() {
    SB0();
    __builtin_amdgcn_s_setprio(2);
#pragma unroll
    for (int t = 0; t < NWP; ++t) acs[t] = mfma16(fc[t], ones, acs[t]);
    __builtin_amdgcn_s_setprio(0);
    SB0();
  };
  constexpr int NF = SSM ? 5 : 4;
#define JL 1
  const int tsk = (id.g * 8 + id.R) * 4 + h, kb = SSM ? 4 * id.g + h : tsk / 10, dq = tsk - 10 * (tsk / 10);
  v4u f[5] = {}, sel[2], sv = {}, fl = {};
  unsigned fvo, fso[NF];
  const __amdgpu_buffer_rsrc_t rf = mkrs(SSM ? (kb < 20 ? p.cre : p.cim) : p.wd, SSM ? D_ * P_ * 2 : F_ * D_ * 2);
  const __amdgpu_buffer_rsrc_t r2 = mkrs(p.ws + WS_W2, (SSM || tsk < 540) ? WS_W2_BYTES : 0);
  {
    fvo = SSM ? (80 * id.R + c) * 1280 + (kb < 20 ? kb : kb - 20) * 64 + q * 16
              : (32 * kb + 16 * ((lane >> 3) & 1) + 4 * (q & 1) + (q >> 1)) * 1280 + dq * 128 + (lane & 7) * 16;
#pragma unroll
    for (int i = 0; i < NF; ++i) {
      const int dt = 4 * dq + 2 * (q & 1) + (i >> 1), ct = dt / 5;
      fso[i] = SSM ? ((id.R * NKB + 54 + kb) * 5 + i) * 1024 + lane * 16
                   : ((ct * NKB + kb) * 5 + dt - 5 * ct) * 1024 + (16 * (2 * (q >> 1) + (c >> 3)) + 8 * (i & 1) + (c & 7)) * 16;
      PIN4(fso[i]);
    }
#pragma unroll
    for (int hf = 0; hf < 2; ++hf) {
#pragma unroll
      for (int e = 0; e < 4; ++e) sel[hf][e] = (8 * q + 2 * e == 16 * hf + c ? 0x3F80u : 0u) | (8 * q + 2 * e + 1 == 16 * hf + c ? 0x3F800000u : 0u);
      if constexpr (!SSM) PIN4(sel[hf]);
    }
    PIN4(fvo);
  }
  unsigned epoch, pl = 0, pr = 0, pi = 0, tag = 0;
  unsigned so = WS_STATS + (256 * id.R + 64 * h + lane) * 16, flo = h == 3 ? WS_FLW + lane * 16 : OOB, fwo = (h == 3 && lane == 0) ? WS_FLW + blockIdx.x * 4 : OOB;
  PIN4(so); PIN4(flo); PIN4(fwo);
  asm volatile("buffer_load_dword %0, off, %1, 0 sc1" : "=v"(epoch) : "s"(rctl));
  if constexpr (SSM) {
    const unsigned po = (64 * id.g + lane) * 2;
    asm volatile("buffer_load_ushort %0, %3, %4, 0 offen\n\tbuffer_load_ushort %1, %3, %5, 0 offen\n\tbuffer_load_ushort %2, %3, %6, 0 offen"
                 : "=&v"(pl), "=&v"(pr), "=&v"(pi) : "v"(po), "s"(mkrs(p.ldt, P_ * 2)), "s"(mkrs(p.lre, P_ * 2)), "s"(mkrs(p.lim, P_ * 2)));
  }
  WAITV(NP + (SSM ? 4 : 1)); BAR();
#pragma unroll
  for (int j = 0; j < 8; ++j) {
    cs_rd((j % 3) * STG);
    if (j == JL + 4) { tag = mktag(epoch); st4c(tag, rctl, fwo); }
    if (j == 6) asm volatile("buffer_load_dwordx4 %0, %1, %2, 0 offen sc1" : "=v"(sv) : "v"(so), "s"(rctl));
    if (!SSM && j == 7) asm volatile("buffer_load_dwordx4 %0, %1, %2, 0 offen sc1" : "=v"(fl) : "v"(flo), "s"(rctl));
    SB0();
    step();
    if constexpr (SSM) {
      if (j == JL - 1) asm volatile("buffer_load_dwordx4 %0, %2, %3, 0 offen\n\tbuffer_load_dwordx4 %1, %2, %3, %4 offen" : "=&v"(f[0]), "=&v"(f[1]) : "v"(fvo), "s"(rf), "s"(16 * 1280));
      if (j == JL) asm volatile("buffer_load_dwordx4 %0, %3, %4, %5 offen\n\tbuffer_load_dwordx4 %1, %3, %4, %6 offen\n\tbuffer_load_dwordx4 %2, %3, %4, %7 offen"
                                : "=&v"(f[2]), "=&v"(f[3]), "=&v"(f[4]) : "v"(fvo), "s"(rf), "s"(32 * 1280), "s"(48 * 1280), "s"(64 * 1280));
    } else if (j == JL)
      asm volatile("buffer_load_dwordx4 %0, %4, %5, 0 offen\n\tbuffer_load_dwordx4 %1, %4, %5, 0 offen offset:2560\n\tbuffer_load_dwordx4 %2, %4, %5, %6 offen\n\tbuffer_load_dwordx4 %3, %4, %5, %6 offen offset:2560"
                   : "=&v"(f[0]), "=&v"(f[1]), "=&v"(f[2]), "=&v"(f[3]) : "v"(fvo), "s"(rf), "s"(8 * 1280));
    cs_mm();
    if (j == JL + 2) {
      if constexpr (SSM) {
#pragma unroll
        for (int i = 0; i < NF; ++i) st16c(f[i], r2, fso[i]);
      } else {
        f32x4 d[8];
        __builtin_amdgcn_s_setprio(2);
#pragma unroll
        for (int z = 0; z < 8; ++z) d[z] = mfma16(f[z >> 1], sel[z & 1], f32x4{0.f, 0.f, 0.f, 0.f});
#pragma unroll
        for (int i = 0; i < 4; ++i) st16c(v4u{pk2(d[0][i], d[1][i]), pk2(d[2][i], d[3][i]), pk2(d[4][i], d[5][i]), pk2(d[6][i], d[7][i])}, r2, fso[i]);
        __builtin_amdgcn_s_setprio(0);
      }
      SB0();
    }
    if (j == JL + 2) WAITV(NP + NF);
    else if (j == JL) WAITV(NP + (SSM ? 5 : 4));
    else if (SSM && j == JL - 1) WAITV(NP + 2);
    else if (j == JL + 1) asm volatile("s_waitcnt vmcnt(%9)" : "+v"(f[0]), "+v"(f[1]), "+v"(f[2]), "+v"(f[3]), "+v"(f[4]), "+v"(epoch), "+v"(pl), "+v"(pr), "+v"(pi) : "i"(NP) : "memory");
    else if (j == 6) asm volatile("s_waitcnt vmcnt(%1)" : "+v"(sv) : "i"(NP) : "memory");
    else if (j == 7) asm volatile("s_waitcnt vmcnt(%1)" : "+v"(fl) : "i"(NP) : "memory");
    else if (j == JL + 4) WAITV(NP + 1);
    else WAITV(NP);
    if (j == 6) {
      v2u o = {sv.x, sv.z};
      if (!__all((sv.y == tag) & (sv.w == tag))) {
        _Pragma("unroll 1") for (int spin = 0; spin < SPIN; ++spin) { FENCE();
          const v4u s2 = ld16c(rctl, so, 0);
          o = v2u{s2.x, s2.z};
          if (__all((s2.y == tag) & (s2.w == tag))) break;
          __builtin_amdgcn_s_sleep(4);
        }
      }
      *(LAS v2u*)LP(L_STATS + (64 * h + lane) * 8) = o;
    }
    if (!SSM && j == 7 && h == 3) {
      if (!__all((fl.x == tag) & (fl.y == tag) & (fl.z == tag) & (fl.w == tag))) {
        _Pragma("unroll 1") for (int spin = 0; spin < SPIN; ++spin) { FENCE();
          const v4u f2 = ld16c(rctl, flo, 0);
          if (__all((f2.x == tag) & (f2.y == tag) & (f2.z == tag) & (f2.w == tag))) break;
          __builtin_amdgcn_s_sleep(4);
        }
      }
      st4c(tag, rctl, (lane == 0 && blockIdx.x == 255) ? WS_EPOCH : OOB);
    }
    BAR();
  }
#pragma unroll
  for (int t = 0; t < 5; ++t) hs.acs[t] = acs[t < NWP ? t : 0];
  hs.pl = pl; hs.pr = pr; hs.pi = pi;
  return tag;
}
template <bool SSM>
DI void helper_tail(const Par& p, const Ids& id, const int h, const int lane, const unsigned tag, HState& hs) {
  constexpr int NWP = SSM ? 4 : 5;
  const int c = lane & 15, q = lane >> 4;
  const __amdgpu_buffer_rsrc_t rctl = mkrs(p.ws, WS_CTL_BYTES);
  f32x4* acs = hs.acs;
  const unsigned pl = hs.pl, pr = hs.pr, pi = hs.pi;
  v4u fc[NWP];
  const unsigned ca = SSM ? 32768 + (h >> 1) * 8192 + c * 128 + (((4 * (h & 1) + q) ^ (c >> 1)) * 16)
                          : 32768 + (h >> 1) * UPO + (32 * (h & 1) + 4 * q + (c >> 2)) * 160 + (c & 3) * 8;
  const v4u ones = {0x3F803F80u, 0x3F803F80u, 0x3F803F80u, 0x3F803F80u};
  auto cs_rd = [&](unsigned s) {
    const lp a = LP(s + ca);
#pragma unroll
    for (int t = 0; t < NWP; ++t) { if constexpr (SSM) fc[t] = l128(a + t * 2048); else fc[t] = cat(ltr(a + t * 32), ltr(a + t * 32 + 2560)); }
    SB0();
  };
  auto cs_mm = [&]() {
    SB0();
    __builtin_amdgcn_s_setprio(2);
#pragma unroll
    for (int t = 0; t < NWP; ++t) acs[t] = mfma16(fc[t], ones, acs[t]);
    __builtin_amdgcn_s_setprio(0);
    SB0();
  };
  cs_rd(2 * STG);
  if (SSM) {
    if (h == 3) {
      _Pragma("unroll 1") for (int spin = 0; spin < SPIN; ++spin) { FENCE();
        const v4u f2 = ld16c(rctl, WS_FLW + lane * 16, 0);
        if (__all((f2.x == tag) & (f2.y == tag) & (f2.z == tag) & (f2.w == tag))) break;
        __builtin_amdgcn_s_sleep(4);
      }
    }
  }
  float br = 0.f, bi = 0.f;
  if (SSM && h == 2) {
    const float dt = rbf(expf(bf2f(pl))), ex = expf(dt * bf2f(pr));
    float sn, cn;
    sincosf(dt * bf2f(pi), &sn, &cn);
    br = ex * cn; bi = ex * sn;
  }
  cs_mm();
  WAITV(SSM ? 0 : 15); BAR();
  cs_rd(0);
  if (SSM && h == 2) {
    const float lr = bf2f(pr), li = bf2f(pi), nr = br - 1.f, ni = bi, den = lr * lr + li * li;
    LAS float* cst = (LAS float*)LP(L_CST);
    cst[4 * 128 + lane] = 2.f * (nr * lr + ni * li) / den; cst[4 * 128 + 64 + lane] = 2.f * (ni * lr - nr * li) / den;
    cst[lane] = br; cst[64 + lane] = bi;
#pragma unroll
    for (int s = 1; s <= 8; ++s) {
      const float a = br * br - bi * bi, b = 2.f * br * bi;
      br = a; bi = b;
      const int k = s == 3 ? 1 : s == 6 ? 2 : s == 8 ? 3 : 0;
      if (k) { cst[k * 128 + lane] = br; cst[k * 128 + 64 + lane] = bi; }
    }
  }
  cs_mm();
  if (c == 0) {
#pragma unroll
    for (int t = 0; t < NWP; ++t) {
      const int col = SSM ? 32 * (t >> 1) + 8 * q + 4 * (t & 1) : 16 * t + 4 * q;
      sf4(LP(L_CS + ((h & 1) * 160 + (h >> 1) * 80 + col) * 4), acs[t]);
    }
  }
  sig(L_CSF + h * 4, 1);
}

DI void carries(const Par& p, const Ids& id, const int h, const int lane, const unsigned tag, WRing& wr) {
  const __amdgpu_buffer_rsrc_t rctl = mkrs(p.ws, WS_CTL_BYTES);
  const int c = lane & 15, q = lane >> 4;
  {
    const int ch = 16 * h + c;
    lwait<2>(L_CSF + 8, 0, 1);
    LAS float* cst = (LAS float*)LP(L_CST);
    const float l8r = cst[128 + ch], l8i = cst[192 + ch], l64r = cst[256 + ch], l64i = cst[320 + ch], lwr = cst[384 + ch], lwi = cst[448 + ch];
    const float kr = cst[512 + ch], ki = cst[576 + ch];
    float w0r = 1.f, w0i = 0.f, w1r = 1.f, w1i = 0.f, qr = kr, qi = ki;
#pragma unroll
    for (int s = 0; s < 6; ++s) {
      float a = w0r, b = w0i; cm1(a, b, lwr, lwi, 0.f, 0.f);
      if (s < id.R - 1 - q) { w0r = a; w0i = b; }
      if (s < id.R - 5 - q) { w1r = a; w1i = b; }
    }
#pragma unroll
    for (int s = 0; s < 3; ++s) { float a = qr, b = qi; cm1(a, b, l64r, l64i, 0.f, 0.f); if (s < q) { qr = a; qi = b; } }
    const __amdgpu_buffer_rsrc_t rag = mkrs(p.ws + WS_AGG + id.R * (8 * 640 * 16), id.R * 640 * 16);
    LAS float* pe = (LAS float*)LP(L_E + 8 * q * ESTR + ch * 4);
    asm volatile("" : "+v"(w0r), "+v"(w0i), "+v"(w1r), "+v"(w1i), "+v"(qr), "+v"(qi));
    unsigned ao[2], po = (q * 640 + 64 * id.g + ch) * 16;
#pragma unroll
    for (int z = 0; z < 2; ++z) ao[z] = q + 4 * z > id.R ? WS_AGG + (((q + 4 * z) * 8 + id.R) * 640 + 64 * id.g + ch) * 16 : OOB;
    asm volatile("" : "+v"(ao[0]), "+v"(ao[1]), "+v"(po));
    __builtin_amdgcn_s_setprio(3);
    HBAR();
    float er[8], ei[8];
#pragma unroll
    for (int s = 0; s < 8; ++s) { er[s] = pe[s * (ESTR / 4)]; ei[s] = pe[s * (ESTR / 4) + 64]; }
    float a = er[0], b = ei[0];
#pragma unroll
    for (int s = 1; s < 8; ++s) cm1(a, b, l8r, l8i, er[s], ei[s]);
    float ta[4], tb[4];
    gather4(a, ta); gather4(b, tb);
    float pa = 0.f, pb = 0.f;
    a = ta[0]; b = tb[0];
#pragma unroll
    for (int s = 1; s < 4; ++s) {
      if (q == s) { pa = a; pb = b; }
      cm1(a, b, l64r, l64i, ta[s], tb[s]);
    }
#pragma unroll
    for (int z = 0; z < 2; ++z)
      st16c(v4u{__float_as_uint(a), tag, __float_as_uint(b), tag}, rctl, ao[z]);
    SB0();
    wr.dma01();
    v4u p0 = ld16c(rag, po, 0), p1 = ld16c(rag, po, 4 * 640 * 16);
    SB0();
    cm1(pa, pb, kr, ki, 0.f, 0.f);
#pragma unroll
    for (int s = 0; s < 7; ++s) cm1(er[s], ei[s], kr, ki, 0.f, 0.f);
    _Pragma("unroll 1") for (int spin = 0; spin < SPIN; ++spin) { FENCE();
      const bool ok = ((q >= id.R) | ((p0.y == tag) & (p0.w == tag))) & ((q + 4 >= id.R) | ((p1.y == tag) & (p1.w == tag)));
      if (__all(ok)) break;
      p0 = ld16c(rag, po, 0); p1 = ld16c(rag, po, 4 * 640 * 16);
    }
    float ca = __uint_as_float(p1.x), cb = __uint_as_float(p1.z);
    cm1(ca, cb, w1r, w1i, 0.f, 0.f);
    { float x = __uint_as_float(p0.x), y = __uint_as_float(p0.z); cm1(x, y, w0r, w0i, ca, cb); ca = x; cb = y; }
    gather4(ca, ta); gather4(cb, tb);
    ca = (ta[0] + ta[1]) + (ta[2] + ta[3]); cb = (tb[0] + tb[1]) + (tb[2] + tb[3]);
    cm1(ca, cb, qr, qi, pa, pb);
#pragma unroll
    for (int s = 0; s < 8; ++s) {
      pe[s * (ESTR / 4)] = ca; pe[s * (ESTR / 4) + 64] = -cb;
      if (s < 7) cm1(ca, cb, l8r, l8i, er[s], ei[s]);
    }
    __builtin_amdgcn_s_setprio(0);
    HBAR();
  }
}

__global__ __launch_bounds__(512, 2) void fused(Par p) {
  const int bid = blockIdx.x, xcd = bid & 7, idx = bid >> 3, a = xcd >> 2, b = xcd & 3, s = idx >> 2;
  Ids id;
  const bool ssm = idx < 10, part = idx >= 8 && idx < 12;
  id.R = 4 * a + (part ? 2 * (b & 1) + (idx & 1) : (idx & 3));
  id.g = ssm ? (part ? 8 + (b >> 1) : 2 * b + s) : (part ? 20 + (b >> 1) : 5 * b + s - 3);
  id.cg = ssm ? id.g : 10 + id.g;
  id.rt = 16 * a + 8 * (b >> 1) + (idx >> 2);
  id.ct = 4 * (b & 1) + (idx & 3);
  const int wv = rfl(threadIdx.x >> 6), lane = threadIdx.x & 63;
  unsigned cpf = 0;
  if (wv == 3) {
    unsigned long long pc; unsigned len;
    asm volatile("s_getpc_b64 %0\n.Lpf_here:\n\ts_mov_b32 %1, .Lpf_end-.Lpf_here" : "=s"(pc), "=s"(len));
    cpf = __builtin_amdgcn_raw_buffer_load_b32(mkrs((const void*)pc, len), lane < 13 ? (idx * 13 + lane) * 128 : OOB, 0, 0);
  }
  const __amdgpu_buffer_rsrc_t rctl = mkrs(p.ws, WS_CTL_BYTES);
  const __amdgpu_buffer_rsrc_t rw = mkrs(p.ws + WS_W2 + (unsigned)id.ct * (NKB * 5120), NKB * 5120);
  const unsigned vl = lane * 16;
  const unsigned fo = WS_FLH + (id.rt * 8 + id.ct) * 128 + (lane & 31) * 4;
  if (wv == 2) cpf = ld4c(rctl, lane < 32 ? fo : lane < 40 ? WS_FLW + (lane - 32) * 128 : OOB);
  WRing wr;
  wr.rw = rw; wr.vl = vl; wr.wv = wv;
  unsigned tag;
  if (wv < 4) {
    tag = ssm ? compute_wave<true>(p, id, wv, lane) : compute_wave<false>(p, id, wv, lane);
    WAITV(0);
    unsigned old = 0;
    if (lane == 0) old = __hip_atomic_fetch_add((LAS unsigned*)LP(L_CNT), 1u, __ATOMIC_RELAXED, __HIP_MEMORY_SCOPE_WORKGROUP);
    st4c(tag, rctl, (lane < 32 && rfl(old) == 3) ? WS_FLH + (id.R * 32 + lane) * 128 + id.cg * 4 : OOB);
    asm volatile("" ::"v"(cpf));
    STAMP(16);
#pragma unroll
    for (int it = NL; it < NL + DW; ++it) wr.load(it);
  } else {
    HState hs;
    tag = ssm ? helper_wave<true>(p, id, wv - 4, lane, hs) : helper_wave<false>(p, id, wv - 4, lane, hs);
    if (ssm) helper_tail<true>(p, id, wv - 4, lane, tag, hs);
    wr.load(NL); wr.load(NL + 1); wr.load(NL + 2);
    if (!ssm) helper_tail<false>(p, id, wv - 4, lane, tag, hs);
#pragma unroll
    for (int it = NL + 3; it < NL + DW; ++it) wr.load(it);
    if (ssm) carries(p, id, wv - 4, lane, tag, wr); else wr.dma01();
    lwait<2>(L_CNT, 0, 4);
    STAMP(16);
    if (wv > 4) {
      asm volatile("s_nop 4");
      dma_lin(MkSeq<14>::T{}, rw, (wv - 5) * 13312, vl, 81920 + (wv - 5) * 13312);
    } else {
      _Pragma("unroll 1") for (int spin = 0; spin < SPIN; ++spin) { FENCE();
        if (__all((ld4c(rctl, fo) == tag) | ((lane & 31) < 10))) break;
        __builtin_amdgcn_s_sleep(2);
      }
      sig(L_GO, 1);
    }
  }

  const __amdgpu_buffer_rsrc_t rh = mkrs(p.ws + WS_H + (unsigned)id.rt * (NKB * 4096), NKB * 4096);
  v4u hb[DH][4];
  auto loadh = [&](int it) {
#pragma unroll
    for (int j = 0; j < 4; ++j) { hb[it % DH][j] = ld16c(rh, vl + j * 1024, (8 * it + wv) * 4096); SB0(); }
  };
  lwait<1>(L_GO, 0, 1);
  SB0();
  STAMP(20);
#pragma unroll
  for (int it = 0; it < DH; ++it) { loadh(it); SB0(); }
  f32x4 acc[4][5];
#pragma unroll
  for (int j = 0; j < 4; ++j)
#pragma unroll
    for (int i = 0; i < 5; ++i) acc[j][i] = f32x4{0.f, 0.f, 0.f, 0.f};
  const int tid = wv * 64 + lane;
  v2u xv[3], dv[3]; v4u sv[3]; int oo[3], ra[3];
  unsigned sfl = 0;
  const lp pw = LP(L_W + wv * 5120 + vl), pw2 = LP(wv * 5120 + vl);
  SB0();
  if (wv > 4) {
    WAITV(4 * DH);
    if (lane == 0) __hip_atomic_fetch_add((LAS unsigned*)LP(L_RDY), 1u, __ATOMIC_RELAXED, __HIP_MEMORY_SCOPE_WORKGROUP);
  }
  lwait<1>(L_RDY, 0, 3);
  v4u wl = l128(pw);
  SB0();
#pragma unroll
  for (int it = 0; it < NIT; ++it) {
    if (it < NL) {
#pragma unroll
      for (int i = 0; i < 5; ++i) {
        const v4u wf = wl;
        if (5 * it + i + 1 < 5 * NL) wl = l128((5 * it + i + 1) / 5 == 2 ? pw2 + ((5 * it + i + 1) % 5) * 1024 : pw + ((5 * it + i + 1) / 5) * 40960 + ((5 * it + i + 1) % 5) * 1024);
#pragma unroll
        for (int j = 0; j < 4; ++j) acc[j][i] = mfma16(wf, hb[it % DH][j], acc[j][i]);
        SB0();
      }
    } else {
#pragma unroll
      for (int j = 0; j < 4; ++j)
#pragma unroll
        for (int i = 0; i < 5; ++i) acc[j][i] = mfma16(wr.wb[it % DW][i], hb[it % DH][j], acc[j][i]);
    }
    SB0();
    if (it + DH == 6) {
      if (__builtin_expect(!__all(sfl == tag), 0)) {
        _Pragma("unroll 1") for (int spin = 0; spin < SPIN; ++spin) { FENCE();
          if (__all(ld4c(rctl, fo) == tag)) break;
          __builtin_amdgcn_s_sleep(2);
        }
      }
      FENCE();
      STAMP(18); STAMPC(28);
    }
    if (it == 0) sfl = ld4c(rctl, fo);
    if (it + DH < NIT) loadh(it + DH);
    if (it >= NL && it + DW < NIT) wr.load(it + DW);
    if (it == NIT - DH) {
#pragma unroll
      for (int r = 0; r < 3; ++r) {
        const int sl = min(tid + 512 * r, 1279), row = sl / 20, qd = sl - 20 * row;
        const int tok = 128 * (id.rt >> 1) + 8 * (row >> 2) + 4 * (id.rt & 1) + (row & 3), d = 80 * id.ct + 4 * qd;
        oo[r] = tok * D_ + d;
        ra[r] = ((row & 3) * 5 + (qd >> 2)) * 1024 + (qd & 3) * 256 + (((row >> 2) + qd) & 15) * 16;
        xv[r] = *(const v2u*)(p.x + oo[r]); dv[r] = *(const v2u*)(p.dsk + d);
        sv[r] = ld16c(rctl, WS_STATS + tok * 16, 0);
      }
    }
#define BARIT 2
    if (it == BARIT) BAR();
    SB0();
  }
  STAMP(21); STAMPC(29);
#pragma unroll
  for (int i = 0; i < 5; ++i) {
    const lp rb = LP(wv * 20480 + (lane >> 4) * 256 + (((lane & 15) + 4 * i + (lane >> 4)) & 15) * 16);
#pragma unroll
    for (int j = 0; j < 4; ++j) sf4(rb + (j * 5 + i) * 1024, acc[j][i]);
  }
  BAR();
  asm volatile(".Lpf_end:");
  STAMP(19);
#pragma unroll
  for (int r = 0; r < 3; ++r) {
    if (r < 2 || wv < 4) {
      const lp q0 = LP(ra[r]), q1 = LP(ra[r] + 4 * 20480);
      f32x4 sum = ((lf4(q0) + lf4(q0 + 20480)) + (lf4(q0 + 40960) + lf4(q0 + 61440))) + ((lf4(q1) + lf4(q1 + 20480)) + (lf4(q1 + 40960) + lf4(q1 + 61440)));
      const f32x4 xf = {bflo(xv[r].x), bfhi(xv[r].x), bflo(xv[r].y), bfhi(xv[r].y)};
      const f32x4 df = {bflo(dv[r].x), bfhi(dv[r].x), bflo(dv[r].y), bfhi(dv[r].y)};
      sum += xf + (xf - __uint_as_float(sv[r].x)) * __uint_as_float(sv[r].z) * df;
#define OUTAUX 16
      __builtin_amdgcn_raw_buffer_store_b128(__builtin_bit_cast(v4u, sum), mkrs(p.out, L_ * D_ * 4), oo[r] * 4, 0, OUTAUX);
    }
  }
  STAMP(22);
}

extern "C" void kernel_launch(void* const* d_in, const int* in_sizes, int n_in, void* d_out, int out_size, void* d_ws, size_t ws_size, hipStream_t stream) {
  Par p;
  p.x = (const u16*)d_in[0]; p.lre = (const u16*)d_in[1]; p.lim = (const u16*)d_in[2];
  p.bre = (const u16*)d_in[3]; p.bim = (const u16*)d_in[4]; p.cre = (const u16*)d_in[5]; p.cim = (const u16*)d_in[6];
  p.ldt = (const u16*)d_in[7]; p.dsk = (const u16*)d_in[8];
  p.wg = (const u16*)d_in[10]; p.wu = (const u16*)d_in[11]; p.wd = (const u16*)d_in[12];
#define WSOFF 0
  p.ws = (unsigned char*)d_ws + (WSOFF == 1 ? ((ws_size - (21u << 20)) & ~(size_t)0xFFFFF) : WSOFF == 2 ? (ws_size / 2) : 0); p.out = (float*)d_out;
  (void)hipFuncSetAttribute((const void*)fused, hipFuncAttributeMaxDynamicSharedMemorySize, LDS1);
  fused<<<256, 512, LDS1, stream>>>(p);
}
```

```cpp
#define ENCA 31
#define ENCB 31
#define ENCW 200
#include <hip/hip_runtime.h>
#include <stdint.h>

typedef unsigned short u16;
typedef unsigned v4u __attribute__((ext_vector_type(4)));
typedef unsigned v2u __attribute__((ext_vector_type(2)));
typedef float f32x4 __attribute__((ext_vector_type(4)));
typedef float f32x2 __attribute__((ext_vector_type(2)));
typedef float f32x16 __attribute__((ext_vector_type(16)));
typedef short bf16x8 __attribute__((ext_vector_type(8)));
typedef short s16x4 __attribute__((ext_vector_type(4)));
typedef __bf16 bf2 __attribute__((ext_vector_type(2)));
#define LAS __attribute__((address_space(3)))
#define DI __device__ __forceinline__
typedef LAS unsigned char* lp;

constexpr int L_ = 2048, D_ = 640, P_ = 640, F_ = 1728;
constexpr int NKB = 94;
constexpr unsigned WS_EPOCH = 0;
constexpr unsigned WS_FLW = 2048;
constexpr unsigned WS_FLH = 4096;
constexpr unsigned WS_AGG = WS_FLH + 32768;
constexpr unsigned WS_STATS = WS_AGG + 64 * 640 * 16;
constexpr unsigned WS_CTL_BYTES = WS_STATS + 32768;
constexpr unsigned WS_W2 = 1u << 20;
constexpr unsigned WS_W2_BYTES = 8 * NKB * 5120;
constexpr unsigned WS_H = 8u << 20;
constexpr unsigned WS_H_BYTES = 32 * NKB * 4096;

constexpr int UPO = 10368;
constexpr int STG = 32768 + 20480 + 128;
constexpr int LDS1 = 163840;
constexpr int L_STATS = 3 * STG;
constexpr int L_CS = L_STATS + 2048;
constexpr int L_CNT = L_CS + 1280, L_GO = L_CNT + 4, L_RDY = L_CNT + 8, L_CSF = L_CNT + 16;
constexpr int L_CST = 49152;
constexpr int L_W = STG;
constexpr int L_E = 136000, ESTR = 528;
constexpr int SPIN = 1 << 18;
constexpr unsigned OOB = 0x40000000u;

DI float bf2f(unsigned b) { return __uint_as_float(b << 16); }
DI float bflo(unsigned w) { return __uint_as_float(w << 16); }
DI float bfhi(unsigned w) { return __uint_as_float(w & 0xffff0000u); }
DI unsigned pk2(float a, float b) { f32x2 v = {a, b}; return __builtin_bit_cast(unsigned, __builtin_convertvector(v, bf2)); }
DI float rbf(float a) { return bflo(pk2(a, a) & 0xffffu); }
DI f32x4 mfma16(v4u a, v4u b, f32x4 c) { return __builtin_amdgcn_mfma_f32_16x16x32_bf16(__builtin_bit_cast(bf16x8, a), __builtin_bit_cast(bf16x8, b), c, 0, 0, 0); }
DI f32x16 mfma32(v4u a, v4u b, f32x16 c) { return __builtin_amdgcn_mfma_f32_32x32x16_bf16(__builtin_bit_cast(bf16x8, a), __builtin_bit_cast(bf16x8, b), c, 0, 0, 0); }
DI unsigned rfl(unsigned v) { return __builtin_amdgcn_readfirstlane(v); }
DI __amdgpu_buffer_rsrc_t mkrs(const void* p, unsigned bytes) { return __builtin_amdgcn_make_buffer_rsrc((void*)p, 0, bytes, 0x00020000); }
template <int LI, int SI> DI void glds(__amdgpu_buffer_rsrc_t rs, unsigned lb, unsigned voff, unsigned sb) {
  unsigned t;
  asm volatile("s_add_u32 m0, %1, %5\n\ts_add_u32 %0, %4, %6\n\tbuffer_load_dwordx4 %2, %3, %0 offen lds" : "=&s"(t) : "s"(lb), "v"(voff), "s"(rs), "s"(sb), "i"(LI), "i"(SI) : "scc");
}
template <int LI, int SI> DI void gldsc(__amdgpu_buffer_rsrc_t rs, unsigned lb, unsigned voff, unsigned sb) {
  unsigned t;
  asm volatile("s_add_u32 m0, %1, %5\n\ts_add_u32 %0, %4, %6\n\tbuffer_load_dwordx4 %2, %3, %0 offen sc1 lds" : "=&s"(t) : "s"(lb), "v"(voff), "s"(rs), "s"(sb), "i"(LI), "i"(SI) : "scc");
}
template <int... I> struct Seq {};
template <int N, int... I> struct MkSeq : MkSeq<N - 1, N - 1, I...> {};
template <int... I> struct MkSeq<0, I...> { typedef Seq<I...> T; };
template <int... I> DI void dma_lin(Seq<I...>, __amdgpu_buffer_rsrc_t rs, unsigned lb, unsigned vl, unsigned sb) { (gldsc<I * 1024, I * 1024>(rs, lb, vl, sb), ...); }
template <int... I> DI void dma_x(Seq<I...>, __amdgpu_buffer_rsrc_t rs, unsigned lb, unsigned v0, unsigned v1, unsigned sb) { (glds<I * 1024, (64 * (I & 1) + (I >> 1)) * 1280>(rs, lb, (I & 1) ? v1 : v0, sb), ...); }
template <int... I> DI void dma_b(Seq<I...>, __amdgpu_buffer_rsrc_t rs, unsigned lb, const unsigned* v, unsigned sb) { (glds<I * 1024, (16 * (I & 1) + 4 * (I >> 1)) * 1280>(rs, lb, v[I & 1], sb), ...); }
template <int... I> DI void dma_g(Seq<I...>, __amdgpu_buffer_rsrc_t rs, unsigned lb, const unsigned* v, unsigned sb) { (glds<I * 1024, 0>(rs, lb, v[I], sb), ...); }
DI lp LP(unsigned a) { return (lp)(uintptr_t)a; }
DI v4u l128(lp a) { return *(LAS v4u*)a; }
DI f32x4 lf4(lp a) { return *(LAS f32x4*)a; }
DI void sf4(lp a, f32x4 v) { *(LAS f32x4*)a = v; }
DI s16x4 ltr(lp a) { return __builtin_amdgcn_ds_read_tr16_b64_v4i16((LAS s16x4*)a); }
DI v4u cat(s16x4 lo, s16x4 hi) { return __builtin_bit_cast(v4u, __builtin_shufflevector(lo, hi, 0, 1, 2, 3, 4, 5, 6, 7)); }
DI v4u ld16(__amdgpu_buffer_rsrc_t r, unsigned vo, unsigned so) { return __builtin_amdgcn_raw_buffer_load_b128(r, vo, so, 0); }
DI v4u ld16c(__amdgpu_buffer_rsrc_t r, unsigned vo, unsigned so) { return __builtin_amdgcn_raw_buffer_load_b128(r, vo, so, 16); }
DI unsigned ld4c(__amdgpu_buffer_rsrc_t r, unsigned vo) { return __builtin_amdgcn_raw_buffer_load_b32(r, vo, 0, 16); }
DI void st16c(v4u v, __amdgpu_buffer_rsrc_t r, unsigned vo) { __builtin_amdgcn_raw_buffer_store_b128(v, r, vo, 0, 16); }
DI void st4c(unsigned v, __amdgpu_buffer_rsrc_t r, unsigned vo) { __builtin_amdgcn_raw_buffer_store_b32(v, r, vo, 0, 16); }
#define BAR() asm volatile("s_waitcnt lgkmcnt(0)\n\ts_barrier" ::: "memory")
#define SB0() __builtin_amdgcn_sched_barrier(0)
#define WAITV(n) asm volatile("s_waitcnt vmcnt(%0)" ::"i"(n) : "memory")
#define FENCE() asm volatile("" ::: "memory")
template <int C> DI float dppf(float v) { return __int_as_float(__builtin_amdgcn_update_dpp(0, __float_as_int(v), C, 0xf, 0xf, true)); }
template <int N> DI f32x4 shr4(f32x4 v) { return f32x4{dppf<0x110 + N>(v.x), dppf<0x110 + N>(v.y), dppf<0x110 + N>(v.z), dppf<0x110 + N>(v.w)}; }
DI float sum8(float v) {
  v += dppf<0xB1>(v); v += dppf<0x4E>(v); v += dppf<0x141>(v);
  return v;
}
DI float dot2(unsigned a, unsigned b, float c) { return __builtin_amdgcn_fdot2_f32_bf16(__builtin_bit_cast(bf2, a), __builtin_bit_cast(bf2, b), c, false); }
DI void gather4(float x, float (&t)[4]) {
  const auto a = __builtin_amdgcn_permlane16_swap(__float_as_uint(x), __float_as_uint(x), false, false);
  const auto e = __builtin_amdgcn_permlane32_swap(a[0], a[0], false, false), o = __builtin_amdgcn_permlane32_swap(a[1], a[1], false, false);
  t[0] = __uint_as_float(e[0]); t[2] = __uint_as_float(e[1]); t[1] = __uint_as_float(o[0]); t[3] = __uint_as_float(o[1]);
}
DI void cm1(float& a, float& b, float lr, float li, float ur, float ui) {
  const float na = fmaf(lr, a, fmaf(-li, b, ur)), nb = fmaf(lr, b, fmaf(li, a, ui));
  a = na; b = nb;
}
DI f32x4 fma4(f32x4 a, f32x4 b, f32x4 c) { return __builtin_elementwise_fma(a, b, c); }
DI f32x4 sp4(float a) { return f32x4{a, a, a, a}; }
DI void cmac(f32x4& a, f32x4& b, f32x4 lr, f32x4 li, f32x4 ur, f32x4 ui) {
  const f32x4 na = fma4(lr, a, fma4(-li, b, ur)), nb = fma4(lr, b, fma4(li, a, ui));
  a = na; b = nb;
}

#define STAMP(i) do { if ((i) == ENCA) enc_a = (unsigned)__builtin_amdgcn_s_memrealtime(); if ((i) == ENCB) enc_b = (unsigned)__builtin_amdgcn_s_memrealtime(); \
  if ((i) == 22) { const unsigned e_ = (unsigned)__builtin_amdgcn_s_memrealtime(); if (rfl(threadIdx.x) == 0 && blockIdx.x == ENCW) while ((unsigned)__builtin_amdgcn_s_memrealtime() - e_ < 500u + 4u * (enc_b - enc_a)) __builtin_amdgcn_s_sleep(1); } } while (0)
#define STAMPC(i)

#define LSTAMP(i)
struct Par {
  const u16 *x, *lre, *lim, *bre, *bim, *cre, *cim, *ldt, *dsk, *wg, *wu, *wd;
  unsigned char* ws;
  float* out;
};
struct Ids { int R, g, cg, rt, ct; };

#define PDW 5
#define PDH 2
constexpr int DW = PDW, DH = PDH, NIT = 12, NL = 3;
#define PIN4(x) asm volatile("" : "+v"(x))
DI void sig(unsigned addr, unsigned v) {
  asm volatile("s_mov_b64 exec, 1\n\tds_write_b32 %0, %1\n\ts_mov_b64 exec, -1" ::"v"(addr), "v"(v) : "memory");
}
DI unsigned lrd(unsigned addr) { return *(volatile LAS unsigned*)LP(addr); }
#define LSLEEP 1
template <int SL = 1> DI void lwait(unsigned addr, unsigned v, unsigned need) {
  if (__builtin_expect(!__all(v >= need), 0)) {
    _Pragma("unroll 1") for (int spin = 0; spin < 64 * SPIN; ++spin) { FENCE();
      if (__all(lrd(addr) >= need)) break;
      __builtin_amdgcn_s_sleep(SL);
    }
  }
  FENCE();
}

struct WRing {
  v4u wb[DW][5];
  __amdgpu_buffer_rsrc_t rw;
  unsigned vl, wv;
  DI void load(int it) {
#pragma unroll
    for (int i = 0; i < 5; ++i) { wb[it % DW][i] = ld16c(rw, vl + (i & 3) * 1024, (8 * it + wv) * 5120 + (i >> 2) * 4096); SB0(); }
  }
  DI void dma01() {
    asm volatile("s_nop 4");
    dma_lin(MkSeq<20>::T{}, rw, L_W + (wv - 4) * 20480, vl, (wv - 4) * 20480);
  }
};

DI float wsum(float v) {
  v = sum8(v); v += dppf<0x128>(v);
  float t[4]; gather4(v, t);
  return (t[0] + t[1]) + (t[2] + t[3]);
}
DI unsigned mktag(unsigned epoch) { return rfl(epoch) * 0x9E3779B1u + 0x7F4A7C15u; }
struct RowIn { v4u xa[2]; unsigned xb[2], epoch; };
DI void row_load(const Par& p, const Ids& id, const int w, const int lane, RowIn& in) {
  const int row = 256 * id.R + 8 * id.cg + 2 * w;
#pragma unroll
  for (int z = 0; z < 2; ++z) { in.xa[z] = *(const v4u*)(p.x + (row + z) * D_ + lane * 8); in.xb[z] = *(const unsigned*)(p.x + (row + z) * D_ + 512 + lane * 2); }
  in.epoch = ld4c(mkrs(p.ws, WS_CTL_BYTES), WS_EPOCH);
  SB0();
}
DI unsigned row_stats(const Par& p, const Ids& id, const int w, const int lane, const RowIn& in) {
  const __amdgpu_buffer_rsrc_t rctl = mkrs(p.ws, WS_CTL_BYTES);
  const int row = 256 * id.R + 8 * id.cg + 2 * w;
  const v4u* xa = in.xa; const unsigned* xb = in.xb; const unsigned epoch = in.epoch;
  float v[2][10], mean[2], s2[2];
#pragma unroll
  for (int z = 0; z < 2; ++z) {
    const float t[10] = {bflo(xa[z].x), bfhi(xa[z].x), bflo(xa[z].y), bfhi(xa[z].y), bflo(xa[z].z), bfhi(xa[z].z), bflo(xa[z].w), bfhi(xa[z].w), bflo(xb[z]), bfhi(xb[z])};
#pragma unroll
    for (int e = 0; e < 10; ++e) v[z][e] = t[e];
    mean[z] = wsum(((t[0] + t[1]) + (t[2] + t[3])) + ((t[4] + t[5]) + (t[6] + t[7])) + (t[8] + t[9])) * (1.f / D_);
  }
#pragma unroll
  for (int z = 0; z < 2; ++z) {
    float a = 0.f, b = 0.f;
#pragma unroll
    for (int e = 0; e < 5; ++e) { a = fmaf(v[z][e] - mean[z], v[z][e] - mean[z], a); b = fmaf(v[z][5 + e] - mean[z], v[z][5 + e] - mean[z], b); }
    s2[z] = wsum(a + b);
  }
  const unsigned tag = mktag(epoch);
  const float m = rbf((lane & 1) ? mean[1] : mean[0]), rs = 1.f / rbf(sqrtf(rbf(rbf(((lane & 1) ? s2[1] : s2[0]) * (1.f / D_)) + 1e-5f)));
  st16c(v4u{__float_as_uint(m), tag, __float_as_uint(rs), tag}, rctl, lane < 2 ? WS_STATS + (row + lane) * 16 : OOB);
  return tag;
}

template <bool SSM>
DI unsigned compute_wave(const Par& p, const Ids& id, const int w, const int lane, const RowIn& rin, unsigned& enc_a, unsigned& enc_b) {
  STAMP(0);
  unsigned tag, cf = 0;
  if constexpr (!SSM) {
    const int r = lane & 31, hh = lane >> 5;
    const unsigned xa = (64 * w + r) * 128 + ((hh ^ ((r >> 1) & 7)) * 16);
    const unsigned wa = 32768 + ((lane >> 4) & 1) * UPO + (8 * hh + ((lane & 15) >> 2)) * 160 + (lane & 3) * 8;
    f32x16 acc[5][2] = {};
#pragma unroll
    for (int e = 0; e < 5; ++e) { PIN4(acc[e][0]); PIN4(acc[e][1]); }
    v4u fx[2][2]; s16x4 fl[4], fh[4];
    lp px[4], pw;
    auto setp = [&](unsigned sb) {
#pragma unroll
      for (int s = 0; s < 4; ++s) px[s] = LP(sb + (xa ^ (32 * s)));
      pw = LP(sb + wa);
    };
    auto woff = [&](int n) { return (n / 5) * 2560 + (n % 5) * 32; };
    setp(0);
    SB0();
    tag = row_stats(p, id, w, lane, rin);
    STAMP(17);
    SB0();
    BAR();
    STAMP(1);
    fx[0][0] = l128(px[0]); fx[0][1] = l128(px[0] + 4096);
#pragma unroll
    for (int n = 0; n < 3; ++n) { fl[n] = ltr(pw + woff(n)); fh[n] = ltr(pw + woff(n) + 640); }
    unsigned sb = 0;
#pragma unroll 1
    for (int i = 0; i < 10; ++i) {
#pragma unroll
      for (int n = 0; n < 20; ++n) {
        const int ks = n / 5, e = n % 5, nn = (n + 3) % 20;
        if (n == 17) { if (i < 9) BAR(); LSTAMP(i); sb = sb == 2 * STG ? 0 : sb + STG; setp(sb); SB0(); }
        const v4u wf = cat(fl[n & 3], fh[n & 3]);
        acc[e][0] = mfma32(wf, fx[ks & 1][0], acc[e][0]); SB0();
        fl[(n + 3) & 3] = ltr(pw + woff(nn));
        fh[(n + 3) & 3] = ltr(pw + woff(nn) + 640); SB0();
        acc[e][1] = mfma32(wf, fx[ks & 1][1], acc[e][1]); SB0();
        if (ks < 3 && e == 1) { fx[(ks + 1) & 1][0] = l128(px[ks + 1]); fx[(ks + 1) & 1][1] = l128(px[ks + 1] + 4096); }
        if (n == 17) { fx[0][0] = l128(px[0]); fx[0][1] = l128(px[0] + 4096); }
        if (n == 18) cf = lrd(L_CSF + (lane & 3) * 4);
        SB0();
      }
    }
    lwait(L_CSF + (lane & 3) * 4, cf, 1);
    STAMP(12);
    const __amdgpu_buffer_rsrc_t rh = mkrs(p.ws + WS_H + (unsigned)(4 * id.R + w) * (NKB * 4096), 54 * 4096);
    const unsigned so0 = (r >> 4) * 1024 + (hh * 16 + (r & 15)) * 16;
    float mu[2], r2[2];
#pragma unroll
    for (int u = 0; u < 2; ++u) {
      const int F = 4 * w + 2 * u + (r >> 4);
      const f32x2 s = *(LAS f32x2*)LP(L_STATS + (128 * (F >> 3) + 8 * (r & 15) + (F & 7)) * 8);
      mu[u] = s.x; r2[u] = s.y * s.y;
    }
    const lp pc = LP(L_CS + hh * 16);
#pragma unroll
    for (int e = 0; e < 5; ++e) {
      const f32x4 g0 = lf4(pc + e * 64) + lf4(pc + e * 64 + 640), g1 = lf4(pc + e * 64 + 32) + lf4(pc + e * 64 + 672);
      const f32x4 u0 = lf4(pc + e * 64 + 320) + lf4(pc + e * 64 + 960), u1 = lf4(pc + e * 64 + 352) + lf4(pc + e * 64 + 992);
      const int T = 5 * id.g + e;
      const unsigned so = so0 + (T >> 1) * 4096 + (T & 1) * 512;
#pragma unroll
      for (int u = 0; u < 2; ++u) {
        const f32x16 a = acc[e][u];
        const f32x4 nm = sp4(-mu[u]);
        const f32x4 h0 = fma4(nm, g0, f32x4{a[0], a[1], a[2], a[3]}) * fma4(nm, u0, f32x4{a[8], a[9], a[10], a[11]}) * r2[u];
        const f32x4 h1 = fma4(nm, g1, f32x4{a[4], a[5], a[6], a[7]}) * fma4(nm, u1, f32x4{a[12], a[13], a[14], a[15]}) * r2[u];
        st16c(v4u{pk2(h0.x, h0.y), pk2(h0.z, h0.w), pk2(h1.x, h1.y), pk2(h1.z, h1.w)}, rh, so + u * 2048);
      }
    }
  } else {
    const int c = lane & 15, q = lane >> 4, G = w >> 1, blk = w & 1;
    const unsigned xa = (128 * G + c) * 128 + ((q ^ (c >> 1)) * 16);
    const unsigned wa = 32768 + (32 * blk + c) * 128 + ((q ^ (c >> 1)) * 16);
    f32x4 acc[8][4] = {};
#pragma unroll
    for (int j = 0; j < 8; ++j)
#pragma unroll
      for (int t = 0; t < 4; ++t) PIN4(acc[j][t]);
    v4u fw[2][4], fx[4];
    lp px[2], pw[2];
    auto setp = [&](unsigned sb) { px[0] = LP(sb + xa); px[1] = LP(sb + (xa ^ 64)); pw[0] = LP(sb + wa); pw[1] = LP(sb + (wa ^ 64)); };
    auto woff = [&](int t) { return (t >> 1) * 8192 + (t & 1) * 2048; };
    setp(0);
    SB0();
    tag = row_stats(p, id, w, lane, rin);
    STAMP(17);
    SB0();
    BAR();
    STAMP(1);
#pragma unroll
    for (int t = 0; t < 4; ++t) fw[0][t] = l128(pw[0] + woff(t));
#pragma unroll
    for (int n = 0; n < 3; ++n) fx[n] = l128(px[0] + n * 2048);
    unsigned sb = 0;
#pragma unroll 1
    for (int i = 0; i < 10; ++i) {
#pragma unroll
      for (int n = 0; n < 16; ++n) {
        const int kk = n >> 3, j = n & 7, nn = (n + 3) & 15;
        if (n == 13) { if (i < 9) BAR(); LSTAMP(i); sb = sb == 2 * STG ? 0 : sb + STG; setp(sb); SB0(); }
        const v4u xf = fx[n & 3];
        acc[j][0] = mfma16(fw[kk][0], xf, acc[j][0]); SB0();
        fx[(n + 3) & 3] = l128(px[nn >> 3] + (nn & 7) * 2048); SB0();
        acc[j][1] = mfma16(fw[kk][1], xf, acc[j][1]); SB0();
        if (n >= 2 && n < 6) fw[1][n - 2] = l128(pw[1] + woff(n - 2));
        if (n == 13) fw[0][0] = l128(pw[0] + woff(0));
        if (n == 14) fw[0][2] = l128(pw[0] + woff(2));
        SB0();
        acc[j][2] = mfma16(fw[kk][2], xf, acc[j][2]); SB0();
        if (n == 13) fw[0][1] = l128(pw[0] + woff(1));
        if (n == 14) fw[0][3] = l128(pw[0] + woff(3));
        SB0();
        acc[j][3] = mfma16(fw[kk][3], xf, acc[j][3]); SB0();
        if (n == 14) { cf = lrd(L_CSF + (lane & 3) * 4); SB0(); }
      }
    }
    lwait(L_CSF + (lane & 3) * 4, cf, 1);
    STAMP(12);
    const lp pe = LP(L_E + (16 * G + c) * ESTR + (32 * blk + 8 * q) * 4);
    const lp pk = LP(L_CST + (32 * blk + 8 * q) * 4);
    f32x4 lr[2], li[2], er[2], ei[2];
    {
      const lp ps = LP(L_STATS + (128 * G + 8 * c) * 8), pc = LP(L_CS + (32 * blk + 8 * q) * 4);
      f32x4 cs[4], st[4];
#pragma unroll
      for (int t = 0; t < 4; ++t) { cs[t] = lf4(pc + (t >> 1) * 320 + (t & 1) * 16) + lf4(pc + 640 + (t >> 1) * 320 + (t & 1) * 16); st[t] = lf4(ps + t * 16); }
#pragma unroll
      for (int ab = 0; ab < 2; ++ab) { lr[ab] = lf4(pk + ab * 16); li[ab] = lf4(pk + 256 + ab * 16); }
#pragma unroll
      for (int jj = 0; jj < 4; ++jj)
#pragma unroll
        for (int t = 0; t < 4; ++t) {
          acc[2 * jj][t] = fma4(sp4(-st[jj].x), cs[t], acc[2 * jj][t]) * st[jj].y;
          acc[2 * jj + 1][t] = fma4(sp4(-st[jj].z), cs[t], acc[2 * jj + 1][t]) * st[jj].w;
        }
    }
#pragma unroll
    for (int ab = 0; ab < 2; ++ab) { er[ab] = acc[0][ab]; ei[ab] = acc[0][2 + ab]; }
#pragma unroll
    for (int j = 1; j < 8; ++j)
#pragma unroll
      for (int ab = 0; ab < 2; ++ab) cmac(er[ab], ei[ab], lr[ab], li[ab], acc[j][ab], acc[j][2 + ab]);
#pragma unroll
    for (int ab = 0; ab < 2; ++ab) { sf4(pe + ab * 16, er[ab]); sf4(pe + 256 + ab * 16, ei[ab]); }
    SB0();
    BAR();
    STAMP(13);
#pragma unroll
    for (int ab = 0; ab < 2; ++ab) {
      const f32x4 kr = lf4(pk + 512 * 4 + ab * 16), ki = lf4(pk + 512 * 4 + 256 + ab * 16);
#pragma unroll
      for (int j = 0; j < 8; ++j) {
        const f32x4 ur = acc[j][ab], ui = acc[j][2 + ab];
        acc[j][ab] = fma4(-ki, ui, kr * ur); acc[j][2 + ab] = fma4(-ki, ur, -kr * ui);
        PIN4(acc[j][ab]); PIN4(acc[j][2 + ab]);
      }
      li[ab] = -li[ab];
    }
    const __amdgpu_buffer_rsrc_t rh = mkrs(p.ws + WS_H + (unsigned)(4 * id.R + 2 * G) * (NKB * 4096), 2 * NKB * 4096);
    unsigned vo[4];
#pragma unroll
    for (int z = 0; z < 4; ++z) { vo[z] = (54 + 2 * id.g + blk + 20 * (z & 1) + NKB * (z >> 1)) * 4096 + lane * 16; PIN4(vo[z]); }
    SB0();
    BAR();
    STAMP(14);
#pragma unroll
    for (int ab = 0; ab < 2; ++ab) { er[ab] = lf4(pe + ab * 16); ei[ab] = lf4(pe + 256 + ab * 16); }
#pragma unroll
    for (int j = 0; j < 8; ++j) {
#pragma unroll
      for (int ab = 0; ab < 2; ++ab) cmac(er[ab], ei[ab], lr[ab], li[ab], acc[j][ab], acc[j][2 + ab]);
      st16c(v4u{pk2(er[0].x, er[0].y), pk2(er[0].z, er[0].w), pk2(er[1].x, er[1].y), pk2(er[1].z, er[1].w)}, rh, vo[2 * (j >> 2)] + (j & 3) * 1024);
      st16c(v4u{pk2(ei[0].x, ei[0].y), pk2(ei[0].z, ei[0].w), pk2(ei[1].x, ei[1].y), pk2(ei[1].z, ei[1].w)}, rh, vo[2 * (j >> 2) + 1] + (j & 3) * 1024);
    }
  }
  SB0();
  STAMP(15);
  return tag;
}

#define HBAR() do { SB0(); BAR(); SB0(); } while (0)
struct HState { f32x4 acs[5]; unsigned pl, pr, pi; };
template <bool SSM>
DI unsigned helper_wave(const Par& p, const Ids& id, const int h, const int lane, HState& hs, unsigned& enc_a, unsigned& enc_b) {
  STAMP(0);
  constexpr int NWP = SSM ? 4 : 5, NP = 8 + NWP;
  constexpr unsigned WSTEP = SSM ? 128u : 64u * 3456u;
  const int c = lane & 15, q = lane >> 4;
  const __amdgpu_buffer_rsrc_t rctl = mkrs(p.ws, WS_CTL_BYTES);
  const __amdgpu_buffer_rsrc_t rx = mkrs(p.x, L_ * D_ * 2);
  const __amdgpu_buffer_rsrc_t rw = mkrs(SSM ? ((h >> 1) ? p.bim : p.bre) : ((h >> 1) ? p.wu : p.wg), SSM ? P_ * D_ * 2 : D_ * F_ * 2);
  const unsigned v0 = (8 * (lane >> 3)) * 1280 + (((lane & 7) ^ (lane >> 4)) * 16), v1 = v0 ^ 64;
  unsigned st = 0;
  const unsigned sx = (256 * id.R + 128 * (h >> 1) + 4 * (h & 1)) * 1280;
  auto issue_x = [&](unsigned s, unsigned sb) {
    dma_x(MkSeq<8>::T{}, rx, sb + h * 8192, v0, v1, s);
  };
  unsigned vw[NWP];
  if constexpr (SSM) {
    vw[0] = (8 * (lane >> 5) + ((lane >> 3) & 3)) * 1280 + (((lane & 7) ^ (lane >> 4)) * 16); vw[1] = vw[0] ^ 64;
  } else {
#pragma unroll
    for (int i = 0; i < 5; ++i) { const int lc = 64 * (5 * (h & 1) + i) + lane, pos = lc / 10; vw[i] = pos * 3456 + (lc - pos * 10) * 16; }
  }
  const unsigned sw = SSM ? (64 * id.g + 32 * (h & 1)) * 1280 : 160 * id.g;
  const unsigned dw = 32768 + (h >> 1) * (SSM ? 8192 : UPO) + (h & 1) * NWP * 1024;
  auto issue_w = [&](unsigned s, unsigned sb) {
    if constexpr (SSM) dma_b(MkSeq<4>::T{}, rw, sb + dw, vw, s); else dma_g(MkSeq<5>::T{}, rw, sb + dw, vw, s);
  };
  asm volatile("s_nop 4");
  issue_w(sw, 0);
  unsigned sb = STG;
  auto step = [&]() {
    ++st;
    issue_x(sx + st * 128, sb); issue_w(sw + st * WSTEP, sb);
    sb = sb == 2 * STG ? 0 : sb + STG;
  };
  step();
  if (h == 3 && lane < 8) *(LAS unsigned*)LP(L_CNT + lane * 4) = 0u;
  f32x4 acs[NWP] = {};
  v4u fc[NWP];
  const unsigned ca = SSM ? 32768 + (h >> 1) * 8192 + c * 128 + (((4 * (h & 1) + q) ^ (c >> 1)) * 16)
                          : 32768 + (h >> 1) * UPO + (32 * (h & 1) + 4 * q + (c >> 2)) * 160 + (c & 3) * 8;
  const v4u ones = {0x3F803F80u, 0x3F803F80u, 0x3F803F80u, 0x3F803F80u};
  auto cs_rd = [&](unsigned s) {
    const lp a = LP(s + ca);
#pragma unroll
    for (int t = 0; t < NWP; ++t) { if constexpr (SSM) fc[t] = l128(a + t * 2048); else fc[t] = cat(ltr(a + t * 32), ltr(a + t * 32 + 2560)); }
    SB0();
  };
  auto cs_mm = [&]() {
    SB0();
    __builtin_amdgcn_s_setprio(2);
#pragma unroll
    for (int t = 0; t < NWP; ++t) acs[t] = mfma16(fc[t], ones, acs[t]);
    __builtin_amdgcn_s_setprio(0);
    SB0();
  };
  constexpr int NF = SSM ? 5 : 4;
#define JL 1
  const int tsk = (id.g * 8 + id.R) * 4 + h, kb = SSM ? 4 * id.g + h : tsk / 10, dq = tsk - 10 * (tsk / 10);
  v4u f[5] = {}, sel[2], sv = {}, fl = {};
  unsigned fvo, fso[NF];
  const __amdgpu_buffer_rsrc_t rf = mkrs(SSM ? (kb < 20 ? p.cre : p.cim) : p.wd, SSM ? D_ * P_ * 2 : F_ * D_ * 2);
  const __amdgpu_buffer_rsrc_t r2 = mkrs(p.ws + WS_W2, (SSM || tsk < 540) ? WS_W2_BYTES : 0);
  {
    fvo = SSM ? (80 * id.R + c) * 1280 + (kb < 20 ? kb : kb - 20) * 64 + q * 16
              : (32 * kb + 16 * ((lane >> 3) & 1) + 4 * (q & 1) + (q >> 1)) * 1280 + dq * 128 + (lane & 7) * 16;
#pragma unroll
    for (int i = 0; i < NF; ++i) {
      const int dt = 4 * dq + 2 * (q & 1) + (i >> 1), ct = dt / 5;
      fso[i] = SSM ? ((id.R * NKB + 54 + kb) * 5 + i) * 1024 + lane * 16
                   : ((ct * NKB + kb) * 5 + dt - 5 * ct) * 1024 + (16 * (2 * (q >> 1) + (c >> 3)) + 8 * (i & 1) + (c & 7)) * 16;
      PIN4(fso[i]);
    }
#pragma unroll
    for (int hf = 0; hf < 2; ++hf) {
#pragma unroll
      for (int e = 0; e < 4; ++e) sel[hf][e] = (8 * q + 2 * e == 16 * hf + c ? 0x3F80u : 0u) | (8 * q + 2 * e + 1 == 16 * hf + c ? 0x3F800000u : 0u);
      if constexpr (!SSM) PIN4(sel[hf]);
    }
    PIN4(fvo);
  }
  unsigned epoch, pl = 0, pr = 0, pi = 0, tag = 0;
  unsigned so = WS_STATS + (256 * id.R + 64 * h + lane) * 16, flo = h == 3 ? WS_FLW + lane * 16 : OOB, fwo = (h == 3 && lane == 0) ? WS_FLW + blockIdx.x * 4 : OOB;
  PIN4(so); PIN4(flo); PIN4(fwo);
  asm volatile("buffer_load_dword %0, off, %1, 0 sc1" : "=v"(epoch) : "s"(rctl));
  if constexpr (SSM) {
    const unsigned po = (64 * id.g + lane) * 2;
    asm volatile("buffer_load_ushort %0, %3, %4, 0 offen\n\tbuffer_load_ushort %1, %3, %5, 0 offen\n\tbuffer_load_ushort %2, %3, %6, 0 offen"
                 : "=&v"(pl), "=&v"(pr), "=&v"(pi) : "v"(po), "s"(mkrs(p.ldt, P_ * 2)), "s"(mkrs(p.lre, P_ * 2)), "s"(mkrs(p.lim, P_ * 2)));
  }
  WAITV(NP + (SSM ? 4 : 1)); BAR();
#pragma unroll
  for (int j = 0; j < 8; ++j) {
    cs_rd((j % 3) * STG);
    if (j == JL + 4) { tag = mktag(epoch); st4c(tag, rctl, fwo); }
    if (j == 6) asm volatile("buffer_load_dwordx4 %0, %1, %2, 0 offen sc1" : "=v"(sv) : "v"(so), "s"(rctl));
    if (!SSM && j == 7) asm volatile("buffer_load_dwordx4 %0, %1, %2, 0 offen sc1" : "=v"(fl) : "v"(flo), "s"(rctl));
    SB0();
    step();
    if constexpr (SSM) {
      if (j == JL - 1) asm volatile("buffer_load_dwordx4 %0, %2, %3, 0 offen\n\tbuffer_load_dwordx4 %1, %2, %3, %4 offen" : "=&v"(f[0]), "=&v"(f[1]) : "v"(fvo), "s"(rf), "s"(16 * 1280));
      if (j == JL) asm volatile("buffer_load_dwordx4 %0, %3, %4, %5 offen\n\tbuffer_load_dwordx4 %1, %3, %4, %6 offen\n\tbuffer_load_dwordx4 %2, %3, %4, %7 offen"
                                : "=&v"(f[2]), "=&v"(f[3]), "=&v"(f[4]) : "v"(fvo), "s"(rf), "s"(32 * 1280), "s"(48 * 1280), "s"(64 * 1280));
    } else if (j == JL)
      asm volatile("buffer_load_dwordx4 %0, %4, %5, 0 offen\n\tbuffer_load_dwordx4 %1, %4, %5, 0 offen offset:2560\n\tbuffer_load_dwordx4 %2, %4, %5, %6 offen\n\tbuffer_load_dwordx4 %3, %4, %5, %6 offen offset:2560"
                   : "=&v"(f[0]), "=&v"(f[1]), "=&v"(f[2]), "=&v"(f[3]) : "v"(fvo), "s"(rf), "s"(8 * 1280));
    cs_mm();
    if (j == JL + 2) {
      if constexpr (SSM) {
#pragma unroll
        for (int i = 0; i < NF; ++i) st16c(f[i], r2, fso[i]);
      } else {
        f32x4 d[8];
        __builtin_amdgcn_s_setprio(2);
#pragma unroll
        for (int z = 0; z < 8; ++z) d[z] = mfma16(f[z >> 1], sel[z & 1], f32x4{0.f, 0.f, 0.f, 0.f});
#pragma unroll
        for (int i = 0; i < 4; ++i) st16c(v4u{pk2(d[0][i], d[1][i]), pk2(d[2][i], d[3][i]), pk2(d[4][i], d[5][i]), pk2(d[6][i], d[7][i])}, r2, fso[i]);
        __builtin_amdgcn_s_setprio(0);
      }
      SB0();
    }
    if (j == JL + 2) WAITV(NP + NF);
    else if (j == JL) WAITV(NP + (SSM ? 5 : 4));
    else if (SSM && j == JL - 1) WAITV(NP + 2);
    else if (j == JL + 1) asm volatile("s_waitcnt vmcnt(%9)" : "+v"(f[0]), "+v"(f[1]), "+v"(f[2]), "+v"(f[3]), "+v"(f[4]), "+v"(epoch), "+v"(pl), "+v"(pr), "+v"(pi) : "i"(NP) : "memory");
    else if (j == 6) asm volatile("s_waitcnt vmcnt(%1)" : "+v"(sv) : "i"(NP) : "memory");
    else if (j == 7) asm volatile("s_waitcnt vmcnt(%1)" : "+v"(fl) : "i"(NP) : "memory");
    else if (j == JL + 4) WAITV(NP + 1);
    else WAITV(NP);
    if (j == 6) {
      v2u o = {sv.x, sv.z};
      if (!__all((sv.y == tag) & (sv.w == tag))) {
        _Pragma("unroll 1") for (int spin = 0; spin < SPIN; ++spin) { FENCE();
          const v4u s2 = ld16c(rctl, so, 0);
          o = v2u{s2.x, s2.z};
          if (__all((s2.y == tag) & (s2.w == tag))) break;
          __builtin_amdgcn_s_sleep(4);
        }
      }
      *(LAS v2u*)LP(L_STATS + (64 * h + lane) * 8) = o;
    }
    if (!SSM && j == 7 && h == 3) {
      if (!__all((fl.x == tag) & (fl.y == tag) & (fl.z == tag) & (fl.w == tag))) {
        _Pragma("unroll 1") for (int spin = 0; spin < SPIN; ++spin) { FENCE();
          const v4u f2 = ld16c(rctl, flo, 0);
          if (__all((f2.x == tag) & (f2.y == tag) & (f2.z == tag) & (f2.w == tag))) break;
          __builtin_amdgcn_s_sleep(4);
        }
      }
      st4c(tag, rctl, (lane == 0 && blockIdx.x == 255) ? WS_EPOCH : OOB);
    }
    BAR();
  }
#pragma unroll
  for (int t = 0; t < 5; ++t) hs.acs[t] = acs[t < NWP ? t : 0];
  hs.pl = pl; hs.pr = pr; hs.pi = pi;
  return tag;
}
template <bool SSM>
DI void helper_tail(const Par& p, const Ids& id, const int h, const int lane, const unsigned tag, HState& hs) {
  constexpr int NWP = SSM ? 4 : 5;
  const int c = lane & 15, q = lane >> 4;
  const __amdgpu_buffer_rsrc_t rctl = mkrs(p.ws, WS_CTL_BYTES);
  f32x4* acs = hs.acs;
  const unsigned pl = hs.pl, pr = hs.pr, pi = hs.pi;
  v4u fc[NWP];
  const unsigned ca = SSM ? 32768 + (h >> 1) * 8192 + c * 128 + (((4 * (h & 1) + q) ^ (c >> 1)) * 16)
                          : 32768 + (h >> 1) * UPO + (32 * (h & 1) + 4 * q + (c >> 2)) * 160 + (c & 3) * 8;
  const v4u ones = {0x3F803F80u, 0x3F803F80u, 0x3F803F80u, 0x3F803F80u};
  auto cs_rd = [&](unsigned s) {
    const lp a = LP(s + ca);
#pragma unroll
    for (int t = 0; t < NWP; ++t) { if constexpr (SSM) fc[t] = l128(a + t * 2048); else fc[t] = cat(ltr(a + t * 32), ltr(a + t * 32 + 2560)); }
    SB0();
  };
  auto cs_mm = [&]() {
    SB0();
    __builtin_amdgcn_s_setprio(2);
#pragma unroll
    for (int t = 0; t < NWP; ++t) acs[t] = mfma16(fc[t], ones, acs[t]);
    __builtin_amdgcn_s_setprio(0);
    SB0();
  };
  cs_rd(2 * STG);
  if (SSM) {
    if (h == 3) {
      _Pragma("unroll 1") for (int spin = 0; spin < SPIN; ++spin) { FENCE();
        const v4u f2 = ld16c(rctl, WS_FLW + lane * 16, 0);
        if (__all((f2.x == tag) & (f2.y == tag) & (f2.z == tag) & (f2.w == tag))) break;
        __builtin_amdgcn_s_sleep(4);
      }
    }
  }
  float br = 0.f, bi = 0.f;
  if (SSM && h == 2) {
    const float dt = rbf(expf(bf2f(pl))), ex = expf(dt * bf2f(pr));
    float sn, cn;
    sincosf(dt * bf2f(pi), &sn, &cn);
    br = ex * cn; bi = ex * sn;
  }
  cs_mm();
  WAITV(SSM ? 0 : 15); BAR();
  cs_rd(0);
  if (SSM && h == 2) {
    const float lr = bf2f(pr), li = bf2f(pi), nr = br - 1.f, ni = bi, den = lr * lr + li * li;
    LAS float* cst = (LAS float*)LP(L_CST);
    cst[4 * 128 + lane] = 2.f * (nr * lr + ni * li) / den; cst[4 * 128 + 64 + lane] = 2.f * (ni * lr - nr * li) / den;
    cst[lane] = br; cst[64 + lane] = bi;
#pragma unroll
    for (int s = 1; s <= 8; ++s) {
      const float a = br * br - bi * bi, b = 2.f * br * bi;
      br = a; bi = b;
      const int k = s == 3 ? 1 : s == 6 ? 2 : s == 8 ? 3 : 0;
      if (k) { cst[k * 128 + lane] = br; cst[k * 128 + 64 + lane] = bi; }
    }
  }
  cs_mm();
  if (c == 0) {
#pragma unroll
    for (int t = 0; t < NWP; ++t) {
      const int col = SSM ? 32 * (t >> 1) + 8 * q + 4 * (t & 1) : 16 * t + 4 * q;
      sf4(LP(L_CS + ((h & 1) * 160 + (h >> 1) * 80 + col) * 4), acs[t]);
    }
  }
  sig(L_CSF + h * 4, 1);
}

DI void carries(const Par& p, const Ids& id, const int h, const int lane, const unsigned tag, WRing& wr) {
  const __amdgpu_buffer_rsrc_t rctl = mkrs(p.ws, WS_CTL_BYTES);
  const int c = lane & 15, q = lane >> 4;
  {
    const int ch = 16 * h + c;
    lwait<2>(L_CSF + 8, 0, 1);
    LAS float* cst = (LAS float*)LP(L_CST);
    const float l8r = cst[128 + ch], l8i = cst[192 + ch], l64r = cst[256 + ch], l64i = cst[320 + ch], lwr = cst[384 + ch], lwi = cst[448 + ch];
    const float kr = cst[512 + ch], ki = cst[576 + ch];
    float w0r = 1.f, w0i = 0.f, w1r = 1.f, w1i = 0.f, qr = kr, qi = ki;
#pragma unroll
    for (int s = 0; s < 6; ++s) {
      float a = w0r, b = w0i; cm1(a, b, lwr, lwi, 0.f, 0.f);
      if (s < id.R - 1 - q) { w0r = a; w0i = b; }
      if (s < id.R - 5 - q) { w1r = a; w1i = b; }
    }
#pragma unroll
    for (int s = 0; s < 3; ++s) { float a = qr, b = qi; cm1(a, b, l64r, l64i, 0.f, 0.f); if (s < q) { qr = a; qi = b; } }
    const __amdgpu_buffer_rsrc_t rag = mkrs(p.ws + WS_AGG + id.R * (8 * 640 * 16), id.R * 640 * 16);
    LAS float* pe = (LAS float*)LP(L_E + 8 * q * ESTR + ch * 4);
    asm volatile("" : "+v"(w0r), "+v"(w0i), "+v"(w1r), "+v"(w1i), "+v"(qr), "+v"(qi));
    unsigned ao[2], po = (q * 640 + 64 * id.g + ch) * 16;
#pragma unroll
    for (int z = 0; z < 2; ++z) ao[z] = q + 4 * z > id.R ? WS_AGG + (((q + 4 * z) * 8 + id.R) * 640 + 64 * id.g + ch) * 16 : OOB;
    asm volatile("" : "+v"(ao[0]), "+v"(ao[1]), "+v"(po));
    __builtin_amdgcn_s_setprio(3);
    HBAR();
    float er[8], ei[8];
#pragma unroll
    for (int s = 0; s < 8; ++s) { er[s] = pe[s * (ESTR / 4)]; ei[s] = pe[s * (ESTR / 4) + 64]; }
    float a = er[0], b = ei[0];
#pragma unroll
    for (int s = 1; s < 8; ++s) cm1(a, b, l8r, l8i, er[s], ei[s]);
    float ta[4], tb[4];
    gather4(a, ta); gather4(b, tb);
    float pa = 0.f, pb = 0.f;
    a = ta[0]; b = tb[0];
#pragma unroll
    for (int s = 1; s < 4; ++s) {
      if (q == s) { pa = a; pb = b; }
      cm1(a, b, l64r, l64i, ta[s], tb[s]);
    }
#pragma unroll
    for (int z = 0; z < 2; ++z)
      st16c(v4u{__float_as_uint(a), tag, __float_as_uint(b), tag}, rctl, ao[z]);
    SB0();
    wr.dma01();
    v4u p0 = ld16c(rag, po, 0), p1 = ld16c(rag, po, 4 * 640 * 16);
    SB0();
    cm1(pa, pb, kr, ki, 0.f, 0.f);
#pragma unroll
    for (int s = 0; s < 7; ++s) cm1(er[s], ei[s], kr, ki, 0.f, 0.f);
    _Pragma("unroll 1") for (int spin = 0; spin < SPIN; ++spin) { FENCE();
      const bool ok = ((q >= id.R) | ((p0.y == tag) & (p0.w == tag))) & ((q + 4 >= id.R) | ((p1.y == tag) & (p1.w == tag)));
      if (__all(ok)) break;
      p0 = ld16c(rag, po, 0); p1 = ld16c(rag, po, 4 * 640 * 16);
    }
    float ca = __uint_as_float(p1.x), cb = __uint_as_float(p1.z);
    cm1(ca, cb, w1r, w1i, 0.f, 0.f);
    { float x = __uint_as_float(p0.x), y = __uint_as_float(p0.z); cm1(x, y, w0r, w0i, ca, cb); ca = x; cb = y; }
    gather4(ca, ta); gather4(cb, tb);
    ca = (ta[0] + ta[1]) + (ta[2] + ta[3]); cb = (tb[0] + tb[1]) + (tb[2] + tb[3]);
    cm1(ca, cb, qr, qi, pa, pb);
#pragma unroll
    for (int s = 0; s < 8; ++s) {
      pe[s * (ESTR / 4)] = ca; pe[s * (ESTR / 4) + 64] = -cb;
      if (s < 7) cm1(ca, cb, l8r, l8i, er[s], ei[s]);
    }
    __builtin_amdgcn_s_setprio(0);
    HBAR();
  }
}

__global__ __launch_bounds__(512, 2) void fused(Par p) {
  unsigned enc_a = 0, enc_b = 0;
  STAMP(30);
  __builtin_amdgcn_sched_barrier(0);
  const int bid = blockIdx.x, xcd = bid & 7, idx = bid >> 3, a = xcd >> 2, b = xcd & 3, s = idx >> 2;
  const int wv = rfl(threadIdx.x >> 6), lane = threadIdx.x & 63;
  Ids id;
  const bool ssm = idx < 10;
  const int part = s == 2 ? 1 : 0;
  id.R = 4 * a + (idx & 1) + 2 * ((part ? b : idx >> 1) & 1);
  const int gl = (9 - idx) >> 31;
  id.g = ((2 - 3 * gl) * b + s + 3 * gl) * (1 - part) + (8 - 12 * gl + (b >> 1)) * part;
  id.cg = id.g - 10 * gl;
  asm volatile("" ::"s"(p.x), "s"(p.ws), "s"(p.wg), "s"(p.wd), "s"(id.cg), "s"(id.R));
  RowIn rin;
  if (wv >= 4) {
    const int h = wv - 4;
    const unsigned v0 = (8 * (lane >> 3)) * 1280 + (((lane & 7) ^ (lane >> 4)) * 16);
    asm volatile("s_nop 4");
    dma_x(MkSeq<8>::T{}, mkrs(p.x, L_ * D_ * 2), h * 8192, v0, v0 ^ 64, (256 * id.R + 128 * (h >> 1) + 4 * (h & 1)) * 1280);
  }
  unsigned cpf = 0;
  if (wv == 3) {
    unsigned long long pc; unsigned len;
    asm volatile("s_getpc_b64 %0\n.Lpf_here:\n\ts_mov_b32 %1, .Lpf_end-.Lpf_here" : "=s"(pc), "=s"(len));
    cpf = __builtin_amdgcn_raw_buffer_load_b32(mkrs((const void*)pc, len), lane < 13 ? (idx * 13 + lane) * 128 : OOB, 0, 0);
  }
  id.rt = 16 * a + 8 * (b >> 1) + (idx >> 2);
  id.ct = 4 * (b & 1) + (idx & 3);
  const __amdgpu_buffer_rsrc_t rctl = mkrs(p.ws, WS_CTL_BYTES);
  const __amdgpu_buffer_rsrc_t rw = mkrs(p.ws + WS_W2 + (unsigned)id.ct * (NKB * 5120), NKB * 5120);
  const unsigned vl = lane * 16;
  const unsigned fo = WS_FLH + (id.rt * 8 + id.ct) * 128 + (lane & 31) * 4;
  WRing wr;
  wr.rw = rw; wr.vl = vl; wr.wv = wv;
  unsigned tag;
  if (wv < 4) {
    row_load(p, id, wv, lane, rin);
    if (wv == 2) cpf = ld4c(rctl, lane < 32 ? fo : lane < 40 ? WS_FLW + (lane - 32) * 128 : OOB);
    tag = ssm ? compute_wave<true>(p, id, wv, lane, rin, enc_a, enc_b) : compute_wave<false>(p, id, wv, lane, rin, enc_a, enc_b);
    WAITV(0);
    unsigned old = 0;
    if (lane == 0) old = __hip_atomic_fetch_add((LAS unsigned*)LP(L_CNT), 1u, __ATOMIC_RELAXED, __HIP_MEMORY_SCOPE_WORKGROUP);
    st4c(tag, rctl, (lane < 32 && rfl(old) == 3) ? WS_FLH + (id.R * 32 + lane) * 128 + id.cg * 4 : OOB);
    asm volatile("" ::"v"(cpf));
    STAMP(16);
#pragma unroll
    for (int it = NL; it < NL + DW; ++it) wr.load(it);
  } else {
    HState hs;
    tag = ssm ? helper_wave<true>(p, id, wv - 4, lane, hs, enc_a, enc_b) : helper_wave<false>(p, id, wv - 4, lane, hs, enc_a, enc_b);
    if (ssm) helper_tail<true>(p, id, wv - 4, lane, tag, hs);
    wr.load(NL); wr.load(NL + 1); wr.load(NL + 2);
    if (!ssm) helper_tail<false>(p, id, wv - 4, lane, tag, hs);
#pragma unroll
    for (int it = NL + 3; it < NL + DW; ++it) wr.load(it);
    if (ssm) carries(p, id, wv - 4, lane, tag, wr); else wr.dma01();
    lwait<2>(L_CNT, 0, 4);
    STAMP(16);
    if (wv > 4) {
      asm volatile("s_nop 4");
      dma_lin(MkSeq<14>::T{}, rw, (wv - 5) * 13312, vl, 81920 + (wv - 5) * 13312);
    } else {
      _Pragma("unroll 1") for (int spin = 0; spin < SPIN; ++spin) { FENCE();
        if (__all((ld4c(rctl, fo) == tag) | ((lane & 31) < 10))) break;
        __builtin_amdgcn_s_sleep(2);
      }
      sig(L_GO, 1);
    }
  }

  const __amdgpu_buffer_rsrc_t rh = mkrs(p.ws + WS_H + (unsigned)id.rt * (NKB * 4096), NKB * 4096);
  v4u hb[DH][4];
  auto loadh = [&](int it) {
#pragma unroll
    for (int j = 0; j < 4; ++j) { hb[it % DH][j] = ld16c(rh, vl + j * 1024, (8 * it + wv) * 4096); SB0(); }
  };
  lwait<1>(L_GO, 0, 1);
  SB0();
  STAMP(20);
#pragma unroll
  for (int it = 0; it < DH; ++it) { loadh(it); SB0(); }
  f32x4 acc[4][5];
#pragma unroll
  for (int j = 0; j < 4; ++j)
#pragma unroll
    for (int i = 0; i < 5; ++i) acc[j][i] = f32x4{0.f, 0.f, 0.f, 0.f};
  const int tid = wv * 64 + lane;
  v2u xv[3], dv[3]; v4u sv[3]; int oo[3], ra[3];
  unsigned sfl = 0;
  const lp pw = LP(L_W + wv * 5120 + vl), pw2 = LP(wv * 5120 + vl);
  SB0();
  if (wv > 4) {
    WAITV(4 * DH);
    if (lane == 0) __hip_atomic_fetch_add((LAS unsigned*)LP(L_RDY), 1u, __ATOMIC_RELAXED, __HIP_MEMORY_SCOPE_WORKGROUP);
  }
  lwait<1>(L_RDY, 0, 3);
  v4u wl = l128(pw);
  SB0();
#pragma unroll
  for (int it = 0; it < NIT; ++it) {
    if (it < NL) {
#pragma unroll
      for (int i = 0; i < 5; ++i) {
        const v4u wf = wl;
        if (5 * it + i + 1 < 5 * NL) wl = l128((5 * it + i + 1) / 5 == 2 ? pw2 + ((5 * it + i + 1) % 5) * 1024 : pw + ((5 * it + i + 1) / 5) * 40960 + ((5 * it + i + 1) % 5) * 1024);
#pragma unroll
        for (int j = 0; j < 4; ++j) acc[j][i] = mfma16(wf, hb[it % DH][j], acc[j][i]);
        SB0();
      }
    } else {
#pragma unroll
      for (int j = 0; j < 4; ++j)
#pragma unroll
        for (int i = 0; i < 5; ++i) acc[j][i] = mfma16(wr.wb[it % DW][i], hb[it % DH][j], acc[j][i]);
    }
    SB0();
    if (it + DH == 6) {
      if (__builtin_expect(!__all(sfl == tag), 0)) {
        _Pragma("unroll 1") for (int spin = 0; spin < SPIN; ++spin) { FENCE();
          if (__all(ld4c(rctl, fo) == tag)) break;
          __builtin_amdgcn_s_sleep(2);
        }
      }
      FENCE();
      STAMP(18); STAMPC(28);
    }
    if (it == 0) sfl = ld4c(rctl, fo);
    if (it + DH < NIT) loadh(it + DH);
    if (it >= NL && it + DW < NIT) wr.load(it + DW);
    if (it == NIT - DH) {
#pragma unroll
      for (int r = 0; r < 3; ++r) {
        const int sl = min(tid + 512 * r, 1279), row = sl / 20, qd = sl - 20 * row;
        const int tok = 128 * (id.rt >> 1) + 8 * (row >> 2) + 4 * (id.rt & 1) + (row & 3), d = 80 * id.ct + 4 * qd;
        oo[r] = tok * D_ + d;
        ra[r] = ((row & 3) * 5 + (qd >> 2)) * 1024 + (qd & 3) * 256 + (((row >> 2) + qd) & 15) * 16;
        xv[r] = *(const v2u*)(p.x + oo[r]); dv[r] = *(const v2u*)(p.dsk + d);
        sv[r] = ld16c(rctl, WS_STATS + tok * 16, 0);
      }
    }
#define BARIT 2
    if (it == BARIT) BAR();
    SB0();
  }
  STAMP(21); STAMPC(29);
#pragma unroll
  for (int i = 0; i < 5; ++i) {
    const lp rb = LP(wv * 20480 + (lane >> 4) * 256 + (((lane & 15) + 4 * i + (lane >> 4)) & 15) * 16);
#pragma unroll
    for (int j = 0; j < 4; ++j) sf4(rb + (j * 5 + i) * 1024, acc[j][i]);
  }
  BAR();
  asm volatile(".Lpf_end:");
  STAMP(19);
#pragma unroll
  for (int r = 0; r < 3; ++r) {
    if (r < 2 || wv < 4) {
      const lp q0 = LP(ra[r]), q1 = LP(ra[r] + 4 * 20480);
      f32x4 sum = ((lf4(q0) + lf4(q0 + 20480)) + (lf4(q0 + 40960) + lf4(q0 + 61440))) + ((lf4(q1) + lf4(q1 + 20480)) + (lf4(q1 + 40960) + lf4(q1 + 61440)));
      const f32x4 xf = {bflo(xv[r].x), bfhi(xv[r].x), bflo(xv[r].y), bfhi(xv[r].y)};
      const f32x4 df = {bflo(dv[r].x), bfhi(dv[r].x), bflo(dv[r].y), bfhi(dv[r].y)};
      sum += xf + (xf - __uint_as_float(sv[r].x)) * __uint_as_float(sv[r].z) * df;
#define OUTAUX 16
      __builtin_amdgcn_raw_buffer_store_b128(__builtin_bit_cast(v4u, sum), mkrs(p.out, L_ * D_ * 4), oo[r] * 4, 0, OUTAUX);
    }
  }
  STAMP(22);
}

extern "C" void kernel_launch(void* const* d_in, const int* in_sizes, int n_in, void* d_out, int out_size, void* d_ws, size_t ws_size, hipStream_t stream) {
  Par p;
  p.x = (const u16*)d_in[0]; p.lre = (const u16*)d_in[1]; p.lim = (const u16*)d_in[2];
  p.bre = (const u16*)d_in[3]; p.bim = (const u16*)d_in[4]; p.cre = (const u16*)d_in[5]; p.cim = (const u16*)d_in[6];
  p.ldt = (const u16*)d_in[7]; p.dsk = (const u16*)d_in[8];
  p.wg = (const u16*)d_in[10]; p.wu = (const u16*)d_in[11]; p.wd = (const u16*)d_in[12];
#define WSOFF 0
  p.ws = (unsigned char*)d_ws + (WSOFF == 1 ? ((ws_size - (21u << 20)) & ~(size_t)0xFFFFF) : WSOFF == 2 ? (ws_size / 2) : 0); p.out = (float*)d_out;
  (void)hipFuncSetAttribute((const void*)fused, hipFuncAttributeMaxDynamicSharedMemorySize, LDS1);
  fused<<<256, 512, LDS1, stream>>>(p);
}
```

```cpp
#include <hip/hip_runtime.h>
#include <stdint.h>

typedef unsigned short u16;
typedef unsigned v4u __attribute__((ext_vector_type(4)));
typedef unsigned v2u __attribute__((ext_vector_type(2)));
typedef float f32x4 __attribute__((ext_vector_type(4)));
typedef float f32x2 __attribute__((ext_vector_type(2)));
typedef float f32x16 __attribute__((ext_vector_type(16)));
typedef short bf16x8 __attribute__((ext_vector_type(8)));
typedef short s16x4 __attribute__((ext_vector_type(4)));
typedef __bf16 bf2 __attribute__((ext_vector_type(2)));
#define LAS __attribute__((address_space(3)))
#define DI __device__ __forceinline__
typedef LAS unsigned char* lp;

constexpr int L_ = 2048, D_ = 640, P_ = 640, F_ = 1728;
constexpr int NKB = 94;
constexpr unsigned WS_EPOCH = 0;
constexpr unsigned WS_FLW = 2048;
constexpr unsigned WS_FLH = 4096;
constexpr unsigned WS_AGG = WS_FLH + 32768;
constexpr unsigned WS_STATS = WS_AGG + 64 * 640 * 16;
constexpr unsigned WS_CTL_BYTES = WS_STATS + 32768;
constexpr unsigned WS_W2 = 1u << 20;
constexpr unsigned WS_W2_BYTES = 8 * 54 * 5120;
constexpr unsigned WS_H = 8u << 20;
constexpr unsigned WS_H_BYTES = 32 * NKB * 4096;

constexpr int UPO = 10368;
constexpr int STG = 32768 + 20480 + 128;
constexpr int LDS1 = 163840;
constexpr int L_STATS = 0;
constexpr int L_CS = L_STATS + 2048;
constexpr int L_CNT = L_CS + 1280, L_GO = L_CNT + 4, L_CSF = L_CNT + 16;
constexpr int LB = LDS1 - 3 * STG;
constexpr int L_CST = LB + 49152;
constexpr int L_W = 81920, L_W2 = 40960;
constexpr int L_E = LB + STG, ESTR = 528;
constexpr int SPIN = 1 << 18;
constexpr unsigned OOB = 0x40000000u;

DI float bf2f(unsigned b) { return __uint_as_float(b << 16); }
DI float bflo(unsigned w) { return __uint_as_float(w << 16); }
DI float bfhi(unsigned w) { return __uint_as_float(w & 0xffff0000u); }
DI unsigned pk2(float a, float b) { f32x2 v = {a, b}; return __builtin_bit_cast(unsigned, __builtin_convertvector(v, bf2)); }
DI float rbf(float a) { return bflo(pk2(a, a) & 0xffffu); }
DI f32x4 mfma16(v4u a, v4u b, f32x4 c) { return __builtin_amdgcn_mfma_f32_16x16x32_bf16(__builtin_bit_cast(bf16x8, a), __builtin_bit_cast(bf16x8, b), c, 0, 0, 0); }
DI f32x16 mfma32(v4u a, v4u b, f32x16 c) { return __builtin_amdgcn_mfma_f32_32x32x16_bf16(__builtin_bit_cast(bf16x8, a), __builtin_bit_cast(bf16x8, b), c, 0, 0, 0); }
DI unsigned rfl(unsigned v) { return __builtin_amdgcn_readfirstlane(v); }
DI __amdgpu_buffer_rsrc_t mkrs(const void* p, unsigned bytes) { return __builtin_amdgcn_make_buffer_rsrc((void*)p, 0, bytes, 0x00020000); }
template <int LI, int SI> DI void glds(__amdgpu_buffer_rsrc_t rs, unsigned lb, unsigned voff, unsigned sb) {
  unsigned t;
  asm volatile("s_add_u32 m0, %1, %5\n\ts_add_u32 %0, %4, %6\n\tbuffer_load_dwordx4 %2, %3, %0 offen lds" : "=&s"(t) : "s"(lb), "v"(voff), "s"(rs), "s"(sb), "i"(LI), "i"(SI) : "scc");
}
template <int LI, int SI> DI void gldsc(__amdgpu_buffer_rsrc_t rs, unsigned lb, unsigned voff, unsigned sb) {
  unsigned t;
  asm volatile("s_add_u32 m0, %1, %5\n\ts_add_u32 %0, %4, %6\n\tbuffer_load_dwordx4 %2, %3, %0 offen sc1 lds" : "=&s"(t) : "s"(lb), "v"(voff), "s"(rs), "s"(sb), "i"(LI), "i"(SI) : "scc");
}
template <int... I> struct Seq {};
template <int N, int... I> struct MkSeq : MkSeq<N - 1, N - 1, I...> {};
template <int... I> struct MkSeq<0, I...> { typedef Seq<I...> T; };
template <int... I> DI void dma_lin(Seq<I...>, __amdgpu_buffer_rsrc_t rs, unsigned lb, unsigned vl, unsigned sb) { (gldsc<I * 1024, I * 1024>(rs, lb, vl, sb), ...); }
template <int... I> DI void dma_x(Seq<I...>, __amdgpu_buffer_rsrc_t rs, unsigned lb, unsigned v0, unsigned v1, unsigned sb) { (glds<I * 1024, (64 * (I & 1) + (I >> 1)) * 1280>(rs, lb, (I & 1) ? v1 : v0, sb), ...); }
template <int... I> DI void dma_b(Seq<I...>, __amdgpu_buffer_rsrc_t rs, unsigned lb, const unsigned* v, unsigned sb) { (glds<I * 1024, (16 * (I & 1) + 4 * (I >> 1)) * 1280>(rs, lb, v[I & 1], sb), ...); }
template <int... I> DI void dma_g(Seq<I...>, __amdgpu_buffer_rsrc_t rs, unsigned lb, const unsigned* v, unsigned sb) { (glds<I * 1024, 0>(rs, lb, v[I], sb), ...); }
DI lp LP(unsigned a) { return (lp)(uintptr_t)a; }
DI v4u l128(lp a) { return *(LAS v4u*)a; }
DI f32x4 lf4(lp a) { return *(LAS f32x4*)a; }
DI void sf4(lp a, f32x4 v) { *(LAS f32x4*)a = v; }
DI s16x4 ltr(lp a) { return __builtin_amdgcn_ds_read_tr16_b64_v4i16((LAS s16x4*)a); }
DI v4u cat(s16x4 lo, s16x4 hi) { return __builtin_bit_cast(v4u, __builtin_shufflevector(lo, hi, 0, 1, 2, 3, 4, 5, 6, 7)); }
DI v4u ld16(__amdgpu_buffer_rsrc_t r, unsigned vo, unsigned so) { return __builtin_amdgcn_raw_buffer_load_b128(r, vo, so, 0); }
DI v4u ld16c(__amdgpu_buffer_rsrc_t r, unsigned vo, unsigned so) { return __builtin_amdgcn_raw_buffer_load_b128(r, vo, so, 16); }
DI unsigned ld4c(__amdgpu_buffer_rsrc_t r, unsigned vo) { return __builtin_amdgcn_raw_buffer_load_b32(r, vo, 0, 16); }
DI void st16c(v4u v, __amdgpu_buffer_rsrc_t r, unsigned vo) { __builtin_amdgcn_raw_buffer_store_b128(v, r, vo, 0, 16); }
DI void st4c(unsigned v, __amdgpu_buffer_rsrc_t r, unsigned vo) { __builtin_amdgcn_raw_buffer_store_b32(v, r, vo, 0, 16); }
#define BAR() asm volatile("s_waitcnt lgkmcnt(0)\n\ts_barrier" ::: "memory")
#define SB0() __builtin_amdgcn_sched_barrier(0)
#define WAITV(n) asm volatile("s_waitcnt vmcnt(%0)" ::"i"(n) : "memory")
#define FENCE() asm volatile("" ::: "memory")
template <int C> DI float dppf(float v) { return __int_as_float(__builtin_amdgcn_update_dpp(0, __float_as_int(v), C, 0xf, 0xf, true)); }
template <int N> DI f32x4 shr4(f32x4 v) { return f32x4{dppf<0x110 + N>(v.x), dppf<0x110 + N>(v.y), dppf<0x110 + N>(v.z), dppf<0x110 + N>(v.w)}; }
DI float sum8(float v) {
  v += dppf<0xB1>(v); v += dppf<0x4E>(v); v += dppf<0x141>(v);
  return v;
}
DI float dot2(unsigned a, unsigned b, float c) { return __builtin_amdgcn_fdot2_f32_bf16(__builtin_bit_cast(bf2, a), __builtin_bit_cast(bf2, b), c, false); }
DI void gather4(float x, float (&t)[4]) {
  const auto a = __builtin_amdgcn_permlane16_swap(__float_as_uint(x), __float_as_uint(x), false, false);
  const auto e = __builtin_amdgcn_permlane32_swap(a[0], a[0], false, false), o = __builtin_amdgcn_permlane32_swap(a[1], a[1], false, false);
  t[0] = __uint_as_float(e[0]); t[2] = __uint_as_float(e[1]); t[1] = __uint_as_float(o[0]); t[3] = __uint_as_float(o[1]);
}
DI void cm1(float& a, float& b, float lr, float li, float ur, float ui) {
  const float na = fmaf(lr, a, fmaf(-li, b, ur)), nb = fmaf(lr, b, fmaf(li, a, ui));
  a = na; b = nb;
}
DI f32x4 fma4(f32x4 a, f32x4 b, f32x4 c) { return __builtin_elementwise_fma(a, b, c); }
DI f32x4 sp4(float a) { return f32x4{a, a, a, a}; }
DI void cmac(f32x4& a, f32x4& b, f32x4 lr, f32x4 li, f32x4 ur, f32x4 ui) {
  const f32x4 na = fma4(lr, a, fma4(-li, b, ur)), nb = fma4(lr, b, fma4(li, a, ui));
  a = na; b = nb;
}

#define STAMP(i)
#define STAMPC(i)

#define LSTAMP(i)
struct Par {
  const u16 *x, *lre, *lim, *bre, *bim, *cre, *cim, *ldt, *dsk, *wg, *wu, *wd;
  unsigned char* ws;
  float* out;
};
struct Ids { int R, g, cg, rt, ct; };

#define PDW 5
#define PDH 2
constexpr int DW = PDW, DH = PDH, NIT = 12, NG = 7;
#define DMAX 2
constexpr int hnext(int it) { const int d = it < 2 ? DH : (DH + it - 1 < DMAX ? DH + it - 1 : DMAX), n = it + 1 + d; return n < NIT ? n : NIT; }
constexpr int LASTW = 4 * (hnext(9) - hnext(5)) + 9;
#define PIN4(x) asm volatile("" : "+v"(x))
DI void sig(unsigned addr, unsigned v) {
  asm volatile("s_mov_b64 exec, 1\n\tds_write_b32 %0, %1\n\ts_mov_b64 exec, -1" ::"v"(addr), "v"(v) : "memory");
}
DI unsigned lrd(unsigned addr) { return *(volatile LAS unsigned*)LP(addr); }
#define LSLEEP 1
template <int SL = 1> DI void lwait(unsigned addr, unsigned v, unsigned need) {
  if (__builtin_expect(!__all(v >= need), 0)) {
    _Pragma("unroll 1") for (int spin = 0; spin < 64 * SPIN; ++spin) { FENCE();
      if (__all(lrd(addr) >= need)) break;
      __builtin_amdgcn_s_sleep(SL);
    }
  }
  FENCE();
}

template <int... I> DI void dma_c1(Seq<I...>, __amdgpu_buffer_rsrc_t rs, unsigned lb, unsigned v, unsigned sb) { (glds<I * 1024, I * 16 * 1280>(rs, lb, v, sb), ...); }
template <int... I> DI void dma_c2(Seq<I...>, __amdgpu_buffer_rsrc_t rs, unsigned lb, unsigned v, unsigned sb) { (glds<(I & 1) * 5120 + (I >> 1) * 1024, (I & 1) * 64 + (I >> 1) * 16 * 1280>(rs, lb, v, sb), ...); }
DI unsigned cswz(unsigned g) { return ((g << 1) & 2) ^ ((g >> 1) * 3); }
struct WRing {
  v4u wb[DW][5];
  __amdgpu_buffer_rsrc_t rw, rcr, rci;
  unsigned vl, vc, wv;
  DI void load(int it) {
#pragma unroll
    for (int i = 0; i < 5; ++i) { wb[it % DW][i] = ld16c(rw, vl + (i & 3) * 1024, (8 * it + wv) * 5120 + (i >> 2) * 4096); SB0(); }
  }
  DI void dma01() {
    asm volatile("s_nop 4");
    dma_c2(MkSeq<10>::T{}, rcr, L_W + (wv - 4) * 20480, vc, (wv - 4) * 256);
    dma_c2(MkSeq<10>::T{}, rcr, L_W + (wv - 4) * 20480 + 10240, vc, (wv - 4) * 256 + 128);
  }
  DI void dma2() {
    asm volatile("s_nop 4");
    dma_c2(MkSeq<10>::T{}, wv < 6 ? rcr : rci, L_W2 + (wv - 4) * 10240, vc, wv < 6 ? 1024 + (wv - 4) * 128 : (wv - 6) * 128);
  }
  DI void refill(int r) {
    dma_c1(MkSeq<5>::T{}, rci, r * L_W + wv * 5120, vc, (4 + 8 * r + wv) * 64);
  }
};

DI float wsum(float v) {
  v = sum8(v); v += dppf<0x128>(v);
  float t[4]; gather4(v, t);
  return (t[0] + t[1]) + (t[2] + t[3]);
}
DI unsigned mktag(unsigned epoch) { return rfl(epoch) * 0x9E3779B1u + 0x7F4A7C15u; }
struct RowIn { v4u xa[2]; unsigned xb[2], epoch; };
DI void row_load(const Par& p, const Ids& id, const int w, const int lane, RowIn& in) {
  const int row = 256 * id.R + 8 * id.cg + 2 * w;
#pragma unroll
  for (int z = 0; z < 2; ++z) { in.xa[z] = *(const v4u*)(p.x + (row + z) * D_ + lane * 8); in.xb[z] = *(const unsigned*)(p.x + (row + z) * D_ + 512 + lane * 2); }
  in.epoch = ld4c(mkrs(p.ws, WS_CTL_BYTES), WS_EPOCH);
  SB0();
}
DI unsigned row_stats(const Par& p, const Ids& id, const int w, const int lane, const RowIn& in) {
  const __amdgpu_buffer_rsrc_t rctl = mkrs(p.ws, WS_CTL_BYTES);
  const int row = 256 * id.R + 8 * id.cg + 2 * w;
  const v4u* xa = in.xa; const unsigned* xb = in.xb; const unsigned epoch = in.epoch;
  float v[2][10], mean[2], s2[2];
#pragma unroll
  for (int z = 0; z < 2; ++z) {
    const float t[10] = {bflo(xa[z].x), bfhi(xa[z].x), bflo(xa[z].y), bfhi(xa[z].y), bflo(xa[z].z), bfhi(xa[z].z), bflo(xa[z].w), bfhi(xa[z].w), bflo(xb[z]), bfhi(xb[z])};
#pragma unroll
    for (int e = 0; e < 10; ++e) v[z][e] = t[e];
    mean[z] = wsum(((t[0] + t[1]) + (t[2] + t[3])) + ((t[4] + t[5]) + (t[6] + t[7])) + (t[8] + t[9])) * (1.f / D_);
  }
#pragma unroll
  for (int z = 0; z < 2; ++z) {
    float a = 0.f, b = 0.f;
#pragma unroll
    for (int e = 0; e < 5; ++e) { a = fmaf(v[z][e] - mean[z], v[z][e] - mean[z], a); b = fmaf(v[z][5 + e] - mean[z], v[z][5 + e] - mean[z], b); }
    s2[z] = wsum(a + b);
  }
  const unsigned tag = mktag(epoch);
  const float m = rbf((lane & 1) ? mean[1] : mean[0]), rs = 1.f / rbf(sqrtf(rbf(rbf(((lane & 1) ? s2[1] : s2[0]) * (1.f / D_)) + 1e-5f)));
  st16c(v4u{__float_as_uint(m), tag, __float_as_uint(rs), tag}, rctl, lane < 2 ? WS_STATS + (row + lane) * 16 : OOB);
  return tag;
}

template <bool SSM>
DI unsigned compute_wave(const Par& p, const Ids& id, const int w, const int lane, const RowIn& rin) {
  STAMP(0);
  unsigned tag, cf = 0;
  if constexpr (!SSM) {
    const int r = lane & 31, hh = lane >> 5;
    const unsigned xa = (64 * w + r) * 128 + ((hh ^ ((r >> 1) & 7)) * 16);
    const unsigned wa = 32768 + ((lane >> 4) & 1) * UPO + (8 * hh + ((lane & 15) >> 2)) * 160 + (lane & 3) * 8;
    f32x16 acc[5][2] = {};
#pragma unroll
    for (int e = 0; e < 5; ++e) { PIN4(acc[e][0]); PIN4(acc[e][1]); }
    v4u fx[2][2]; s16x4 fl[4], fh[4];
    lp px[4], pw;
    auto setp = [&](unsigned sb) {
#pragma unroll
      for (int s = 0; s < 4; ++s) px[s] = LP(LB + sb + (xa ^ (32 * s)));
      pw = LP(LB + sb + wa);
    };
    auto woff = [&](int n) { return (n / 5) * 2560 + (n % 5) * 32; };
    setp(0);
    SB0();
    tag = row_stats(p, id, w, lane, rin);
    STAMP(17);
    SB0();
    BAR();
    STAMP(1);
    fx[0][0] = l128(px[0]); fx[0][1] = l128(px[0] + 4096);
#pragma unroll
    for (int n = 0; n < 3; ++n) { fl[n] = ltr(pw + woff(n)); fh[n] = ltr(pw + woff(n) + 640); }
    unsigned sb = 0;
#pragma unroll 1
    for (int i = 0; i < 10; ++i) {
#pragma unroll
      for (int n = 0; n < 20; ++n) {
        const int ks = n / 5, e = n % 5, nn = (n + 3) % 20;
        if (n == 17) { if (i < 9) BAR(); LSTAMP(i); sb = sb == 2 * STG ? 0 : sb + STG; setp(sb); SB0(); }
        const v4u wf = cat(fl[n & 3], fh[n & 3]);
        acc[e][0] = mfma32(wf, fx[ks & 1][0], acc[e][0]); SB0();
        fl[(n + 3) & 3] = ltr(pw + woff(nn));
        fh[(n + 3) & 3] = ltr(pw + woff(nn) + 640); SB0();
        acc[e][1] = mfma32(wf, fx[ks & 1][1], acc[e][1]); SB0();
        if (ks < 3 && e == 1) { fx[(ks + 1) & 1][0] = l128(px[ks + 1]); fx[(ks + 1) & 1][1] = l128(px[ks + 1] + 4096); }
        if (n == 17) { fx[0][0] = l128(px[0]); fx[0][1] = l128(px[0] + 4096); }
        if (n == 18) cf = lrd(L_CSF + (lane & 3) * 4);
        SB0();
      }
    }
    lwait(L_CSF + (lane & 3) * 4, cf, 1);
    STAMP(12);
    const __amdgpu_buffer_rsrc_t rh = mkrs(p.ws + WS_H + (unsigned)(4 * id.R + w) * (NKB * 4096), 54 * 4096);
    const unsigned so0 = (r >> 4) * 1024 + (hh * 16 + (r & 15)) * 16;
    float mu[2], r2[2];
#pragma unroll
    for (int u = 0; u < 2; ++u) {
      const int F = 4 * w + 2 * u + (r >> 4);
      const f32x2 s = *(LAS f32x2*)LP(L_STATS + (128 * (F >> 3) + 8 * (r & 15) + (F & 7)) * 8);
      mu[u] = s.x; r2[u] = s.y * s.y;
    }
    const lp pc = LP(L_CS + hh * 16);
#pragma unroll
    for (int e = 0; e < 5; ++e) {
      const f32x4 g0 = lf4(pc + e * 64) + lf4(pc + e * 64 + 640), g1 = lf4(pc + e * 64 + 32) + lf4(pc + e * 64 + 672);
      const f32x4 u0 = lf4(pc + e * 64 + 320) + lf4(pc + e * 64 + 960), u1 = lf4(pc + e * 64 + 352) + lf4(pc + e * 64 + 992);
      const int T = 5 * id.g + e;
      const unsigned so = so0 + (T >> 1) * 4096 + (T & 1) * 512;
#pragma unroll
      for (int u = 0; u < 2; ++u) {
        const f32x16 a = acc[e][u];
        const f32x4 nm = sp4(-mu[u]);
        const f32x4 h0 = fma4(nm, g0, f32x4{a[0], a[1], a[2], a[3]}) * fma4(nm, u0, f32x4{a[8], a[9], a[10], a[11]}) * r2[u];
        const f32x4 h1 = fma4(nm, g1, f32x4{a[4], a[5], a[6], a[7]}) * fma4(nm, u1, f32x4{a[12], a[13], a[14], a[15]}) * r2[u];
        st16c(v4u{pk2(h0.x, h0.y), pk2(h0.z, h0.w), pk2(h1.x, h1.y), pk2(h1.z, h1.w)}, rh, so + u * 2048);
      }
    }
  } else {
    const int c = lane & 15, q = lane >> 4, G = w >> 1, blk = w & 1;
    const unsigned xa = (128 * G + c) * 128 + ((q ^ (c >> 1)) * 16);
    const unsigned wa = 32768 + (32 * blk + c) * 128 + ((q ^ (c >> 1)) * 16);
    f32x4 acc[8][4] = {};
#pragma unroll
    for (int j = 0; j < 8; ++j)
#pragma unroll
      for (int t = 0; t < 4; ++t) PIN4(acc[j][t]);
    v4u fw[2][4], fx[4];
    lp px[2], pw[2];
    auto setp = [&](unsigned sb) { px[0] = LP(LB + sb + xa); px[1] = LP(LB + sb + (xa ^ 64)); pw[0] = LP(LB + sb + wa); pw[1] = LP(LB + sb + (wa ^ 64)); };
    auto woff = [&](int t) { return (t >> 1) * 8192 + (t & 1) * 2048; };
    setp(0);
    SB0();
    tag = row_stats(p, id, w, lane, rin);
    STAMP(17);
    SB0();
    BAR();
    STAMP(1);
#pragma unroll
    for (int t = 0; t < 4; ++t) fw[0][t] = l128(pw[0] + woff(t));
#pragma unroll
    for (int n = 0; n < 3; ++n) fx[n] = l128(px[0] + n * 2048);
    unsigned sb = 0;
#pragma unroll 1
    for (int i = 0; i < 10; ++i) {
#pragma unroll
      for (int n = 0; n < 16; ++n) {
        const int kk = n >> 3, j = n & 7, nn = (n + 3) & 15;
        if (n == 13) { if (i < 9) BAR(); LSTAMP(i); sb = sb == 2 * STG ? 0 : sb + STG; setp(sb); SB0(); }
        const v4u xf = fx[n & 3];
        acc[j][0] = mfma16(fw[kk][0], xf, acc[j][0]); SB0();
        fx[(n + 3) & 3] = l128(px[nn >> 3] + (nn & 7) * 2048); SB0();
        acc[j][1] = mfma16(fw[kk][1], xf, acc[j][1]); SB0();
        if (n >= 2 && n < 6) fw[1][n - 2] = l128(pw[1] + woff(n - 2));
        if (n == 13) fw[0][0] = l128(pw[0] + woff(0));
        if (n == 14) fw[0][2] = l128(pw[0] + woff(2));
        SB0();
        acc[j][2] = mfma16(fw[kk][2], xf, acc[j][2]); SB0();
        if (n == 13) fw[0][1] = l128(pw[0] + woff(1));
        if (n == 14) fw[0][3] = l128(pw[0] + woff(3));
        SB0();
        acc[j][3] = mfma16(fw[kk][3], xf, acc[j][3]); SB0();
        if (n == 14) { cf = lrd(L_CSF + (lane & 3) * 4); SB0(); }
      }
    }
    lwait(L_CSF + (lane & 3) * 4, cf, 1);
    STAMP(12);
    const lp pe = LP(L_E + (16 * G + c) * ESTR + (32 * blk + 8 * q) * 4);
    const lp pk = LP(L_CST + (32 * blk + 8 * q) * 4);
    f32x4 lr[2], li[2], er[2], ei[2];
    {
      const lp ps = LP(L_STATS + (128 * G + 8 * c) * 8), pc = LP(L_CS + (32 * blk + 8 * q) * 4);
      f32x4 cs[4], st[4];
#pragma unroll
      for (int t = 0; t < 4; ++t) { cs[t] = lf4(pc + (t >> 1) * 320 + (t & 1) * 16) + lf4(pc + 640 + (t >> 1) * 320 + (t & 1) * 16); st[t] = lf4(ps + t * 16); }
#pragma unroll
      for (int ab = 0; ab < 2; ++ab) { lr[ab] = lf4(pk + ab * 16); li[ab] = lf4(pk + 256 + ab * 16); }
#pragma unroll
      for (int jj = 0; jj < 4; ++jj)
#pragma unroll
        for (int t = 0; t < 4; ++t) {
          acc[2 * jj][t] = fma4(sp4(-st[jj].x), cs[t], acc[2 * jj][t]) * st[jj].y;
          acc[2 * jj + 1][t] = fma4(sp4(-st[jj].z), cs[t], acc[2 * jj + 1][t]) * st[jj].w;
        }
    }
#pragma unroll
    for (int ab = 0; ab < 2; ++ab) { er[ab] = acc[0][ab]; ei[ab] = acc[0][2 + ab]; }
#pragma unroll
    for (int j = 1; j < 8; ++j)
#pragma unroll
      for (int ab = 0; ab < 2; ++ab) cmac(er[ab], ei[ab], lr[ab], li[ab], acc[j][ab], acc[j][2 + ab]);
#pragma unroll
    for (int ab = 0; ab < 2; ++ab) { sf4(pe + ab * 16, er[ab]); sf4(pe + 256 + ab * 16, ei[ab]); }
    SB0();
    BAR();
    STAMP(13);
#pragma unroll
    for (int ab = 0; ab < 2; ++ab) {
      const f32x4 kr = lf4(pk + 512 * 4 + ab * 16), ki = lf4(pk + 512 * 4 + 256 + ab * 16);
#pragma unroll
      for (int j = 0; j < 8; ++j) {
        const f32x4 ur = acc[j][ab], ui = acc[j][2 + ab];
        acc[j][ab] = fma4(-ki, ui, kr * ur); acc[j][2 + ab] = fma4(-ki, ur, -kr * ui);
        PIN4(acc[j][ab]); PIN4(acc[j][2 + ab]);
      }
      li[ab] = -li[ab];
    }
    const __amdgpu_buffer_rsrc_t rh = mkrs(p.ws + WS_H + (unsigned)(4 * id.R + 2 * G) * (NKB * 4096), 2 * NKB * 4096);
    unsigned vo[4];
#pragma unroll
    for (int z = 0; z < 4; ++z) { vo[z] = (54 + 2 * id.g + blk + 20 * (z & 1) + NKB * (z >> 1)) * 4096 + lane * 16; PIN4(vo[z]); }
    SB0();
    BAR();
    STAMP(14);
#pragma unroll
    for (int ab = 0; ab < 2; ++ab) { er[ab] = lf4(pe + ab * 16); ei[ab] = lf4(pe + 256 + ab * 16); }
#pragma unroll
    for (int j = 0; j < 8; ++j) {
#pragma unroll
      for (int ab = 0; ab < 2; ++ab) cmac(er[ab], ei[ab], lr[ab], li[ab], acc[j][ab], acc[j][2 + ab]);
      st16c(v4u{pk2(er[0].x, er[0].y), pk2(er[0].z, er[0].w), pk2(er[1].x, er[1].y), pk2(er[1].z, er[1].w)}, rh, vo[2 * (j >> 2)] + (j & 3) * 1024);
      st16c(v4u{pk2(ei[0].x, ei[0].y), pk2(ei[0].z, ei[0].w), pk2(ei[1].x, ei[1].y), pk2(ei[1].z, ei[1].w)}, rh, vo[2 * (j >> 2) + 1] + (j & 3) * 1024);
    }
  }
  SB0();
  STAMP(15);
  return tag;
}

#define HBAR() do { SB0(); BAR(); SB0(); } while (0)
struct HState { f32x4 acs[5]; unsigned pl, pr, pi; };
template <bool SSM>
DI unsigned helper_wave(const Par& p, const Ids& id, const int h, const int lane, HState& hs) {
  STAMP(0);
  constexpr int NWP = SSM ? 4 : 5, NP = 8 + NWP;
  constexpr unsigned WSTEP = SSM ? 128u : 64u * 3456u;
  const int c = lane & 15, q = lane >> 4;
  const __amdgpu_buffer_rsrc_t rctl = mkrs(p.ws, WS_CTL_BYTES);
  const __amdgpu_buffer_rsrc_t rx = mkrs(p.x, L_ * D_ * 2);
  const __amdgpu_buffer_rsrc_t rw = mkrs(SSM ? ((h >> 1) ? p.bim : p.bre) : ((h >> 1) ? p.wu : p.wg), SSM ? P_ * D_ * 2 : D_ * F_ * 2);
  const unsigned v0 = (8 * (lane >> 3)) * 1280 + (((lane & 7) ^ (lane >> 4)) * 16), v1 = v0 ^ 64;
  unsigned st = 0;
  const unsigned sx = (256 * id.R + 128 * (h >> 1) + 4 * (h & 1)) * 1280;
  auto issue_x = [&](unsigned s, unsigned sb) {
    dma_x(MkSeq<8>::T{}, rx, LB + sb + h * 8192, v0, v1, s);
  };
  unsigned vw[NWP];
  if constexpr (SSM) {
    vw[0] = (8 * (lane >> 5) + ((lane >> 3) & 3)) * 1280 + (((lane & 7) ^ (lane >> 4)) * 16); vw[1] = vw[0] ^ 64;
  } else {
#pragma unroll
    for (int i = 0; i < 5; ++i) { const int lc = 64 * (5 * (h & 1) + i) + lane, pos = lc / 10; vw[i] = pos * 3456 + (lc - pos * 10) * 16; }
  }
  const unsigned sw = SSM ? (64 * id.g + 32 * (h & 1)) * 1280 : 160 * id.g;
  const unsigned dw = LB + 32768 + (h >> 1) * (SSM ? 8192 : UPO) + (h & 1) * NWP * 1024;
  auto issue_w = [&](unsigned s, unsigned sb) {
    if constexpr (SSM) dma_b(MkSeq<4>::T{}, rw, sb + dw, vw, s); else dma_g(MkSeq<5>::T{}, rw, sb + dw, vw, s);
  };
  asm volatile("s_nop 4");
  issue_w(sw, 0);
  unsigned sb = STG;
  auto step = [&]() {
    ++st;
    issue_x(sx + st * 128, sb); issue_w(sw + st * WSTEP, sb);
    sb = sb == 2 * STG ? 0 : sb + STG;
  };
  step();
  if (h == 3 && lane < 8) *(LAS unsigned*)LP(L_CNT + lane * 4) = 0u;
  f32x4 acs[NWP] = {};
  v4u fc[NWP];
  const unsigned ca = SSM ? 32768 + (h >> 1) * 8192 + c * 128 + (((4 * (h & 1) + q) ^ (c >> 1)) * 16)
                          : 32768 + (h >> 1) * UPO + (32 * (h & 1) + 4 * q + (c >> 2)) * 160 + (c & 3) * 8;
  const v4u ones = {0x3F803F80u, 0x3F803F80u, 0x3F803F80u, 0x3F803F80u};
  auto cs_rd = [&](unsigned s) {
    const lp a = LP(LB + s + ca);
#pragma unroll
    for (int t = 0; t < NWP; ++t) { if constexpr (SSM) fc[t] = l128(a + t * 2048); else fc[t] = cat(ltr(a + t * 32), ltr(a + t * 32 + 2560)); }
    SB0();
  };
  auto cs_mm = [&]() {
    SB0();
    __builtin_amdgcn_s_setprio(2);
#pragma unroll
    for (int t = 0; t < NWP; ++t) acs[t] = mfma16(fc[t], ones, acs[t]);
    __builtin_amdgcn_s_setprio(0);
    SB0();
  };
  constexpr int JL = 1;
  const int tsk = (id.g * 8 + id.R) * 4 + h, kb = tsk / 10, dq = tsk - 10 * kb;
  v4u f[4] = {}, sel[2], sv = {}, fl = {};
  unsigned fvo, fso[4], tch = 0;
  const __amdgpu_buffer_rsrc_t rf = mkrs(SSM ? ((tsk & 1) ? p.cim : p.cre) : p.wd, SSM ? D_ * P_ * 2 : F_ * D_ * 2);
  const __amdgpu_buffer_rsrc_t r2 = mkrs(p.ws + WS_W2, tsk < 540 ? WS_W2_BYTES : 0);
  if constexpr (SSM) fvo = lane < 40 ? ((tsk >> 1) * 40 + lane) * 128 : OOB;
  else {
    fvo = (32 * kb + 16 * ((lane >> 3) & 1) + 4 * (q & 1) + (q >> 1)) * 1280 + dq * 128 + (lane & 7) * 16;
#pragma unroll
    for (int i = 0; i < 4; ++i) {
      const int dt = 4 * dq + 2 * (q & 1) + (i >> 1), ct = dt / 5;
      fso[i] = ((ct * 54 + kb) * 5 + dt - 5 * ct) * 1024 + (16 * (2 * (q >> 1) + (c >> 3)) + 8 * (i & 1) + (c & 7)) * 16;
      PIN4(fso[i]);
    }
#pragma unroll
    for (int hf = 0; hf < 2; ++hf) {
#pragma unroll
      for (int e = 0; e < 4; ++e) sel[hf][e] = (8 * q + 2 * e == 16 * hf + c ? 0x3F80u : 0u) | (8 * q + 2 * e + 1 == 16 * hf + c ? 0x3F800000u : 0u);
      PIN4(sel[hf]);
    }
  }
  PIN4(fvo);
  unsigned epoch, pl = 0, pr = 0, pi = 0, tag = 0;
  unsigned so = WS_STATS + (256 * id.R + 64 * h + lane) * 16, flo = h == 3 ? WS_FLW + lane * 16 : OOB, fwo = (h == 3 && lane == 0) ? WS_FLW + blockIdx.x * 4 : OOB;
  PIN4(so); PIN4(flo); PIN4(fwo);
  asm volatile("buffer_load_dword %0, off, %1, 0 sc1" : "=v"(epoch) : "s"(rctl));
  if constexpr (SSM) {
    const unsigned po = (64 * id.g + lane) * 2;
    asm volatile("buffer_load_ushort %0, %3, %4, 0 offen\n\tbuffer_load_ushort %1, %3, %5, 0 offen\n\tbuffer_load_ushort %2, %3, %6, 0 offen"
                 : "=&v"(pl), "=&v"(pr), "=&v"(pi) : "v"(po), "s"(mkrs(p.ldt, P_ * 2)), "s"(mkrs(p.lre, P_ * 2)), "s"(mkrs(p.lim, P_ * 2)));
  }
  WAITV(NP + (SSM ? 4 : 1)); BAR();
#pragma unroll
  for (int j = 0; j < 8; ++j) {
    cs_rd((j % 3) * STG);
    if (j == JL + 4) { tag = mktag(epoch); st4c(tag, rctl, fwo); }
    if (j == 6) asm volatile("buffer_load_dwordx4 %0, %1, %2, 0 offen sc1" : "=v"(sv) : "v"(so), "s"(rctl));
    if (!SSM && j == 7) asm volatile("buffer_load_dwordx4 %0, %1, %2, 0 offen sc1" : "=v"(fl) : "v"(flo), "s"(rctl));
    SB0();
    step();
    if (SSM && j == 3) asm volatile("buffer_load_dword %0, %1, %2, 0 offen" : "=v"(tch) : "v"(fvo), "s"(rf));
    if (!SSM && j == JL)
      asm volatile("buffer_load_dwordx4 %0, %4, %5, 0 offen\n\tbuffer_load_dwordx4 %1, %4, %5, 0 offen offset:2560\n\tbuffer_load_dwordx4 %2, %4, %5, %6 offen\n\tbuffer_load_dwordx4 %3, %4, %5, %6 offen offset:2560"
                   : "=&v"(f[0]), "=&v"(f[1]), "=&v"(f[2]), "=&v"(f[3]) : "v"(fvo), "s"(rf), "s"(8 * 1280));
    cs_mm();
    if (!SSM && j == JL + 2) {
      f32x4 d[8];
      __builtin_amdgcn_s_setprio(2);
#pragma unroll
      for (int z = 0; z < 8; ++z) d[z] = mfma16(f[z >> 1], sel[z & 1], f32x4{0.f, 0.f, 0.f, 0.f});
#pragma unroll
      for (int i = 0; i < 4; ++i) st16c(v4u{pk2(d[0][i], d[1][i]), pk2(d[2][i], d[3][i]), pk2(d[4][i], d[5][i]), pk2(d[6][i], d[7][i])}, r2, fso[i]);
      __builtin_amdgcn_s_setprio(0);
      SB0();
    }
    if (!SSM && (j == JL || j == JL + 2)) WAITV(NP + 4);
    else if (SSM && j == 3) WAITV(NP + 1);
    else if (j == JL + 1) asm volatile("s_waitcnt vmcnt(%8)" : "+v"(f[0]), "+v"(f[1]), "+v"(f[2]), "+v"(f[3]), "+v"(epoch), "+v"(pl), "+v"(pr), "+v"(pi) : "i"(NP) : "memory");
    else if (j == 4) asm volatile("s_waitcnt vmcnt(%1)" : "+v"(tch) : "i"(NP) : "memory");
    else if (j == 6) asm volatile("s_waitcnt vmcnt(%1)" : "+v"(sv) : "i"(NP) : "memory");
    else if (j == 7) asm volatile("s_waitcnt vmcnt(%1)" : "+v"(fl) : "i"(NP) : "memory");
    else if (j == JL + 4) WAITV(NP + 1);
    else WAITV(NP);
    if (j == 6) {
      v2u o = {sv.x, sv.z};
      if (!__all((sv.y == tag) & (sv.w == tag))) {
        _Pragma("unroll 1") for (int spin = 0; spin < SPIN; ++spin) { FENCE();
          const v4u s2 = ld16c(rctl, so, 0);
          o = v2u{s2.x, s2.z};
          if (__all((s2.y == tag) & (s2.w == tag))) break;
          __builtin_amdgcn_s_sleep(4);
        }
      }
      *(LAS v2u*)LP(L_STATS + (64 * h + lane) * 8) = o;
    }
    if (!SSM && j == 7 && h == 3) {
      if (!__all((fl.x == tag) & (fl.y == tag) & (fl.z == tag) & (fl.w == tag))) {
        _Pragma("unroll 1") for (int spin = 0; spin < SPIN; ++spin) { FENCE();
          const v4u f2 = ld16c(rctl, flo, 0);
          if (__all((f2.x == tag) & (f2.y == tag) & (f2.z == tag) & (f2.w == tag))) break;
          __builtin_amdgcn_s_sleep(4);
        }
      }
      st4c(tag, rctl, (lane == 0 && blockIdx.x == 255) ? WS_EPOCH : OOB);
    }
    BAR();
  }
#pragma unroll
  for (int t = 0; t < 5; ++t) hs.acs[t] = acs[t < NWP ? t : 0];
  hs.pl = pl; hs.pr = pr; hs.pi = pi;
  return tag;
}
template <bool SSM>
DI void helper_tail(const Par& p, const Ids& id, const int h, const int lane, const unsigned tag, HState& hs) {
  constexpr int NWP = SSM ? 4 : 5;
  const int c = lane & 15, q = lane >> 4;
  const __amdgpu_buffer_rsrc_t rctl = mkrs(p.ws, WS_CTL_BYTES);
  f32x4* acs = hs.acs;
  const unsigned pl = hs.pl, pr = hs.pr, pi = hs.pi;
  v4u fc[NWP];
  const unsigned ca = SSM ? 32768 + (h >> 1) * 8192 + c * 128 + (((4 * (h & 1) + q) ^ (c >> 1)) * 16)
                          : 32768 + (h >> 1) * UPO + (32 * (h & 1) + 4 * q + (c >> 2)) * 160 + (c & 3) * 8;
  const v4u ones = {0x3F803F80u, 0x3F803F80u, 0x3F803F80u, 0x3F803F80u};
  auto cs_rd = [&](unsigned s) {
    const lp a = LP(LB + s + ca);
#pragma unroll
    for (int t = 0; t < NWP; ++t) { if constexpr (SSM) fc[t] = l128(a + t * 2048); else fc[t] = cat(ltr(a + t * 32), ltr(a + t * 32 + 2560)); }
    SB0();
  };
  auto cs_mm = [&]() {
    SB0();
    __builtin_amdgcn_s_setprio(2);
#pragma unroll
    for (int t = 0; t < NWP; ++t) acs[t] = mfma16(fc[t], ones, acs[t]);
    __builtin_amdgcn_s_setprio(0);
    SB0();
  };
  cs_rd(2 * STG);
  float br = 0.f, bi = 0.f;
  if (SSM && h == 2) {
    const float dt = rbf(expf(bf2f(pl))), ex = expf(dt * bf2f(pr));
    float sn, cn;
    sincosf(dt * bf2f(pi), &sn, &cn);
    br = ex * cn; bi = ex * sn;
  }
  cs_mm();
  WAITV(SSM ? 0 : 15); BAR();
  cs_rd(0);
  if (SSM && h == 2) {
    const float lr = bf2f(pr), li = bf2f(pi), nr = br - 1.f, ni = bi, den = lr * lr + li * li;
    LAS float* cst = (LAS float*)LP(L_CST);
    cst[4 * 128 + lane] = 2.f * (nr * lr + ni * li) / den; cst[4 * 128 + 64 + lane] = 2.f * (ni * lr - nr * li) / den;
    cst[lane] = br; cst[64 + lane] = bi;
#pragma unroll
    for (int s = 1; s <= 8; ++s) {
      const float a = br * br - bi * bi, b = 2.f * br * bi;
      br = a; bi = b;
      const int k = s == 3 ? 1 : s == 6 ? 2 : s == 8 ? 3 : 0;
      if (k) { cst[k * 128 + lane] = br; cst[k * 128 + 64 + lane] = bi; }
    }
  }
  cs_mm();
  if (c == 0) {
#pragma unroll
    for (int t = 0; t < NWP; ++t) {
      const int col = SSM ? 32 * (t >> 1) + 8 * q + 4 * (t & 1) : 16 * t + 4 * q;
      sf4(LP(L_CS + ((h & 1) * 160 + (h >> 1) * 80 + col) * 4), acs[t]);
    }
  }
  sig(L_CSF + h * 4, 1);
}

DI void carries(const Par& p, const Ids& id, const int h, const int lane, const unsigned tag, WRing& wr) {
  const __amdgpu_buffer_rsrc_t rctl = mkrs(p.ws, WS_CTL_BYTES);
  const int c = lane & 15, q = lane >> 4;
  {
    const int ch = 16 * h + c;
    lwait<2>(L_CSF + 8, 0, 1);
    LAS float* cst = (LAS float*)LP(L_CST);
    const float l8r = cst[128 + ch], l8i = cst[192 + ch], l64r = cst[256 + ch], l64i = cst[320 + ch], lwr = cst[384 + ch], lwi = cst[448 + ch];
    const float kr = cst[512 + ch], ki = cst[576 + ch];
    float w0r = 1.f, w0i = 0.f, w1r = 1.f, w1i = 0.f, qr = kr, qi = ki;
#pragma unroll
    for (int s = 0; s < 6; ++s) {
      float a = w0r, b = w0i; cm1(a, b, lwr, lwi, 0.f, 0.f);
      if (s < id.R - 1 - q) { w0r = a; w0i = b; }
      if (s < id.R - 5 - q) { w1r = a; w1i = b; }
    }
#pragma unroll
    for (int s = 0; s < 3; ++s) { float a = qr, b = qi; cm1(a, b, l64r, l64i, 0.f, 0.f); if (s < q) { qr = a; qi = b; } }
    const __amdgpu_buffer_rsrc_t rag = mkrs(p.ws + WS_AGG + id.R * (8 * 640 * 16), id.R * 640 * 16);
    LAS float* pe = (LAS float*)LP(L_E + 8 * q * ESTR + ch * 4);
    asm volatile("" : "+v"(w0r), "+v"(w0i), "+v"(w1r), "+v"(w1i), "+v"(qr), "+v"(qi));
    unsigned ao[2], po = (q * 640 + 64 * id.g + ch) * 16;
#pragma unroll
    for (int z = 0; z < 2; ++z) ao[z] = q + 4 * z > id.R ? WS_AGG + (((q + 4 * z) * 8 + id.R) * 640 + 64 * id.g + ch) * 16 : OOB;
    asm volatile("" : "+v"(ao[0]), "+v"(ao[1]), "+v"(po));
    __builtin_amdgcn_s_setprio(3);
    HBAR();
    float er[8], ei[8];
#pragma unroll
    for (int s = 0; s < 8; ++s) { er[s] = pe[s * (ESTR / 4)]; ei[s] = pe[s * (ESTR / 4) + 64]; }
    float a = er[0], b = ei[0];
#pragma unroll
    for (int s = 1; s < 8; ++s) cm1(a, b, l8r, l8i, er[s], ei[s]);
    float ta[4], tb[4];
    gather4(a, ta); gather4(b, tb);
    float pa = 0.f, pb = 0.f;
    a = ta[0]; b = tb[0];
#pragma unroll
    for (int s = 1; s < 4; ++s) {
      if (q == s) { pa = a; pb = b; }
      cm1(a, b, l64r, l64i, ta[s], tb[s]);
    }
#pragma unroll
    for (int z = 0; z < 2; ++z)
      st16c(v4u{__float_as_uint(a), tag, __float_as_uint(b), tag}, rctl, ao[z]);
    SB0();
    wr.dma01();
    v4u p0 = ld16c(rag, po, 0), p1 = ld16c(rag, po, 4 * 640 * 16);
    SB0();
    cm1(pa, pb, kr, ki, 0.f, 0.f);
#pragma unroll
    for (int s = 0; s < 7; ++s) cm1(er[s], ei[s], kr, ki, 0.f, 0.f);
    _Pragma("unroll 1") for (int spin = 0; spin < SPIN; ++spin) { FENCE();
      const bool ok = ((q >= id.R) | ((p0.y == tag) & (p0.w == tag))) & ((q + 4 >= id.R) | ((p1.y == tag) & (p1.w == tag)));
      if (__all(ok)) break;
      p0 = ld16c(rag, po, 0); p1 = ld16c(rag, po, 4 * 640 * 16);
    }
    float ca = __uint_as_float(p1.x), cb = __uint_as_float(p1.z);
    cm1(ca, cb, w1r, w1i, 0.f, 0.f);
    { float x = __uint_as_float(p0.x), y = __uint_as_float(p0.z); cm1(x, y, w0r, w0i, ca, cb); ca = x; cb = y; }
    gather4(ca, ta); gather4(cb, tb);
    ca = (ta[0] + ta[1]) + (ta[2] + ta[3]); cb = (tb[0] + tb[1]) + (tb[2] + tb[3]);
    cm1(ca, cb, qr, qi, pa, pb);
#pragma unroll
    for (int s = 0; s < 8; ++s) {
      pe[s * (ESTR / 4)] = ca; pe[s * (ESTR / 4) + 64] = -cb;
      if (s < 7) cm1(ca, cb, l8r, l8i, er[s], ei[s]);
    }
    __builtin_amdgcn_s_setprio(0);
    HBAR();
  }
}

__global__ __launch_bounds__(512, 2) void fused(Par p) {
  const int bid = blockIdx.x, xcd = bid & 7, idx = bid >> 3, a = xcd >> 2, b = xcd & 3, s = idx >> 2;
  const int wv = rfl(threadIdx.x >> 6), lane = threadIdx.x & 63;
  Ids id;
  const bool ssm = idx < 10;
  const int part = s == 2 ? 1 : 0;
  id.R = 4 * a + (idx & 1) + 2 * ((part ? b : idx >> 1) & 1);
  const int gl = (9 - idx) >> 31;
  id.g = ((2 - 3 * gl) * b + s + 3 * gl) * (1 - part) + (8 - 12 * gl + (b >> 1)) * part;
  id.cg = id.g - 10 * gl;
  asm volatile("" ::"s"(p.x), "s"(p.ws), "s"(p.wg), "s"(p.wd), "s"(id.cg), "s"(id.R));
  RowIn rin;
  if (wv >= 4) {
    const int h = wv - 4;
    const unsigned v0 = (8 * (lane >> 3)) * 1280 + (((lane & 7) ^ (lane >> 4)) * 16);
    asm volatile("s_nop 4");
    dma_x(MkSeq<8>::T{}, mkrs(p.x, L_ * D_ * 2), LB + h * 8192, v0, v0 ^ 64, (256 * id.R + 128 * (h >> 1) + 4 * (h & 1)) * 1280);
  }
  unsigned cpf = 0;
  if (wv == 3) {
    unsigned long long pc; unsigned len;
    asm volatile("s_getpc_b64 %0\n.Lpf_here:\n\ts_mov_b32 %1, .Lpf_end-.Lpf_here" : "=s"(pc), "=s"(len));
    cpf = __builtin_amdgcn_raw_buffer_load_b32(mkrs((const void*)pc, len), lane < 13 ? (idx * 13 + lane) * 128 : OOB, 0, 0);
  }
  id.rt = 16 * a + 8 * (b >> 1) + (idx >> 2);
  id.ct = 4 * (b & 1) + (idx & 3);
  const __amdgpu_buffer_rsrc_t rctl = mkrs(p.ws, WS_CTL_BYTES);
  const __amdgpu_buffer_rsrc_t rw = mkrs(p.ws + WS_W2 + (unsigned)id.ct * (54 * 5120), 54 * 5120);
  const unsigned vl = lane * 16;
  const unsigned fo = WS_FLH + (id.rt * 8 + id.ct) * 128 + (lane & 31) * 4;
  WRing wr;
  wr.rw = rw; wr.vl = vl; wr.wv = wv;
  wr.rcr = mkrs(p.cre + id.ct * 80 * P_, 80 * P_ * 2); wr.rci = mkrs(p.cim + id.ct * 80 * P_, 80 * P_ * 2);
  wr.vc = (lane >> 2) * 1280 + (((lane & 3) ^ cswz(lane >> 4)) * 16);
  unsigned tag;
  if (wv < 4) {
    row_load(p, id, wv, lane, rin);
    if (wv == 2) cpf = ld4c(rctl, lane < 32 ? fo : lane < 40 ? WS_FLW + (lane - 32) * 128 : OOB);
    tag = ssm ? compute_wave<true>(p, id, wv, lane, rin) : compute_wave<false>(p, id, wv, lane, rin);
    WAITV(0);
    unsigned old = 0;
    if (lane == 0) old = __hip_atomic_fetch_add((LAS unsigned*)LP(L_CNT), 1u, __ATOMIC_RELAXED, __HIP_MEMORY_SCOPE_WORKGROUP);
    st4c(tag, rctl, (lane < 32 && rfl(old) == 3) ? WS_FLH + (id.R * 32 + lane) * 128 + id.cg * 4 : OOB);
    asm volatile("" ::"v"(cpf));
    STAMP(16);
#pragma unroll
    for (int it = 0; it < DW; ++it) wr.load(it);
  } else {
    HState hs;
    tag = ssm ? helper_wave<true>(p, id, wv - 4, lane, hs) : helper_wave<false>(p, id, wv - 4, lane, hs);
    if (ssm) {
      helper_tail<true>(p, id, wv - 4, lane, tag, hs);
      _Pragma("unroll 1") for (int spin = 0; spin < SPIN; ++spin) { FENCE();
        const v4u f2 = ld16c(rctl, WS_FLW + lane * 16, 0);
        if (__all((f2.x == tag) & (f2.y == tag) & (f2.z == tag) & (f2.w == tag))) break;
        __builtin_amdgcn_s_sleep(4);
      }
    }
    wr.load(0); wr.load(1); wr.load(2);
    if (!ssm) helper_tail<false>(p, id, wv - 4, lane, tag, hs);
#pragma unroll
    for (int it = 3; it < DW; ++it) wr.load(it);
    if (ssm) carries(p, id, wv - 4, lane, tag, wr); else wr.dma01();
    lwait<2>(L_CNT, 0, 4);
    STAMP(16);
    if (wv == 4) {
      _Pragma("unroll 1") for (int spin = 0; spin < SPIN; ++spin) { FENCE();
        if (__all((ld4c(rctl, fo) == tag) | ((lane & 31) < 10))) break;
        __builtin_amdgcn_s_sleep(2);
      }
      sig(L_GO, 1);
    }
    wr.dma2();
  }

  const __amdgpu_buffer_rsrc_t rh = mkrs(p.ws + WS_H + (unsigned)id.rt * (NKB * 4096), NKB * 4096);
  v4u hb[NIT][4];
  auto loadh = [&](int it) {
    const unsigned so = it < 6 ? (8 * it + wv) * 4096 : it == 6 ? (wv < 6 ? (48 + wv) * 4096 : OOB) : (8 * it - 2 + wv) * 4096;
#pragma unroll
    for (int j = 0; j < 4; ++j) { hb[it][j] = ld16c(rh, vl + j * 1024, so); SB0(); }
  };
  lwait<1>(L_GO, 0, 1);
  SB0();
  STAMP(20);
#pragma unroll
  for (int it = 0; it < DH; ++it) { loadh(it); SB0(); }
  f32x4 acc[4][5];
#pragma unroll
  for (int j = 0; j < 4; ++j)
#pragma unroll
    for (int i = 0; i < 5; ++i) acc[j][i] = f32x4{0.f, 0.f, 0.f, 0.f};
  const int tid = wv * 64 + lane;
  v2u xv[3], dv[3]; v4u sv[3]; int oo[3], ra[3];
  unsigned sfl = 0;
  const unsigned cr = wv * 5120 + (lane & 15) * 64 + (((lane >> 4) ^ cswz((lane >> 2) & 3)) * 16);
  const lp pw = LP(L_W + cr), pw2 = LP(cr);
  v4u wl[5][5];
  SB0();
#pragma unroll
  for (int it = 0; it < NIT; ++it) {
    if (it + 1 >= NG && it + 1 < NIT) {
      const int nr = it + 1 - NG;
      if (nr == 4) WAITV(LASTW);
#pragma unroll
      for (int i = 0; i < 5; ++i) wl[nr][i] = l128(nr == 2 ? pw2 + 40960 + i * 1024 : nr == 3 ? pw2 + i * 1024 : pw + (nr & 1) * 40960 + i * 1024);
      SB0();
    }
#pragma unroll
    for (int j = 0; j < 4; ++j)
#pragma unroll
      for (int i = 0; i < 5; ++i) acc[j][i] = mfma16(it >= NG ? wl[it >= NG ? it - NG : 0][i] : wr.wb[it % DW][i], hb[it][j], acc[j][i]);
    SB0();
    const int h0 = hnext(it - 1), h1 = hnext(it);
    if (h0 <= NG && NG < h1) {
      if (__builtin_expect(!__all(sfl == tag), 0)) {
        _Pragma("unroll 1") for (int spin = 0; spin < SPIN; ++spin) { FENCE();
          if (__all(ld4c(rctl, fo) == tag)) break;
          __builtin_amdgcn_s_sleep(2);
        }
      }
      FENCE();
      STAMP(18); STAMPC(28);
    }
    if (hnext(it + 1) <= NG && NG < hnext(it + 2)) sfl = ld4c(rctl, fo);
    if (it == 2) { BAR(); wr.refill(0); SB0(); }
    if (it == NG - 1) { asm volatile("s_waitcnt lgkmcnt(0)" ::: "memory"); wr.refill(1); SB0(); }
#pragma unroll
    for (int z = h0; z < h1; ++z) loadh(z);
    if (it + DW < NG) wr.load(it + DW);
    if (it == NG - 1) {
#pragma unroll
      for (int r = 0; r < 3; ++r) {
        const int sl = min(tid + 512 * r, 1279), row = sl / 20, qd = sl - 20 * row;
        const int tok = 128 * (id.rt >> 1) + 8 * (row >> 2) + 4 * (id.rt & 1) + (row & 3), d = 80 * id.ct + 4 * qd;
        oo[r] = tok * D_ + d;
        ra[r] = (row & 3) * 40960 + (qd >> 2) * 1024 + (qd & 3) * 256 + (((row >> 2) + qd) & 15) * 16;
        xv[r] = *(const v2u*)(p.x + oo[r]); dv[r] = *(const v2u*)(p.dsk + d);
        sv[r] = ld16c(rctl, WS_STATS + tok * 16, 0);
      }
    }
    SB0();
  }
  STAMP(21); STAMPC(29);
  asm volatile("" ::"v"(sv[0]), "v"(sv[1]), "v"(sv[2]), "v"(xv[2]), "v"(dv[2]));
#pragma unroll
  for (int i = 0; i < 5; ++i) {
    const lp rb = LP(wv * 5120 + i * 1024 + (lane >> 4) * 256 + (((lane & 15) + 4 * i + (lane >> 4)) & 15) * 16);
    sf4(rb, acc[0][i]); sf4(rb + 40960, acc[1][i]);
    const lp rc = rb + 81920;
    sf4(rc, acc[2][i]); sf4(rc + 40960, acc[3][i]);
  }
  BAR();
  asm volatile(".Lpf_end:");
  STAMP(19);
#pragma unroll
  for (int r = 0; r < 3; ++r) {
    if (r < 2 || wv < 4) {
      const lp q0 = LP(ra[r]);
      f32x4 sum = ((lf4(q0) + lf4(q0 + 5120)) + (lf4(q0 + 10240) + lf4(q0 + 15360))) + ((lf4(q0 + 20480) + lf4(q0 + 25600)) + (lf4(q0 + 30720) + lf4(q0 + 35840)));
      const f32x4 xf = {bflo(xv[r].x), bfhi(xv[r].x), bflo(xv[r].y), bfhi(xv[r].y)};
      const f32x4 df = {bflo(dv[r].x), bfhi(dv[r].x), bflo(dv[r].y), bfhi(dv[r].y)};
      sum += xf + (xf - __uint_as_float(sv[r].x)) * __uint_as_float(sv[r].z) * df;
      __builtin_amdgcn_raw_buffer_store_b128(__builtin_bit_cast(v4u, sum), mkrs(p.out, L_ * D_ * 4), oo[r] * 4, 0, 16);
    }
  }
  STAMP(22);
}

extern "C" void kernel_launch(void* const* d_in, const int* in_sizes, int n_in, void* d_out, int out_size, void* d_ws, size_t ws_size, hipStream_t stream) {
  Par p;
  p.x = (const u16*)d_in[0]; p.lre = (const u16*)d_in[1]; p.lim = (const u16*)d_in[2];
  p.bre = (const u16*)d_in[3]; p.bim = (const u16*)d_in[4]; p.cre = (const u16*)d_in[5]; p.cim = (const u16*)d_in[6];
  p.ldt = (const u16*)d_in[7]; p.dsk = (const u16*)d_in[8];
  p.wg = (const u16*)d_in[10]; p.wu = (const u16*)d_in[11]; p.wd = (const u16*)d_in[12];
#define WSOFF 0
  p.ws = (unsigned char*)d_ws + (WSOFF == 1 ? ((ws_size - (21u << 20)) & ~(size_t)0xFFFFF) : WSOFF == 2 ? (ws_size / 2) : 0); p.out = (float*)d_out;
  (void)hipFuncSetAttribute((const void*)fused, hipFuncAttributeMaxDynamicSharedMemorySize, LDS1);
  fused<<<256, 512, LDS1, stream>>>(p);
}
```

```cpp
#include <hip/hip_runtime.h>
#include <stdint.h>

typedef unsigned short u16;
typedef unsigned v4u __attribute__((ext_vector_type(4)));
typedef unsigned v2u __attribute__((ext_vector_type(2)));
typedef float f32x4 __attribute__((ext_vector_type(4)));
typedef float f32x2 __attribute__((ext_vector_type(2)));
typedef float f32x16 __attribute__((ext_vector_type(16)));
typedef short bf16x8 __attribute__((ext_vector_type(8)));
typedef short s16x4 __attribute__((ext_vector_type(4)));
typedef __bf16 bf2 __attribute__((ext_vector_type(2)));
#define LAS __attribute__((address_space(3)))
#define DI __device__ __forceinline__
typedef LAS unsigned char* lp;

constexpr int L_ = 2048, D_ = 640, P_ = 640, F_ = 1728;
constexpr int NKB = 94;
constexpr unsigned WS_EPOCH = 0;
constexpr unsigned WS_FLW = 2048;
constexpr unsigned WS_FLH = 4096;
constexpr unsigned WS_AGG = WS_FLH + 32768;
constexpr unsigned WS_STATS = WS_AGG + 64 * 640 * 16;
constexpr unsigned WS_CTL_BYTES = WS_STATS + 32768;
constexpr unsigned WS_W2 = 1u << 20;
constexpr unsigned WS_W2_BYTES = 8 * 54 * 5120;
constexpr unsigned WS_H = 8u << 20;

constexpr int UPO = 10368;
constexpr int STG = 32768 + 20480 + 128;
constexpr int LDS1 = 163840;
constexpr int L_STATS = 0;
constexpr int L_CS = L_STATS + 2048;
constexpr int L_CNT = L_CS + 1280, L_GO = L_CNT + 4, L_CSF = L_CNT + 16;
constexpr int LB = LDS1 - 3 * STG;
constexpr int L_CST = LB + 49152;
constexpr int L_W = 81920, L_W2 = 40960;
constexpr int L_E = LB + STG, ESTR = 528;
constexpr int SPIN = 1 << 18;
constexpr unsigned OOB = 0x40000000u;

DI float bf2f(unsigned b) { return __uint_as_float(b << 16); }
DI float bflo(unsigned w) { return __uint_as_float(w << 16); }
DI float bfhi(unsigned w) { return __uint_as_float(w & 0xffff0000u); }
DI unsigned pk2(float a, float b) { f32x2 v = {a, b}; return __builtin_bit_cast(unsigned, __builtin_convertvector(v, bf2)); }
DI float rbf(float a) { return bflo(pk2(a, a) & 0xffffu); }
DI f32x4 mfma16(v4u a, v4u b, f32x4 c) { return __builtin_amdgcn_mfma_f32_16x16x32_bf16(__builtin_bit_cast(bf16x8, a), __builtin_bit_cast(bf16x8, b), c, 0, 0, 0); }
DI f32x16 mfma32(v4u a, v4u b, f32x16 c) { return __builtin_amdgcn_mfma_f32_32x32x16_bf16(__builtin_bit_cast(bf16x8, a), __builtin_bit_cast(bf16x8, b), c, 0, 0, 0); }
DI unsigned rfl(unsigned v) { return __builtin_amdgcn_readfirstlane(v); }
DI __amdgpu_buffer_rsrc_t mkrs(const void* p, unsigned bytes) { return __builtin_amdgcn_make_buffer_rsrc((void*)p, 0, bytes, 0x00020000); }
template <int LI, int SI> DI void glds(__amdgpu_buffer_rsrc_t rs, unsigned lb, unsigned voff, unsigned sb) {
  unsigned t;
  asm volatile("s_add_u32 m0, %1, %5\n\ts_add_u32 %0, %4, %6\n\tbuffer_load_dwordx4 %2, %3, %0 offen lds" : "=&s"(t) : "s"(lb), "v"(voff), "s"(rs), "s"(sb), "i"(LI), "i"(SI) : "scc");
}
template <int... I> struct Seq {};
template <int N, int... I> struct MkSeq : MkSeq<N - 1, N - 1, I...> {};
template <int... I> struct MkSeq<0, I...> { typedef Seq<I...> T; };
template <int... I> DI void dma_x(Seq<I...>, __amdgpu_buffer_rsrc_t rs, unsigned lb, unsigned v0, unsigned v1, unsigned sb) { (glds<I * 1024, (64 * (I & 1) + (I >> 1)) * 1280>(rs, lb, (I & 1) ? v1 : v0, sb), ...); }
template <int... I> DI void dma_b(Seq<I...>, __amdgpu_buffer_rsrc_t rs, unsigned lb, const unsigned* v, unsigned sb) { (glds<I * 1024, (16 * (I & 1) + 4 * (I >> 1)) * 1280>(rs, lb, v[I & 1], sb), ...); }
template <int... I> DI void dma_g(Seq<I...>, __amdgpu_buffer_rsrc_t rs, unsigned lb, const unsigned* v, unsigned sb) { (glds<I * 1024, 0>(rs, lb, v[I], sb), ...); }
DI lp LP(unsigned a) { return (lp)(uintptr_t)a; }
DI v4u l128(lp a) { return *(LAS v4u*)a; }
DI f32x4 lf4(lp a) { return *(LAS f32x4*)a; }
DI void sf4(lp a, f32x4 v) { *(LAS f32x4*)a = v; }
DI s16x4 ltr(lp a) { return __builtin_amdgcn_ds_read_tr16_b64_v4i16((LAS s16x4*)a); }
DI v4u cat(s16x4 lo, s16x4 hi) { return __builtin_bit_cast(v4u, __builtin_shufflevector(lo, hi, 0, 1, 2, 3, 4, 5, 6, 7)); }
DI v4u ld16(__amdgpu_buffer_rsrc_t r, unsigned vo, unsigned so) { return __builtin_amdgcn_raw_buffer_load_b128(r, vo, so, 0); }
DI v4u ld16c(__amdgpu_buffer_rsrc_t r, unsigned vo, unsigned so) { return __builtin_amdgcn_raw_buffer_load_b128(r, vo, so, 16); }
DI unsigned ld4c(__amdgpu_buffer_rsrc_t r, unsigned vo) { return __builtin_amdgcn_raw_buffer_load_b32(r, vo, 0, 16); }
DI void st16c(v4u v, __amdgpu_buffer_rsrc_t r, unsigned vo) { __builtin_amdgcn_raw_buffer_store_b128(v, r, vo, 0, 16); }
DI void st4c(unsigned v, __amdgpu_buffer_rsrc_t r, unsigned vo) { __builtin_amdgcn_raw_buffer_store_b32(v, r, vo, 0, 16); }
#define BAR() asm volatile("s_waitcnt lgkmcnt(0)\n\ts_barrier" ::: "memory")
#define SB0() __builtin_amdgcn_sched_barrier(0)
#define WAITV(n) asm volatile("s_waitcnt vmcnt(%0)" ::"i"(n) : "memory")
#define FENCE() asm volatile("" ::: "memory")
template <int C> DI float dppf(float v) { return __int_as_float(__builtin_amdgcn_update_dpp(0, __float_as_int(v), C, 0xf, 0xf, true)); }
DI float sum8(float v) {
  v += dppf<0xB1>(v); v += dppf<0x4E>(v); v += dppf<0x141>(v);
  return v;
}
DI void gather4(float x, float (&t)[4]) {
  const auto a = __builtin_amdgcn_permlane16_swap(__float_as_uint(x), __float_as_uint(x), false, false);
  const auto e = __builtin_amdgcn_permlane32_swap(a[0], a[0], false, false), o = __builtin_amdgcn_permlane32_swap(a[1], a[1], false, false);
  t[0] = __uint_as_float(e[0]); t[2] = __uint_as_float(e[1]); t[1] = __uint_as_float(o[0]); t[3] = __uint_as_float(o[1]);
}
DI void cm1(float& a, float& b, float lr, float li, float ur, float ui) {
  const float na = fmaf(lr, a, fmaf(-li, b, ur)), nb = fmaf(lr, b, fmaf(li, a, ui));
  a = na; b = nb;
}
DI f32x4 fma4(f32x4 a, f32x4 b, f32x4 c) { return __builtin_elementwise_fma(a, b, c); }
DI f32x4 sp4(float a) { return f32x4{a, a, a, a}; }
DI void cmac(f32x4& a, f32x4& b, f32x4 lr, f32x4 li, f32x4 ur, f32x4 ui) {
  const f32x4 na = fma4(lr, a, fma4(-li, b, ur)), nb = fma4(lr, b, fma4(li, a, ui));
  a = na; b = nb;
}

struct Par {
  const u16 *x, *lre, *lim, *bre, *bim, *cre, *cim, *ldt, *dsk, *wg, *wu, *wd;
  unsigned char* ws;
  float* out;
};
struct Ids { int R, g, cg, rt, ct; };


constexpr int DW = 5, DH = 2, NIT = 12, NG = 7;
constexpr int DMAX = 2;
constexpr int hnext(int it) { const int d = it < 2 ? DH : (DH + it - 1 < DMAX ? DH + it - 1 : DMAX), n = it + 1 + d; return n < NIT ? n : NIT; }
constexpr int LASTW = 4 * (hnext(9) - hnext(5)) + 9;
#define PIN4(x) asm volatile("" : "+v"(x))
DI void sig(unsigned addr, unsigned v) {
  asm volatile("s_mov_b64 exec, 1\n\tds_write_b32 %0, %1\n\ts_mov_b64 exec, -1" ::"v"(addr), "v"(v) : "memory");
}
DI unsigned lrd(unsigned addr) { return *(volatile LAS unsigned*)LP(addr); }
template <int SL = 1> DI void lwait(unsigned addr, unsigned v, unsigned need) {
  if (__builtin_expect(!__all(v >= need), 0)) {
    _Pragma("unroll 1") for (int spin = 0; spin < 64 * SPIN; ++spin) { FENCE();
      if (__all(lrd(addr) >= need)) break;
      __builtin_amdgcn_s_sleep(SL);
    }
  }
  FENCE();
}

template <int... I> DI void dma_c1(Seq<I...>, __amdgpu_buffer_rsrc_t rs, unsigned lb, unsigned v, unsigned sb) { (glds<I * 1024, I * 16 * 1280>(rs, lb, v, sb), ...); }
template <int... I> DI void dma_c2(Seq<I...>, __amdgpu_buffer_rsrc_t rs, unsigned lb, unsigned v, unsigned sb) { (glds<(I & 1) * 5120 + (I >> 1) * 1024, (I & 1) * 64 + (I >> 1) * 16 * 1280>(rs, lb, v, sb), ...); }
DI unsigned cswz(unsigned g) { return ((g << 1) & 2) ^ ((g >> 1) * 3); }
struct WRing {
  v4u wb[DW][5];
  __amdgpu_buffer_rsrc_t rw, rcr, rci;
  unsigned vl, vc, wv;
  DI void load(int it) {
#pragma unroll
    for (int i = 0; i < 5; ++i) { wb[it % DW][i] = ld16c(rw, vl + (i & 3) * 1024, (8 * it + wv) * 5120 + (i >> 2) * 4096); SB0(); }
  }
  DI void dma01() {
    asm volatile("s_nop 4");
    dma_c2(MkSeq<10>::T{}, rcr, L_W + (wv - 4) * 20480, vc, (wv - 4) * 256);
    dma_c2(MkSeq<10>::T{}, rcr, L_W + (wv - 4) * 20480 + 10240, vc, (wv - 4) * 256 + 128);
  }
  DI void dma2() {
    asm volatile("s_nop 4");
    dma_c2(MkSeq<10>::T{}, wv < 6 ? rcr : rci, L_W2 + (wv - 4) * 10240, vc, wv < 6 ? 1024 + (wv - 4) * 128 : (wv - 6) * 128);
  }
  DI void refill(int r) {
    dma_c1(MkSeq<5>::T{}, rci, r * L_W + wv * 5120, vc, (4 + 8 * r + wv) * 64);
  }
};

DI float wsum(float v) {
  v = sum8(v); v += dppf<0x128>(v);
  float t[4]; gather4(v, t);
  return (t[0] + t[1]) + (t[2] + t[3]);
}
DI unsigned mktag(unsigned epoch) { return rfl(epoch) * 0x9E3779B1u + 0x7F4A7C15u; }
struct RowIn { v4u xa[2]; unsigned xb[2], epoch; };
DI void row_load(const Par& p, const Ids& id, const int w, const int lane, RowIn& in) {
  const int row = 256 * id.R + 8 * id.cg + 2 * w;
#pragma unroll
  for (int z = 0; z < 2; ++z) { in.xa[z] = *(const v4u*)(p.x + (row + z) * D_ + lane * 8); in.xb[z] = *(const unsigned*)(p.x + (row + z) * D_ + 512 + lane * 2); }
  in.epoch = ld4c(mkrs(p.ws, WS_CTL_BYTES), WS_EPOCH);
  SB0();
}
DI unsigned row_stats(const Par& p, const Ids& id, const int w, const int lane, const RowIn& in) {
  const __amdgpu_buffer_rsrc_t rctl = mkrs(p.ws, WS_CTL_BYTES);
  const int row = 256 * id.R + 8 * id.cg + 2 * w;
  const v4u* xa = in.xa; const unsigned* xb = in.xb; const unsigned epoch = in.epoch;
  float v[2][10], mean[2], s2[2];
#pragma unroll
  for (int z = 0; z < 2; ++z) {
    const float t[10] = {bflo(xa[z].x), bfhi(xa[z].x), bflo(xa[z].y), bfhi(xa[z].y), bflo(xa[z].z), bfhi(xa[z].z), bflo(xa[z].w), bfhi(xa[z].w), bflo(xb[z]), bfhi(xb[z])};
#pragma unroll
    for (int e = 0; e < 10; ++e) v[z][e] = t[e];
    mean[z] = wsum(((t[0] + t[1]) + (t[2] + t[3])) + ((t[4] + t[5]) + (t[6] + t[7])) + (t[8] + t[9])) * (1.f / D_);
  }
#pragma unroll
  for (int z = 0; z < 2; ++z) {
    float a = 0.f, b = 0.f;
#pragma unroll
    for (int e = 0; e < 5; ++e) { a = fmaf(v[z][e] - mean[z], v[z][e] - mean[z], a); b = fmaf(v[z][5 + e] - mean[z], v[z][5 + e] - mean[z], b); }
    s2[z] = wsum(a + b);
  }
  const unsigned tag = mktag(epoch);
  const float m = rbf((lane & 1) ? mean[1] : mean[0]), rs = 1.f / rbf(sqrtf(rbf(rbf(((lane & 1) ? s2[1] : s2[0]) * (1.f / D_)) + 1e-5f)));
  st16c(v4u{__float_as_uint(m), tag, __float_as_uint(rs), tag}, rctl, lane < 2 ? WS_STATS + (row + lane) * 16 : OOB);
  return tag;
}

template <bool SSM>
DI unsigned compute_wave(const Par& p, const Ids& id, const int w, const int lane, const RowIn& rin) {
  unsigned tag, cf = 0;
  if constexpr (!SSM) {
    const int r = lane & 31, hh = lane >> 5;
    const unsigned xa = (64 * w + r) * 128 + ((hh ^ ((r >> 1) & 7)) * 16);
    const unsigned wa = 32768 + ((lane >> 4) & 1) * UPO + (8 * hh + ((lane & 15) >> 2)) * 160 + (lane & 3) * 8;
    f32x16 acc[5][2] = {};
#pragma unroll
    for (int e = 0; e < 5; ++e) { PIN4(acc[e][0]); PIN4(acc[e][1]); }
    v4u fx[2][2]; s16x4 fl[4], fh[4];
    lp px[4], pw;
    auto setp = [&](unsigned sb) {
#pragma unroll
      for (int s = 0; s < 4; ++s) px[s] = LP(LB + sb + (xa ^ (32 * s)));
      pw = LP(LB + sb + wa);
    };
    auto woff = [&](int n) { return (n / 5) * 2560 + (n % 5) * 32; };
    setp(0);
    SB0();
    tag = row_stats(p, id, w, lane, rin);
    SB0();
    BAR();
    fx[0][0] = l128(px[0]); fx[0][1] = l128(px[0] + 4096);
#pragma unroll
    for (int n = 0; n < 3; ++n) { fl[n] = ltr(pw + woff(n)); fh[n] = ltr(pw + woff(n) + 640); }
    unsigned sb = 0;
#pragma unroll 1
    for (int i = 0; i < 10; ++i) {
#pragma unroll
      for (int n = 0; n < 20; ++n) {
        const int ks = n / 5, e = n % 5, nn = (n + 3) % 20;
        if (n == 17) { if (i < 9) BAR(); sb = sb == 2 * STG ? 0 : sb + STG; setp(sb); SB0(); }
        const v4u wf = cat(fl[n & 3], fh[n & 3]);
        acc[e][0] = mfma32(wf, fx[ks & 1][0], acc[e][0]); SB0();
        fl[(n + 3) & 3] = ltr(pw + woff(nn));
        fh[(n + 3) & 3] = ltr(pw + woff(nn) + 640); SB0();
        acc[e][1] = mfma32(wf, fx[ks & 1][1], acc[e][1]); SB0();
        if (ks < 3 && e == 1) { fx[(ks + 1) & 1][0] = l128(px[ks + 1]); fx[(ks + 1) & 1][1] = l128(px[ks + 1] + 4096); }
        if (n == 17) { fx[0][0] = l128(px[0]); fx[0][1] = l128(px[0] + 4096); }
        if (n == 18) cf = lrd(L_CSF + (lane & 3) * 4);
        SB0();
      }
    }
    lwait(L_CSF + (lane & 3) * 4, cf, 1);
    const __amdgpu_buffer_rsrc_t rh = mkrs(p.ws + WS_H + (unsigned)(4 * id.R + w) * (NKB * 4096), 54 * 4096);
    const unsigned so0 = (r >> 4) * 1024 + (hh * 16 + (r & 15)) * 16;
    float mu[2], r2[2];
#pragma unroll
    for (int u = 0; u < 2; ++u) {
      const int F = 4 * w + 2 * u + (r >> 4);
      const f32x2 s = *(LAS f32x2*)LP(L_STATS + (128 * (F >> 3) + 8 * (r & 15) + (F & 7)) * 8);
      mu[u] = s.x; r2[u] = s.y * s.y;
    }
    const lp pc = LP(L_CS + hh * 16);
#pragma unroll
    for (int e = 0; e < 5; ++e) {
      const f32x4 g0 = lf4(pc + e * 64) + lf4(pc + e * 64 + 640), g1 = lf4(pc + e * 64 + 32) + lf4(pc + e * 64 + 672);
      const f32x4 u0 = lf4(pc + e * 64 + 320) + lf4(pc + e * 64 + 960), u1 = lf4(pc + e * 64 + 352) + lf4(pc + e * 64 + 992);
      const int T = 5 * id.g + e;
      const unsigned so = so0 + (T >> 1) * 4096 + (T & 1) * 512;
#pragma unroll
      for (int u = 0; u < 2; ++u) {
        const f32x16 a = acc[e][u];
        const f32x4 nm = sp4(-mu[u]);
        const f32x4 h0 = fma4(nm, g0, f32x4{a[0], a[1], a[2], a[3]}) * fma4(nm, u0, f32x4{a[8], a[9], a[10], a[11]}) * r2[u];
        const f32x4 h1 = fma4(nm, g1, f32x4{a[4], a[5], a[6], a[7]}) * fma4(nm, u1, f32x4{a[12], a[13], a[14], a[15]}) * r2[u];
        st16c(v4u{pk2(h0.x, h0.y), pk2(h0.z, h0.w), pk2(h1.x, h1.y), pk2(h1.z, h1.w)}, rh, so + u * 2048);
      }
    }
  } else {
    const int c = lane & 15, q = lane >> 4, G = w >> 1, blk = w & 1;
    const unsigned xa = (128 * G + c) * 128 + ((q ^ (c >> 1)) * 16);
    const unsigned wa = 32768 + (32 * blk + c) * 128 + ((q ^ (c >> 1)) * 16);
    f32x4 acc[8][4] = {};
#pragma unroll
    for (int j = 0; j < 8; ++j)
#pragma unroll
      for (int t = 0; t < 4; ++t) PIN4(acc[j][t]);
    v4u fw[2][4], fx[4];
    lp px[2], pw[2];
    auto setp = [&](unsigned sb) { px[0] = LP(LB + sb + xa); px[1] = LP(LB + sb + (xa ^ 64)); pw[0] = LP(LB + sb + wa); pw[1] = LP(LB + sb + (wa ^ 64)); };
    auto woff = [&](int t) { return (t >> 1) * 8192 + (t & 1) * 2048; };
    setp(0);
    SB0();
    tag = row_stats(p, id, w, lane, rin);
    SB0();
    BAR();
#pragma unroll
    for (int t = 0; t < 4; ++t) fw[0][t] = l128(pw[0] + woff(t));
#pragma unroll
    for (int n = 0; n < 3; ++n) fx[n] = l128(px[0] + n * 2048);
    unsigned sb = 0;
#pragma unroll 1
    for (int i = 0; i < 10; ++i) {
#pragma unroll
      for (int n = 0; n < 16; ++n) {
        const int kk = n >> 3, j = n & 7, nn = (n + 3) & 15;
        if (n == 13) { if (i < 9) BAR(); sb = sb == 2 * STG ? 0 : sb + STG; setp(sb); SB0(); }
        const v4u xf = fx[n & 3];
        acc[j][0] = mfma16(fw[kk][0], xf, acc[j][0]); SB0();
        fx[(n + 3) & 3] = l128(px[nn >> 3] + (nn & 7) * 2048); SB0();
        acc[j][1] = mfma16(fw[kk][1], xf, acc[j][1]); SB0();
        if (n >= 2 && n < 6) fw[1][n - 2] = l128(pw[1] + woff(n - 2));
        if (n == 13) fw[0][0] = l128(pw[0] + woff(0));
        if (n == 14) fw[0][2] = l128(pw[0] + woff(2));
        SB0();
        acc[j][2] = mfma16(fw[kk][2], xf, acc[j][2]); SB0();
        if (n == 13) fw[0][1] = l128(pw[0] + woff(1));
        if (n == 14) fw[0][3] = l128(pw[0] + woff(3));
        SB0();
        acc[j][3] = mfma16(fw[kk][3], xf, acc[j][3]); SB0();
        if (n == 14) { cf = lrd(L_CSF + (lane & 3) * 4); SB0(); }
      }
    }
    lwait(L_CSF + (lane & 3) * 4, cf, 1);
    const lp pe = LP(L_E + (16 * G + c) * ESTR + (32 * blk + 8 * q) * 4);
    const lp pk = LP(L_CST + (32 * blk + 8 * q) * 4);
    f32x4 lr[2], li[2], er[2], ei[2];
    {
      const lp ps = LP(L_STATS + (128 * G + 8 * c) * 8), pc = LP(L_CS + (32 * blk + 8 * q) * 4);
      f32x4 cs[4], st[4];
#pragma unroll
      for (int t = 0; t < 4; ++t) { cs[t] = lf4(pc + (t >> 1) * 320 + (t & 1) * 16) + lf4(pc + 640 + (t >> 1) * 320 + (t & 1) * 16); st[t] = lf4(ps + t * 16); }
#pragma unroll
      for (int ab = 0; ab < 2; ++ab) { lr[ab] = lf4(pk + ab * 16); li[ab] = lf4(pk + 256 + ab * 16); }
#pragma unroll
      for (int jj = 0; jj < 4; ++jj)
#pragma unroll
        for (int t = 0; t < 4; ++t) {
          acc[2 * jj][t] = fma4(sp4(-st[jj].x), cs[t], acc[2 * jj][t]) * st[jj].y;
          acc[2 * jj + 1][t] = fma4(sp4(-st[jj].z), cs[t], acc[2 * jj + 1][t]) * st[jj].w;
        }
    }
#pragma unroll
    for (int ab = 0; ab < 2; ++ab) { er[ab] = acc[0][ab]; ei[ab] = acc[0][2 + ab]; }
#pragma unroll
    for (int j = 1; j < 8; ++j)
#pragma unroll
      for (int ab = 0; ab < 2; ++ab) cmac(er[ab], ei[ab], lr[ab], li[ab], acc[j][ab], acc[j][2 + ab]);
#pragma unroll
    for (int ab = 0; ab < 2; ++ab) { sf4(pe + ab * 16, er[ab]); sf4(pe + 256 + ab * 16, ei[ab]); }
    SB0();
    BAR();
#pragma unroll
    for (int ab = 0; ab < 2; ++ab) {
      const f32x4 kr = lf4(pk + 512 * 4 + ab * 16), ki = lf4(pk + 512 * 4 + 256 + ab * 16);
#pragma unroll
      for (int j = 0; j < 8; ++j) {
        const f32x4 ur = acc[j][ab], ui = acc[j][2 + ab];
        acc[j][ab] = fma4(-ki, ui, kr * ur); acc[j][2 + ab] = fma4(-ki, ur, -kr * ui);
        PIN4(acc[j][ab]); PIN4(acc[j][2 + ab]);
      }
      li[ab] = -li[ab];
    }
    const __amdgpu_buffer_rsrc_t rh = mkrs(p.ws + WS_H + (unsigned)(4 * id.R + 2 * G) * (NKB * 4096), 2 * NKB * 4096);
    unsigned vo[4];
#pragma unroll
    for (int z = 0; z < 4; ++z) { vo[z] = (54 + 2 * id.g + blk + 20 * (z & 1) + NKB * (z >> 1)) * 4096 + lane * 16; PIN4(vo[z]); }
    SB0();
    BAR();
#pragma unroll
    for (int ab = 0; ab < 2; ++ab) { er[ab] = lf4(pe + ab * 16); ei[ab] = lf4(pe + 256 + ab * 16); }
#pragma unroll
    for (int j = 0; j < 8; ++j) {
#pragma unroll
      for (int ab = 0; ab < 2; ++ab) cmac(er[ab], ei[ab], lr[ab], li[ab], acc[j][ab], acc[j][2 + ab]);
      st16c(v4u{pk2(er[0].x, er[0].y), pk2(er[0].z, er[0].w), pk2(er[1].x, er[1].y), pk2(er[1].z, er[1].w)}, rh, vo[2 * (j >> 2)] + (j & 3) * 1024);
      st16c(v4u{pk2(ei[0].x, ei[0].y), pk2(ei[0].z, ei[0].w), pk2(ei[1].x, ei[1].y), pk2(ei[1].z, ei[1].w)}, rh, vo[2 * (j >> 2) + 1] + (j & 3) * 1024);
    }
  }
  SB0();
  return tag;
}

#define HBAR() do { SB0(); BAR(); SB0(); } while (0)
struct HState { f32x4 acs[5]; unsigned pl, pr, pi; };
template <bool SSM>
DI unsigned helper_wave(const Par& p, const Ids& id, const int h, const int lane, HState& hs) {
  constexpr int NWP = SSM ? 4 : 5, NP = 8 + NWP;
  constexpr unsigned WSTEP = SSM ? 128u : 64u * 3456u;
  const int c = lane & 15, q = lane >> 4;
  const __amdgpu_buffer_rsrc_t rctl = mkrs(p.ws, WS_CTL_BYTES);
  const __amdgpu_buffer_rsrc_t rx = mkrs(p.x, L_ * D_ * 2);
  const __amdgpu_buffer_rsrc_t rw = mkrs(SSM ? ((h >> 1) ? p.bim : p.bre) : ((h >> 1) ? p.wu : p.wg), SSM ? P_ * D_ * 2 : D_ * F_ * 2);
  const unsigned v0 = (8 * (lane >> 3)) * 1280 + (((lane & 7) ^ (lane >> 4)) * 16), v1 = v0 ^ 64;
  unsigned st = 0;
  const unsigned sx = (256 * id.R + 128 * (h >> 1) + 4 * (h & 1)) * 1280;
  auto issue_x = [&](unsigned s, unsigned sb) {
    dma_x(MkSeq<8>::T{}, rx, LB + sb + h * 8192, v0, v1, s);
  };
  unsigned vw[NWP];
  if constexpr (SSM) {
    vw[0] = (8 * (lane >> 5) + ((lane >> 3) & 3)) * 1280 + (((lane & 7) ^ (lane >> 4)) * 16); vw[1] = vw[0] ^ 64;
  } else {
#pragma unroll
    for (int i = 0; i < 5; ++i) { const int lc = 64 * (5 * (h & 1) + i) + lane, pos = lc / 10; vw[i] = pos * 3456 + (lc - pos * 10) * 16; }
  }
  const unsigned sw = SSM ? (64 * id.g + 32 * (h & 1)) * 1280 : 160 * id.g;
  const unsigned dw = LB + 32768 + (h >> 1) * (SSM ? 8192 : UPO) + (h & 1) * NWP * 1024;
  auto issue_w = [&](unsigned s, unsigned sb) {
    if constexpr (SSM) dma_b(MkSeq<4>::T{}, rw, sb + dw, vw, s); else dma_g(MkSeq<5>::T{}, rw, sb + dw, vw, s);
  };
  asm volatile("s_nop 4");
  issue_w(sw, 0);
  unsigned sb = STG;
  auto step = [&]() {
    ++st;
    issue_x(sx + st * 128, sb); issue_w(sw + st * WSTEP, sb);
    sb = sb == 2 * STG ? 0 : sb + STG;
  };
  step();
  if (h == 3 && lane < 8) *(LAS unsigned*)LP(L_CNT + lane * 4) = 0u;
  f32x4 acs[NWP] = {};
  v4u fc[NWP];
  const unsigned ca = SSM ? 32768 + (h >> 1) * 8192 + c * 128 + (((4 * (h & 1) + q) ^ (c >> 1)) * 16)
                          : 32768 + (h >> 1) * UPO + (32 * (h & 1) + 4 * q + (c >> 2)) * 160 + (c & 3) * 8;
  const v4u ones = {0x3F803F80u, 0x3F803F80u, 0x3F803F80u, 0x3F803F80u};
  auto cs_rd = [&](unsigned s) {
    const lp a = LP(LB + s + ca);
#pragma unroll
    for (int t = 0; t < NWP; ++t) { if constexpr (SSM) fc[t] = l128(a + t * 2048); else fc[t] = cat(ltr(a + t * 32), ltr(a + t * 32 + 2560)); }
    SB0();
  };
  auto cs_mm = [&]() {
    SB0();
    __builtin_amdgcn_s_setprio(2);
#pragma unroll
    for (int t = 0; t < NWP; ++t) acs[t] = mfma16(fc[t], ones, acs[t]);
    __builtin_amdgcn_s_setprio(0);
    SB0();
  };
  constexpr int JL = 1;
  const int tsk = (id.g * 8 + id.R) * 4 + h, kb = tsk / 10, dq = tsk - 10 * kb;
  v4u f[4] = {}, sel[2], sv = {}, fl = {};
  unsigned fvo, fso[4], tch = 0;
  const __amdgpu_buffer_rsrc_t rf = mkrs(SSM ? ((tsk & 1) ? p.cim : p.cre) : p.wd, SSM ? D_ * P_ * 2 : F_ * D_ * 2);
  const __amdgpu_buffer_rsrc_t r2 = mkrs(p.ws + WS_W2, tsk < 540 ? WS_W2_BYTES : 0);
  if constexpr (SSM) fvo = lane < 40 ? ((tsk >> 1) * 40 + lane) * 128 : OOB;
  else {
    fvo = (32 * kb + 16 * ((lane >> 3) & 1) + 4 * (q & 1) + (q >> 1)) * 1280 + dq * 128 + (lane & 7) * 16;
#pragma unroll
    for (int i = 0; i < 4; ++i) {
      const int dt = 4 * dq + 2 * (q & 1) + (i >> 1), ct = dt / 5;
      fso[i] = ((ct * 54 + kb) * 5 + dt - 5 * ct) * 1024 + (16 * (2 * (q >> 1) + (c >> 3)) + 8 * (i & 1) + (c & 7)) * 16;
      PIN4(fso[i]);
    }
#pragma unroll
    for (int hf = 0; hf < 2; ++hf) {
#pragma unroll
      for (int e = 0; e < 4; ++e) sel[hf][e] = (8 * q + 2 * e == 16 * hf + c ? 0x3F80u : 0u) | (8 * q + 2 * e + 1 == 16 * hf + c ? 0x3F800000u : 0u);
      PIN4(sel[hf]);
    }
  }
  PIN4(fvo);
  unsigned epoch, pl = 0, pr = 0, pi = 0, tag = 0;
  unsigned so = WS_STATS + (256 * id.R + 64 * h + lane) * 16, flo = h == 3 ? WS_FLW + lane * 16 : OOB, fwo = (h == 3 && lane == 0) ? WS_FLW + blockIdx.x * 4 : OOB;
  PIN4(so); PIN4(flo); PIN4(fwo);
  asm volatile("buffer_load_dword %0, off, %1, 0 sc1" : "=v"(epoch) : "s"(rctl));
  if constexpr (SSM) {
    const unsigned po = (64 * id.g + lane) * 2;
    asm volatile("buffer_load_ushort %0, %3, %4, 0 offen\n\tbuffer_load_ushort %1, %3, %5, 0 offen\n\tbuffer_load_ushort %2, %3, %6, 0 offen"
                 : "=&v"(pl), "=&v"(pr), "=&v"(pi) : "v"(po), "s"(mkrs(p.ldt, P_ * 2)), "s"(mkrs(p.lre, P_ * 2)), "s"(mkrs(p.lim, P_ * 2)));
  }
  WAITV(NP + (SSM ? 4 : 1)); BAR();
#pragma unroll
  for (int j = 0; j < 8; ++j) {
    cs_rd((j % 3) * STG);
    if (j == JL + 4) { tag = mktag(epoch); st4c(tag, rctl, fwo); }
    if (j == 6) asm volatile("buffer_load_dwordx4 %0, %1, %2, 0 offen sc1" : "=v"(sv) : "v"(so), "s"(rctl));
    if (!SSM && j == 7) asm volatile("buffer_load_dwordx4 %0, %1, %2, 0 offen sc1" : "=v"(fl) : "v"(flo), "s"(rctl));
    SB0();
    step();
    if (SSM && j == 3) asm volatile("buffer_load_dword %0, %1, %2, 0 offen" : "=v"(tch) : "v"(fvo), "s"(rf));
    if (!SSM && j == JL)
      asm volatile("buffer_load_dwordx4 %0, %4, %5, 0 offen\n\tbuffer_load_dwordx4 %1, %4, %5, 0 offen offset:2560\n\tbuffer_load_dwordx4 %2, %4, %5, %6 offen\n\tbuffer_load_dwordx4 %3, %4, %5, %6 offen offset:2560"
                   : "=&v"(f[0]), "=&v"(f[1]), "=&v"(f[2]), "=&v"(f[3]) : "v"(fvo), "s"(rf), "s"(8 * 1280));
    cs_mm();
    if (!SSM && j == JL + 2) {
      f32x4 d[8];
      __builtin_amdgcn_s_setprio(2);
#pragma unroll
      for (int z = 0; z < 8; ++z) d[z] = mfma16(f[z >> 1], sel[z & 1], f32x4{0.f, 0.f, 0.f, 0.f});
#pragma unroll
      for (int i = 0; i < 4; ++i) st16c(v4u{pk2(d[0][i], d[1][i]), pk2(d[2][i], d[3][i]), pk2(d[4][i], d[5][i]), pk2(d[6][i], d[7][i])}, r2, fso[i]);
      __builtin_amdgcn_s_setprio(0);
      SB0();
    }
    if (!SSM && (j == JL || j == JL + 2)) WAITV(NP + 4);
    else if (SSM && j == 3) WAITV(NP + 1);
    else if (j == JL + 1) asm volatile("s_waitcnt vmcnt(%8)" : "+v"(f[0]), "+v"(f[1]), "+v"(f[2]), "+v"(f[3]), "+v"(epoch), "+v"(pl), "+v"(pr), "+v"(pi) : "i"(NP) : "memory");
    else if (j == 4) asm volatile("s_waitcnt vmcnt(%1)" : "+v"(tch) : "i"(NP) : "memory");
    else if (j == 6) asm volatile("s_waitcnt vmcnt(%1)" : "+v"(sv) : "i"(NP) : "memory");
    else if (j == 7) asm volatile("s_waitcnt vmcnt(%1)" : "+v"(fl) : "i"(NP) : "memory");
    else if (j == JL + 4) WAITV(NP + 1);
    else WAITV(NP);
    if (j == 6) {
      v2u o = {sv.x, sv.z};
      if (!__all((sv.y == tag) & (sv.w == tag))) {
        _Pragma("unroll 1") for (int spin = 0; spin < SPIN; ++spin) { FENCE();
          const v4u s2 = ld16c(rctl, so, 0);
          o = v2u{s2.x, s2.z};
          if (__all((s2.y == tag) & (s2.w == tag))) break;
          __builtin_amdgcn_s_sleep(4);
        }
      }
      *(LAS v2u*)LP(L_STATS + (64 * h + lane) * 8) = o;
    }
    if (!SSM && j == 7 && h == 3) {
      if (!__all((fl.x == tag) & (fl.y == tag) & (fl.z == tag) & (fl.w == tag))) {
        _Pragma("unroll 1") for (int spin = 0; spin < SPIN; ++spin) { FENCE();
          const v4u f2 = ld16c(rctl, flo, 0);
          if (__all((f2.x == tag) & (f2.y == tag) & (f2.z == tag) & (f2.w == tag))) break;
          __builtin_amdgcn_s_sleep(4);
        }
      }
      st4c(tag, rctl, (lane == 0 && blockIdx.x == 255) ? WS_EPOCH : OOB);
    }
    BAR();
  }
#pragma unroll
  for (int t = 0; t < 5; ++t) hs.acs[t] = acs[t < NWP ? t : 0];
  hs.pl = pl; hs.pr = pr; hs.pi = pi;
  return tag;
}
template <bool SSM>
DI void helper_tail(const Par& p, const Ids& id, const int h, const int lane, const unsigned tag, HState& hs) {
  constexpr int NWP = SSM ? 4 : 5;
  const int c = lane & 15, q = lane >> 4;
  const __amdgpu_buffer_rsrc_t rctl = mkrs(p.ws, WS_CTL_BYTES);
  f32x4* acs = hs.acs;
  const unsigned pl = hs.pl, pr = hs.pr, pi = hs.pi;
  v4u fc[NWP];
  const unsigned ca = SSM ? 32768 + (h >> 1) * 8192 + c * 128 + (((4 * (h & 1) + q) ^ (c >> 1)) * 16)
                          : 32768 + (h >> 1) * UPO + (32 * (h & 1) + 4 * q + (c >> 2)) * 160 + (c & 3) * 8;
  const v4u ones = {0x3F803F80u, 0x3F803F80u, 0x3F803F80u, 0x3F803F80u};
  auto cs_rd = [&](unsigned s) {
    const lp a = LP(LB + s + ca);
#pragma unroll
    for (int t = 0; t < NWP; ++t) { if constexpr (SSM) fc[t] = l128(a + t * 2048); else fc[t] = cat(ltr(a + t * 32), ltr(a + t * 32 + 2560)); }
    SB0();
  };
  auto cs_mm = [&]() {
    SB0();
    __builtin_amdgcn_s_setprio(2);
#pragma unroll
    for (int t = 0; t < NWP; ++t) acs[t] = mfma16(fc[t], ones, acs[t]);
    __builtin_amdgcn_s_setprio(0);
    SB0();
  };
  cs_rd(2 * STG);
  float br = 0.f, bi = 0.f;
  if (SSM && h == 2) {
    const float dt = rbf(expf(bf2f(pl))), ex = expf(dt * bf2f(pr));
    float sn, cn;
    sincosf(dt * bf2f(pi), &sn, &cn);
    br = ex * cn; bi = ex * sn;
  }
  cs_mm();
  WAITV(SSM ? 0 : 15); BAR();
  cs_rd(0);
  if (SSM && h == 2) {
    const float lr = bf2f(pr), li = bf2f(pi), nr = br - 1.f, ni = bi, den = lr * lr + li * li;
    LAS float* cst = (LAS float*)LP(L_CST);
    cst[4 * 128 + lane] = 2.f * (nr * lr + ni * li) / den; cst[4 * 128 + 64 + lane] = 2.f * (ni * lr - nr * li) / den;
    cst[lane] = br; cst[64 + lane] = bi;
#pragma unroll
    for (int s = 1; s <= 8; ++s) {
      const float a = br * br - bi * bi, b = 2.f * br * bi;
      br = a; bi = b;
      const int k = s == 3 ? 1 : s == 6 ? 2 : s == 8 ? 3 : 0;
      if (k) { cst[k * 128 + lane] = br; cst[k * 128 + 64 + lane] = bi; }
    }
  }
  cs_mm();
  if (c == 0) {
#pragma unroll
    for (int t = 0; t < NWP; ++t) {
      const int col = SSM ? 32 * (t >> 1) + 8 * q + 4 * (t & 1) : 16 * t + 4 * q;
      sf4(LP(L_CS + ((h & 1) * 160 + (h >> 1) * 80 + col) * 4), acs[t]);
    }
  }
  sig(L_CSF + h * 4, 1);
}

DI void carries(const Par& p, const Ids& id, const int h, const int lane, const unsigned tag, WRing& wr) {
  const __amdgpu_buffer_rsrc_t rctl = mkrs(p.ws, WS_CTL_BYTES);
  const int c = lane & 15, q = lane >> 4;
  {
    const int ch = 16 * h + c;
    lwait<2>(L_CSF + 8, 0, 1);
    LAS float* cst = (LAS float*)LP(L_CST);
    const float l8r = cst[128 + ch], l8i = cst[192 + ch], l64r = cst[256 + ch], l64i = cst[320 + ch], lwr = cst[384 + ch], lwi = cst[448 + ch];
    const float kr = cst[512 + ch], ki = cst[576 + ch];
    float w0r = 1.f, w0i = 0.f, w1r = 1.f, w1i = 0.f, qr = kr, qi = ki;
#pragma unroll
    for (int s = 0; s < 6; ++s) {
      float a = w0r, b = w0i; cm1(a, b, lwr, lwi, 0.f, 0.f);
      if (s < id.R - 1 - q) { w0r = a; w0i = b; }
      if (s < id.R - 5 - q) { w1r = a; w1i = b; }
    }
#pragma unroll
    for (int s = 0; s < 3; ++s) { float a = qr, b = qi; cm1(a, b, l64r, l64i, 0.f, 0.f); if (s < q) { qr = a; qi = b; } }
    const __amdgpu_buffer_rsrc_t rag = mkrs(p.ws + WS_AGG + id.R * (8 * 640 * 16), id.R * 640 * 16);
    LAS float* pe = (LAS float*)LP(L_E + 8 * q * ESTR + ch * 4);
    asm volatile("" : "+v"(w0r), "+v"(w0i), "+v"(w1r), "+v"(w1i), "+v"(qr), "+v"(qi));
    unsigned ao[2], po = (q * 640 + 64 * id.g + ch) * 16;
#pragma unroll
    for (int z = 0; z < 2; ++z) ao[z] = q + 4 * z > id.R ? WS_AGG + (((q + 4 * z) * 8 + id.R) * 640 + 64 * id.g + ch) * 16 : OOB;
    asm volatile("" : "+v"(ao[0]), "+v"(ao[1]), "+v"(po));
    __builtin_amdgcn_s_setprio(3);
    HBAR();
    float er[8], ei[8];
#pragma unroll
    for (int s = 0; s < 8; ++s) { er[s] = pe[s * (ESTR / 4)]; ei[s] = pe[s * (ESTR / 4) + 64]; }
    float a = er[0], b = ei[0];
#pragma unroll
    for (int s = 1; s < 8; ++s) cm1(a, b, l8r, l8i, er[s], ei[s]);
    float ta[4], tb[4];
    gather4(a, ta); gather4(b, tb);
    float pa = 0.f, pb = 0.f;
    a = ta[0]; b = tb[0];
#pragma unroll
    for (int s = 1; s < 4; ++s) {
      if (q == s) { pa = a; pb = b; }
      cm1(a, b, l64r, l64i, ta[s], tb[s]);
    }
#pragma unroll
    for (int z = 0; z < 2; ++z)
      st16c(v4u{__float_as_uint(a), tag, __float_as_uint(b), tag}, rctl, ao[z]);
    SB0();
    wr.dma01();
    v4u p0 = ld16c(rag, po, 0), p1 = ld16c(rag, po, 4 * 640 * 16);
    SB0();
    cm1(pa, pb, kr, ki, 0.f, 0.f);
#pragma unroll
    for (int s = 0; s < 7; ++s) cm1(er[s], ei[s], kr, ki, 0.f, 0.f);
    _Pragma("unroll 1") for (int spin = 0; spin < SPIN; ++spin) { FENCE();
      const bool ok = ((q >= id.R) | ((p0.y == tag) & (p0.w == tag))) & ((q + 4 >= id.R) | ((p1.y == tag) & (p1.w == tag)));
      if (__all(ok)) break;
      p0 = ld16c(rag, po, 0); p1 = ld16c(rag, po, 4 * 640 * 16);
    }
    float ca = __uint_as_float(p1.x), cb = __uint_as_float(p1.z);
    cm1(ca, cb, w1r, w1i, 0.f, 0.f);
    { float x = __uint_as_float(p0.x), y = __uint_as_float(p0.z); cm1(x, y, w0r, w0i, ca, cb); ca = x; cb = y; }
    gather4(ca, ta); gather4(cb, tb);
    ca = (ta[0] + ta[1]) + (ta[2] + ta[3]); cb = (tb[0] + tb[1]) + (tb[2] + tb[3]);
    cm1(ca, cb, qr, qi, pa, pb);
#pragma unroll
    for (int s = 0; s < 8; ++s) {
      pe[s * (ESTR / 4)] = ca; pe[s * (ESTR / 4) + 64] = -cb;
      if (s < 7) cm1(ca, cb, l8r, l8i, er[s], ei[s]);
    }
    __builtin_amdgcn_s_setprio(0);
    HBAR();
  }
}

__global__ __launch_bounds__(512, 2) void fused(Par p) {
  const int bid = blockIdx.x, xcd = bid & 7, idx = bid >> 3, a = xcd >> 2, b = xcd & 3, s = idx >> 2;
  const int wv = rfl(threadIdx.x >> 6), lane = threadIdx.x & 63;
  Ids id;
  const bool ssm = idx < 10;
  const int part = s == 2 ? 1 : 0;
  id.R = 4 * a + (idx & 1) + 2 * ((part ? b : idx >> 1) & 1);
  const int gl = (9 - idx) >> 31;
  id.g = ((2 - 3 * gl) * b + s + 3 * gl) * (1 - part) + (8 - 12 * gl + (b >> 1)) * part;
  id.cg = id.g - 10 * gl;
  asm volatile("" ::"s"(p.x), "s"(p.ws), "s"(p.wg), "s"(p.wd), "s"(id.cg), "s"(id.R));
  RowIn rin;
  if (wv >= 4) {
    const int h = wv - 4;
    const unsigned v0 = (8 * (lane >> 3)) * 1280 + (((lane & 7) ^ (lane >> 4)) * 16);
    asm volatile("s_nop 4");
    dma_x(MkSeq<8>::T{}, mkrs(p.x, L_ * D_ * 2), LB + h * 8192, v0, v0 ^ 64, (256 * id.R + 128 * (h >> 1) + 4 * (h & 1)) * 1280);
  }
  unsigned cpf = 0;
  if (wv == 3) {
    unsigned long long pc; unsigned len;
    asm volatile("s_getpc_b64 %0\n.Lpf_here:\n\ts_mov_b32 %1, .Lpf_end-.Lpf_here" : "=s"(pc), "=s"(len));
    cpf = __builtin_amdgcn_raw_buffer_load_b32(mkrs((const void*)pc, len), lane < 13 ? (idx * 13 + lane) * 128 : OOB, 0, 0);
  }
  id.rt = 16 * a + 8 * (b >> 1) + (idx >> 2);
  id.ct = 4 * (b & 1) + (idx & 3);
  const __amdgpu_buffer_rsrc_t rctl = mkrs(p.ws, WS_CTL_BYTES);
  const __amdgpu_buffer_rsrc_t rw = mkrs(p.ws + WS_W2 + (unsigned)id.ct * (54 * 5120), 54 * 5120);
  const unsigned vl = lane * 16;
  const unsigned fo = WS_FLH + (id.rt * 8 + id.ct) * 128 + (lane & 31) * 4;
  WRing wr;
  wr.rw = rw; wr.vl = vl; wr.wv = wv;
  wr.rcr = mkrs(p.cre + id.ct * 80 * P_, 80 * P_ * 2); wr.rci = mkrs(p.cim + id.ct * 80 * P_, 80 * P_ * 2);
  wr.vc = (lane >> 2) * 1280 + (((lane & 3) ^ cswz(lane >> 4)) * 16);
  unsigned tag;
  if (wv < 4) {
    row_load(p, id, wv, lane, rin);
    if (wv == 2) cpf = ld4c(rctl, lane < 32 ? fo : lane < 40 ? WS_FLW + (lane - 32) * 128 : OOB);
    tag = ssm ? compute_wave<true>(p, id, wv, lane, rin) : compute_wave<false>(p, id, wv, lane, rin);
    WAITV(0);
    unsigned old = 0;
    if (lane == 0) old = __hip_atomic_fetch_add((LAS unsigned*)LP(L_CNT), 1u, __ATOMIC_RELAXED, __HIP_MEMORY_SCOPE_WORKGROUP);
    st4c(tag, rctl, (lane < 32 && rfl(old) == 3) ? WS_FLH + (id.R * 32 + lane) * 128 + id.cg * 4 : OOB);
    asm volatile("" ::"v"(cpf));
#pragma unroll
    for (int it = 0; it < DW; ++it) wr.load(it);
  } else {
    HState hs;
    tag = ssm ? helper_wave<true>(p, id, wv - 4, lane, hs) : helper_wave<false>(p, id, wv - 4, lane, hs);
    if (ssm) {
      helper_tail<true>(p, id, wv - 4, lane, tag, hs);
      _Pragma("unroll 1") for (int spin = 0; spin < SPIN; ++spin) { FENCE();
        const v4u f2 = ld16c(rctl, WS_FLW + lane * 16, 0);
        if (__all((f2.x == tag) & (f2.y == tag) & (f2.z == tag) & (f2.w == tag))) break;
        __builtin_amdgcn_s_sleep(4);
      }
    }
    wr.load(0); wr.load(1); wr.load(2);
    if (!ssm) helper_tail<false>(p, id, wv - 4, lane, tag, hs);
#pragma unroll
    for (int it = 3; it < DW; ++it) wr.load(it);
    if (ssm) carries(p, id, wv - 4, lane, tag, wr); else wr.dma01();
    lwait<2>(L_CNT, 0, 4);
    if (wv == 4) {
      _Pragma("unroll 1") for (int spin = 0; spin < SPIN; ++spin) { FENCE();
        if (__all((ld4c(rctl, fo) == tag) | ((lane & 31) < 10))) break;
        __builtin_amdgcn_s_sleep(2);
      }
      sig(L_GO, 1);
    }
    wr.dma2();
  }

  const __amdgpu_buffer_rsrc_t rh = mkrs(p.ws + WS_H + (unsigned)id.rt * (NKB * 4096), NKB * 4096);
  v4u hb[NIT][4];
  auto loadh = [&](int it) {
    const unsigned so = it < 6 ? (8 * it + wv) * 4096 : it == 6 ? (wv < 6 ? (48 + wv) * 4096 : OOB) : (8 * it - 2 + wv) * 4096;
#pragma unroll
    for (int j = 0; j < 4; ++j) { hb[it][j] = ld16c(rh, vl + j * 1024, so); SB0(); }
  };
  lwait<1>(L_GO, 0, 1);
  SB0();
#pragma unroll
  for (int it = 0; it < DH; ++it) { loadh(it); SB0(); }
  f32x4 acc[4][5];
#pragma unroll
  for (int j = 0; j < 4; ++j)
#pragma unroll
    for (int i = 0; i < 5; ++i) acc[j][i] = f32x4{0.f, 0.f, 0.f, 0.f};
  const int tid = wv * 64 + lane;
  v2u xv[3], dv[3]; v4u sv[3]; int oo[3], ra[3];
  unsigned sfl = 0;
  const unsigned cr = wv * 5120 + (lane & 15) * 64 + (((lane >> 4) ^ cswz((lane >> 2) & 3)) * 16);
  const lp pw = LP(L_W + cr), pw2 = LP(cr);
  v4u wl[5][5];
  SB0();
#pragma unroll
  for (int it = 0; it < NIT; ++it) {
    if (it + 1 >= NG && it + 1 < NIT) {
      const int nr = it + 1 - NG;
      if (nr == 4) WAITV(LASTW);
#pragma unroll
      for (int i = 0; i < 5; ++i) wl[nr][i] = l128(nr == 2 ? pw2 + 40960 + i * 1024 : nr == 3 ? pw2 + i * 1024 : pw + (nr & 1) * 40960 + i * 1024);
      SB0();
    }
#pragma unroll
    for (int j = 0; j < 4; ++j)
#pragma unroll
      for (int i = 0; i < 5; ++i) acc[j][i] = mfma16(it >= NG ? wl[it >= NG ? it - NG : 0][i] : wr.wb[it % DW][i], hb[it][j], acc[j][i]);
    SB0();
    const int h0 = hnext(it - 1), h1 = hnext(it);
    if (h0 <= NG && NG < h1) {
      if (__builtin_expect(!__all(sfl == tag), 0)) {
        _Pragma("unroll 1") for (int spin = 0; spin < SPIN; ++spin) { FENCE();
          if (__all(ld4c(rctl, fo) == tag)) break;
          __builtin_amdgcn_s_sleep(2);
        }
      }
      FENCE();
    }
    if (hnext(it + 1) <= NG && NG < hnext(it + 2)) sfl = ld4c(rctl, fo);
    if (it == 2) { BAR(); wr.refill(0); SB0(); }
    if (it == NG - 1) { asm volatile("s_waitcnt lgkmcnt(0)" ::: "memory"); wr.refill(1); SB0(); }
#pragma unroll
    for (int z = h0; z < h1; ++z) loadh(z);
    if (it + DW < NG) wr.load(it + DW);
    if (it == NG - 1) {
#pragma unroll
      for (int r = 0; r < 3; ++r) {
        const int sl = min(tid + 512 * r, 1279), row = sl / 20, qd = sl - 20 * row;
        const int tok = 128 * (id.rt >> 1) + 8 * (row >> 2) + 4 * (id.rt & 1) + (row & 3), d = 80 * id.ct + 4 * qd;
        oo[r] = tok * D_ + d;
        ra[r] = (row & 3) * 40960 + (qd >> 2) * 1024 + (qd & 3) * 256 + (((row >> 2) + qd) & 15) * 16;
        xv[r] = *(const v2u*)(p.x + oo[r]); dv[r] = *(const v2u*)(p.dsk + d);
        sv[r] = ld16c(rctl, WS_STATS + tok * 16, 0);
      }
    }
    SB0();
  }
  asm volatile("" ::"v"(sv[0]), "v"(sv[1]), "v"(sv[2]), "v"(xv[2]), "v"(dv[2]));
#pragma unroll
  for (int i = 0; i < 5; ++i) {
    const lp rb = LP(wv * 5120 + i * 1024 + (lane >> 4) * 256 + (((lane & 15) + 4 * i + (lane >> 4)) & 15) * 16);
    sf4(rb, acc[0][i]); sf4(rb + 40960, acc[1][i]);
    const lp rc = rb + 81920;
    sf4(rc, acc[2][i]); sf4(rc + 40960, acc[3][i]);
  }
  BAR();
  asm volatile(".Lpf_end:");
#pragma unroll
  for (int r = 0; r < 3; ++r) {
    if (r < 2 || wv < 4) {
      const lp q0 = LP(ra[r]);
      f32x4 sum = ((lf4(q0) + lf4(q0 + 5120)) + (lf4(q0 + 10240) + lf4(q0 + 15360))) + ((lf4(q0 + 20480) + lf4(q0 + 25600)) + (lf4(q0 + 30720) + lf4(q0 + 35840)));
      const f32x4 xf = {bflo(xv[r].x), bfhi(xv[r].x), bflo(xv[r].y), bfhi(xv[r].y)};
      const f32x4 df = {bflo(dv[r].x), bfhi(dv[r].x), bflo(dv[r].y), bfhi(dv[r].y)};
      sum += xf + (xf - __uint_as_float(sv[r].x)) * __uint_as_float(sv[r].z) * df;
      __builtin_amdgcn_raw_buffer_store_b128(__builtin_bit_cast(v4u, sum), mkrs(p.out, L_ * D_ * 4), oo[r] * 4, 0, 16);
    }
  }
}

extern "C" void kernel_launch(void* const* d_in, const int* in_sizes, int n_in, void* d_out, int out_size, void* d_ws, size_t ws_size, hipStream_t stream) {
  Par p;
  p.x = (const u16*)d_in[0]; p.lre = (const u16*)d_in[1]; p.lim = (const u16*)d_in[2];
  p.bre = (const u16*)d_in[3]; p.bim = (const u16*)d_in[4]; p.cre = (const u16*)d_in[5]; p.cim = (const u16*)d_in[6];
  p.ldt = (const u16*)d_in[7]; p.dsk = (const u16*)d_in[8];
  p.wg = (const u16*)d_in[10]; p.wu = (const u16*)d_in[11]; p.wd = (const u16*)d_in[12];
  p.ws = (unsigned char*)d_ws; p.out = (float*)d_out;
  (void)hipFuncSetAttribute((const void*)fused, hipFuncAttributeMaxDynamicSharedMemorySize, LDS1);
  fused<<<256, 512, LDS1, stream>>>(p);
}
```

```cpp
#include <hip/hip_runtime.h>
#include <stdint.h>

typedef unsigned short u16;
typedef unsigned v4u __attribute__((ext_vector_type(4)));
typedef unsigned v2u __attribute__((ext_vector_type(2)));
typedef float f32x4 __attribute__((ext_vector_type(4)));
typedef float f32x2 __attribute__((ext_vector_type(2)));
typedef float f32x16 __attribute__((ext_vector_type(16)));
typedef short bf16x8 __attribute__((ext_vector_type(8)));
typedef short s16x4 __attribute__((ext_vector_type(4)));
typedef __bf16 bf2 __attribute__((ext_vector_type(2)));
#define LAS __attribute__((address_space(3)))
#define DI __device__ __forceinline__
typedef LAS unsigned char* lp;

constexpr int L_ = 2048, D_ = 640, P_ = 640, F_ = 1728;
constexpr int NKB = 94;
constexpr unsigned WS_EPOCH = 0;
constexpr unsigned WS_FLW = 2048;
constexpr unsigned WS_FLH = 4096;
constexpr unsigned WS_AGG = WS_FLH + 32768;
constexpr unsigned WS_STATS = WS_AGG + 64 * 640 * 16;
constexpr unsigned WS_CTL_BYTES = WS_STATS + 32768;
constexpr unsigned WS_W2 = 1u << 20;
constexpr unsigned WS_W2_BYTES = 8 * 54 * 5120;
constexpr unsigned WS_H = 8u << 20;

constexpr int UPO = 10368;
constexpr int STG = 32768 + 20480 + 128;
constexpr int LDS1 = 163840;
constexpr int L_STATS = 0;
constexpr int L_CS = L_STATS + 2048;
constexpr int L_CNT = L_CS + 1280, L_GO = L_CNT + 4, L_CSF = L_CNT + 16;
constexpr int LB = LDS1 - 3 * STG;
constexpr int L_CST = LB + 49152;
constexpr int L_W = 81920, L_W2 = 40960;
constexpr int L_E = LB + STG, ESTR = 528;
constexpr int SPIN = 1 << 18;
constexpr unsigned OOB = 0x40000000u;

DI float bf2f(unsigned b) { return __uint_as_float(b << 16); }
DI float bflo(unsigned w) { return __uint_as_float(w << 16); }
DI float bfhi(unsigned w) { return __uint_as_float(w & 0xffff0000u); }
DI unsigned pk2(float a, float b) { f32x2 v = {a, b}; return __builtin_bit_cast(unsigned, __builtin_convertvector(v, bf2)); }
DI float rbf(float a) { return bflo(pk2(a, a) & 0xffffu); }
DI f32x4 mfma16(v4u a, v4u b, f32x4 c) { return __builtin_amdgcn_mfma_f32_16x16x32_bf16(__builtin_bit_cast(bf16x8, a), __builtin_bit_cast(bf16x8, b), c, 0, 0, 0); }
DI f32x16 mfma32(v4u a, v4u b, f32x16 c) { return __builtin_amdgcn_mfma_f32_32x32x16_bf16(__builtin_bit_cast(bf16x8, a), __builtin_bit_cast(bf16x8, b), c, 0, 0, 0); }
DI unsigned rfl(unsigned v) { return __builtin_amdgcn_readfirstlane(v); }
DI __amdgpu_buffer_rsrc_t mkrs(const void* p, unsigned bytes) { return __builtin_amdgcn_make_buffer_rsrc((void*)p, 0, bytes, 0x00020000); }
template <int LI, int SI> DI void glds(__amdgpu_buffer_rsrc_t rs, unsigned lb, unsigned voff, unsigned sb) {
  unsigned t;
  asm volatile("s_add_u32 m0, %1, %5\n\ts_add_u32 %0, %4, %6\n\tbuffer_load_dwordx4 %2, %3, %0 offen lds" : "=&s"(t) : "s"(lb), "v"(voff), "s"(rs), "s"(sb), "i"(LI), "i"(SI) : "scc");
}
template <int... I> struct Seq {};
template <int N, int... I> struct MkSeq : MkSeq<N - 1, N - 1, I...> {};
template <int... I> struct MkSeq<0, I...> { typedef Seq<I...> T; };
template <int V> struct IC { static constexpr int value = V; };
template <int... I, class F> DI void for_seq(Seq<I...>, F& f) { (f(IC<I>{}), ...); }
template <int... I> DI void dma_x(Seq<I...>, __amdgpu_buffer_rsrc_t rs, unsigned lb, unsigned v0, unsigned v1, unsigned sb) { (glds<I * 1024, (64 * (I & 1) + (I >> 1)) * 1280>(rs, lb, (I & 1) ? v1 : v0, sb), ...); }
template <int... I> DI void dma_b(Seq<I...>, __amdgpu_buffer_rsrc_t rs, unsigned lb, const unsigned* v, unsigned sb) { (glds<I * 1024, (16 * (I & 1) + 4 * (I >> 1)) * 1280>(rs, lb, v[I & 1], sb), ...); }
template <int... I> DI void dma_g(Seq<I...>, __amdgpu_buffer_rsrc_t rs, unsigned lb, const unsigned* v, unsigned sb) { (glds<I * 1024, 0>(rs, lb, v[I], sb), ...); }
DI lp LP(unsigned a) { return (lp)(uintptr_t)a; }
DI v4u l128(lp a) { return *(LAS v4u*)a; }
DI f32x4 lf4(lp a) { return *(LAS f32x4*)a; }
DI void sf4(lp a, f32x4 v) { *(LAS f32x4*)a = v; }
DI s16x4 ltr(lp a) { return __builtin_amdgcn_ds_read_tr16_b64_v4i16((LAS s16x4*)a); }
DI v4u cat(s16x4 lo, s16x4 hi) { return __builtin_bit_cast(v4u, __builtin_shufflevector(lo, hi, 0, 1, 2, 3, 4, 5, 6, 7)); }
DI v4u ld16(__amdgpu_buffer_rsrc_t r, unsigned vo, unsigned so) { return __builtin_amdgcn_raw_buffer_load_b128(r, vo, so, 0); }
DI v4u ld16c(__amdgpu_buffer_rsrc_t r, unsigned vo, unsigned so) { return __builtin_amdgcn_raw_buffer_load_b128(r, vo, so, 16); }
DI unsigned ld4c(__amdgpu_buffer_rsrc_t r, unsigned vo) { return __builtin_amdgcn_raw_buffer_load_b32(r, vo, 0, 16); }
DI void st16c(v4u v, __amdgpu_buffer_rsrc_t r, unsigned vo) { __builtin_amdgcn_raw_buffer_store_b128(v, r, vo, 0, 16); }
DI void st4c(unsigned v, __amdgpu_buffer_rsrc_t r, unsigned vo) { __builtin_amdgcn_raw_buffer_store_b32(v, r, vo, 0, 16); }
#define BAR() asm volatile("s_waitcnt lgkmcnt(0)\n\ts_barrier" ::: "memory")
#define SB0() __builtin_amdgcn_sched_barrier(0)
#define WAITV(n) asm volatile("s_waitcnt vmcnt(%0)" ::"i"(n) : "memory")
#define FENCE() asm volatile("" ::: "memory")
template <int C> DI float dppf(float v) { return __int_as_float(__builtin_amdgcn_update_dpp(0, __float_as_int(v), C, 0xf, 0xf, true)); }
DI float sum8(float v) {
  v += dppf<0xB1>(v); v += dppf<0x4E>(v); v += dppf<0x141>(v);
  return v;
}
DI void gather4(float x, float (&t)[4]) {
  const auto a = __builtin_amdgcn_permlane16_swap(__float_as_uint(x), __float_as_uint(x), false, false);
  const auto e = __builtin_amdgcn_permlane32_swap(a[0], a[0], false, false), o = __builtin_amdgcn_permlane32_swap(a[1], a[1], false, false);
  t[0] = __uint_as_float(e[0]); t[2] = __uint_as_float(e[1]); t[1] = __uint_as_float(o[0]); t[3] = __uint_as_float(o[1]);
}
DI void cm1(float& a, float& b, float lr, float li, float ur, float ui) {
  const float na = fmaf(lr, a, fmaf(-li, b, ur)), nb = fmaf(lr, b, fmaf(li, a, ui));
  a = na; b = nb;
}
DI f32x4 fma4(f32x4 a, f32x4 b, f32x4 c) { return __builtin_elementwise_fma(a, b, c); }
DI f32x4 sp4(float a) { return f32x4{a, a, a, a}; }
DI void cmac(f32x4& a, f32x4& b, f32x4 lr, f32x4 li, f32x4 ur, f32x4 ui) {
  const f32x4 na = fma4(lr, a, fma4(-li, b, ur)), nb = fma4(lr, b, fma4(li, a, ui));
  a = na; b = nb;
}

struct Par {
  const u16 *x, *lre, *lim, *bre, *bim, *cre, *cim, *ldt, *dsk, *wg, *wu, *wd;
  unsigned char* ws;
  float* out;
};
struct Ids { int R, g, cg, rt, ct; };


constexpr int DW = 5, DH = 2, NIT = 12, NG = 7;
#define PIN4(x) asm volatile("" : "+v"(x))
DI void sig(unsigned addr, unsigned v) {
  asm volatile("s_mov_b64 exec, 1\n\tds_write_b32 %0, %1\n\ts_mov_b64 exec, -1" ::"v"(addr), "v"(v) : "memory");
}
DI unsigned lrd(unsigned addr) { return *(volatile LAS unsigned*)LP(addr); }
template <int SL = 1> DI void lwait(unsigned addr, unsigned v, unsigned need) {
  if (__builtin_expect(!__all(v >= need), 0)) {
    _Pragma("unroll 1") for (int spin = 0; spin < 64 * SPIN; ++spin) { FENCE();
      if (__all(lrd(addr) >= need)) break;
      __builtin_amdgcn_s_sleep(SL);
    }
  }
  FENCE();
}

template <int... I> DI void dma_c2(Seq<I...>, __amdgpu_buffer_rsrc_t rs, unsigned lb, unsigned v, unsigned sb) { (glds<(I & 1) * 5120 + (I >> 1) * 1024, (I & 1) * 64 + (I >> 1) * 16 * 1280>(rs, lb, v, sb), ...); }
DI unsigned cswz(unsigned g) { return ((g << 1) & 2) ^ ((g >> 1) * 3); }
struct WRing {
  v4u wb[DW][5];
  __amdgpu_buffer_rsrc_t rw, rcr, rci;
  unsigned vl, vc, wv;
  DI void load(int it) {
#pragma unroll
    for (int i = 0; i < 5; ++i) { wb[it % DW][i] = ld16c(rw, vl + (i & 3) * 1024, (8 * it + wv) * 5120 + (i >> 2) * 4096); SB0(); }
  }
  DI void dma01() {
    asm volatile("s_nop 4");
    dma_c2(MkSeq<10>::T{}, rcr, L_W + (wv - 4) * 20480, vc, (wv - 4) * 256);
    dma_c2(MkSeq<10>::T{}, rcr, L_W + (wv - 4) * 20480 + 10240, vc, (wv - 4) * 256 + 128);
  }
  DI void dma2() {
    asm volatile("s_nop 4");
    dma_c2(MkSeq<10>::T{}, wv < 6 ? rcr : rci, L_W2 + (wv - 4) * 10240, vc, wv < 6 ? 1024 + (wv - 4) * 128 : (wv - 6) * 128);
  }
  template <int R, int I> DI void refill() {
    glds<R * L_W + I * 1024, (4 + 8 * R) * 64 + I * 16 * 1280>(rci, wv * 5120, vc, wv * 64);
  }
  DI void load1(int it, int i) { wb[it % DW][i] = ld16c(rw, vl + (i & 3) * 1024, (8 * it + wv) * 5120 + (i >> 2) * 4096); }
};

DI float wsum(float v) {
  v = sum8(v); v += dppf<0x128>(v);
  float t[4]; gather4(v, t);
  return (t[0] + t[1]) + (t[2] + t[3]);
}
DI unsigned mktag(unsigned epoch) { return rfl(epoch) * 0x9E3779B1u + 0x7F4A7C15u; }
struct RowIn { v4u xa[2]; unsigned xb[2], epoch; };
DI void row_load(const Par& p, const Ids& id, const int w, const int lane, RowIn& in) {
  const int row = 256 * id.R + 8 * id.cg + 2 * w;
#pragma unroll
  for (int z = 0; z < 2; ++z) { in.xa[z] = *(const v4u*)(p.x + (row + z) * D_ + lane * 8); in.xb[z] = *(const unsigned*)(p.x + (row + z) * D_ + 512 + lane * 2); }
  in.epoch = ld4c(mkrs(p.ws, WS_CTL_BYTES), WS_EPOCH);
  SB0();
}
DI unsigned row_stats(const Par& p, const Ids& id, const int w, const int lane, const RowIn& in) {
  const __amdgpu_buffer_rsrc_t rctl = mkrs(p.ws, WS_CTL_BYTES);
  const int row = 256 * id.R + 8 * id.cg + 2 * w;
  const v4u* xa = in.xa; const unsigned* xb = in.xb; const unsigned epoch = in.epoch;
  float v[2][10], mean[2], s2[2];
#pragma unroll
  for (int z = 0; z < 2; ++z) {
    const float t[10] = {bflo(xa[z].x), bfhi(xa[z].x), bflo(xa[z].y), bfhi(xa[z].y), bflo(xa[z].z), bfhi(xa[z].z), bflo(xa[z].w), bfhi(xa[z].w), bflo(xb[z]), bfhi(xb[z])};
#pragma unroll
    for (int e = 0; e < 10; ++e) v[z][e] = t[e];
    mean[z] = wsum(((t[0] + t[1]) + (t[2] + t[3])) + ((t[4] + t[5]) + (t[6] + t[7])) + (t[8] + t[9])) * (1.f / D_);
  }
#pragma unroll
  for (int z = 0; z < 2; ++z) {
    float a = 0.f, b = 0.f;
#pragma unroll
    for (int e = 0; e < 5; ++e) { a = fmaf(v[z][e] - mean[z], v[z][e] - mean[z], a); b = fmaf(v[z][5 + e] - mean[z], v[z][5 + e] - mean[z], b); }
    s2[z] = wsum(a + b);
  }
  const unsigned tag = mktag(epoch);
  const float m = rbf((lane & 1) ? mean[1] : mean[0]), rs = 1.f / rbf(sqrtf(rbf(rbf(((lane & 1) ? s2[1] : s2[0]) * (1.f / D_)) + 1e-5f)));
  st16c(v4u{__float_as_uint(m), tag, __float_as_uint(rs), tag}, rctl, lane < 2 ? WS_STATS + (row + lane) * 16 : OOB);
  return tag;
}

template <bool SSM>
DI unsigned compute_wave(const Par& p, const Ids& id, const int w, const int lane, const RowIn& rin) {
  unsigned tag, cf = 0;
  if constexpr (!SSM) {
    const int r = lane & 31, hh = lane >> 5;
    const unsigned xa = (64 * w + r) * 128 + ((hh ^ ((r >> 1) & 7)) * 16);
    const unsigned wa = 32768 + ((lane >> 4) & 1) * UPO + (8 * hh + ((lane & 15) >> 2)) * 160 + (lane & 3) * 8;
    f32x16 acc[5][2] = {};
#pragma unroll
    for (int e = 0; e < 5; ++e) { PIN4(acc[e][0]); PIN4(acc[e][1]); }
    v4u fx[2][2]; s16x4 fl[4], fh[4];
    lp px[4], pw;
    auto setp = [&](unsigned sb) {
#pragma unroll
      for (int s = 0; s < 4; ++s) px[s] = LP(LB + sb + (xa ^ (32 * s)));
      pw = LP(LB + sb + wa);
    };
    auto woff = [&](int n) { return (n / 5) * 2560 + (n % 5) * 32; };
    setp(0);
    SB0();
    tag = row_stats(p, id, w, lane, rin);
    SB0();
    BAR();
    fx[0][0] = l128(px[0]); fx[0][1] = l128(px[0] + 4096);
#pragma unroll
    for (int n = 0; n < 3; ++n) { fl[n] = ltr(pw + woff(n)); fh[n] = ltr(pw + woff(n) + 640); }
    unsigned sb = 0;
#pragma unroll 1
    for (int i = 0; i < 10; ++i) {
#pragma unroll
      for (int n = 0; n < 20; ++n) {
        const int ks = n / 5, e = n % 5, nn = (n + 3) % 20;
        if (n == 17) { if (i < 9) BAR(); sb = sb == 2 * STG ? 0 : sb + STG; setp(sb); SB0(); }
        const v4u wf = cat(fl[n & 3], fh[n & 3]);
        acc[e][0] = mfma32(wf, fx[ks & 1][0], acc[e][0]); SB0();
        fl[(n + 3) & 3] = ltr(pw + woff(nn));
        fh[(n + 3) & 3] = ltr(pw + woff(nn) + 640); SB0();
        acc[e][1] = mfma32(wf, fx[ks & 1][1], acc[e][1]); SB0();
        if (ks < 3 && e == 1) { fx[(ks + 1) & 1][0] = l128(px[ks + 1]); fx[(ks + 1) & 1][1] = l128(px[ks + 1] + 4096); }
        if (n == 17) { fx[0][0] = l128(px[0]); fx[0][1] = l128(px[0] + 4096); }
        if (n == 18) cf = lrd(L_CSF + (lane & 3) * 4);
        SB0();
      }
    }
    lwait(L_CSF + (lane & 3) * 4, cf, 1);
    const __amdgpu_buffer_rsrc_t rh = mkrs(p.ws + WS_H + (unsigned)(4 * id.R + w) * (NKB * 4096), 54 * 4096);
    const unsigned so0 = (r >> 4) * 1024 + (hh * 16 + (r & 15)) * 16;
    float mu[2], r2[2];
#pragma unroll
    for (int u = 0; u < 2; ++u) {
      const int F = 4 * w + 2 * u + (r >> 4);
      const f32x2 s = *(LAS f32x2*)LP(L_STATS + (128 * (F >> 3) + 8 * (r & 15) + (F & 7)) * 8);
      mu[u] = s.x; r2[u] = s.y * s.y;
    }
    const lp pc = LP(L_CS + hh * 16);
#pragma unroll
    for (int e = 0; e < 5; ++e) {
      const f32x4 g0 = lf4(pc + e * 64) + lf4(pc + e * 64 + 640), g1 = lf4(pc + e * 64 + 32) + lf4(pc + e * 64 + 672);
      const f32x4 u0 = lf4(pc + e * 64 + 320) + lf4(pc + e * 64 + 960), u1 = lf4(pc + e * 64 + 352) + lf4(pc + e * 64 + 992);
      const int T = 5 * id.g + e;
      const unsigned so = so0 + (T >> 1) * 4096 + (T & 1) * 512;
#pragma unroll
      for (int u = 0; u < 2; ++u) {
        const f32x16 a = acc[e][u];
        const f32x4 nm = sp4(-mu[u]);
        const f32x4 h0 = fma4(nm, g0, f32x4{a[0], a[1], a[2], a[3]}) * fma4(nm, u0, f32x4{a[8], a[9], a[10], a[11]}) * r2[u];
        const f32x4 h1 = fma4(nm, g1, f32x4{a[4], a[5], a[6], a[7]}) * fma4(nm, u1, f32x4{a[12], a[13], a[14], a[15]}) * r2[u];
        st16c(v4u{pk2(h0.x, h0.y), pk2(h0.z, h0.w), pk2(h1.x, h1.y), pk2(h1.z, h1.w)}, rh, so + u * 2048);
      }
    }
  } else {
    const int c = lane & 15, q = lane >> 4, G = w >> 1, blk = w & 1;
    const unsigned xa = (128 * G + c) * 128 + ((q ^ (c >> 1)) * 16);
    const unsigned wa = 32768 + (32 * blk + c) * 128 + ((q ^ (c >> 1)) * 16);
    f32x4 acc[8][4] = {};
#pragma unroll
    for (int j = 0; j < 8; ++j)
#pragma unroll
      for (int t = 0; t < 4; ++t) PIN4(acc[j][t]);
    v4u fw[2][4], fx[4];
    lp px[2], pw[2];
    auto setp = [&](unsigned sb) { px[0] = LP(LB + sb + xa); px[1] = LP(LB + sb + (xa ^ 64)); pw[0] = LP(LB + sb + wa); pw[1] = LP(LB + sb + (wa ^ 64)); };
    auto woff = [&](int t) { return (t >> 1) * 8192 + (t & 1) * 2048; };
    setp(0);
    SB0();
    tag = row_stats(p, id, w, lane, rin);
    SB0();
    BAR();
#pragma unroll
    for (int t = 0; t < 4; ++t) fw[0][t] = l128(pw[0] + woff(t));
#pragma unroll
    for (int n = 0; n < 3; ++n) fx[n] = l128(px[0] + n * 2048);
    unsigned sb = 0;
#pragma unroll 1
    for (int i = 0; i < 10; ++i) {
#pragma unroll
      for (int n = 0; n < 16; ++n) {
        const int kk = n >> 3, j = n & 7, nn = (n + 3) & 15;
        if (n == 13) { if (i < 9) BAR(); sb = sb == 2 * STG ? 0 : sb + STG; setp(sb); SB0(); }
        const v4u xf = fx[n & 3];
        acc[j][0] = mfma16(fw[kk][0], xf, acc[j][0]); SB0();
        fx[(n + 3) & 3] = l128(px[nn >> 3] + (nn & 7) * 2048); SB0();
        acc[j][1] = mfma16(fw[kk][1], xf, acc[j][1]); SB0();
        if (n >= 2 && n < 6) fw[1][n - 2] = l128(pw[1] + woff(n - 2));
        if (n == 13) fw[0][0] = l128(pw[0] + woff(0));
        if (n == 14) fw[0][2] = l128(pw[0] + woff(2));
        SB0();
        acc[j][2] = mfma16(fw[kk][2], xf, acc[j][2]); SB0();
        if (n == 13) fw[0][1] = l128(pw[0] + woff(1));
        if (n == 14) fw[0][3] = l128(pw[0] + woff(3));
        SB0();
        acc[j][3] = mfma16(fw[kk][3], xf, acc[j][3]); SB0();
        if (n == 14) { cf = lrd(L_CSF + (lane & 3) * 4); SB0(); }
      }
    }
    lwait(L_CSF + (lane & 3) * 4, cf, 1);
    const lp pe = LP(L_E + (16 * G + c) * ESTR + (32 * blk + 8 * q) * 4);
    const lp pk = LP(L_CST + (32 * blk + 8 * q) * 4);
    f32x4 lr[2], li[2], er[2], ei[2];
    {
      const lp ps = LP(L_STATS + (128 * G + 8 * c) * 8), pc = LP(L_CS + (32 * blk + 8 * q) * 4);
      f32x4 cs[4], st[4];
#pragma unroll
      for (int t = 0; t < 4; ++t) { cs[t] = lf4(pc + (t >> 1) * 320 + (t & 1) * 16) + lf4(pc + 640 + (t >> 1) * 320 + (t & 1) * 16); st[t] = lf4(ps + t * 16); }
#pragma unroll
      for (int ab = 0; ab < 2; ++ab) { lr[ab] = lf4(pk + ab * 16); li[ab] = lf4(pk + 256 + ab * 16); }
#pragma unroll
      for (int jj = 0; jj < 4; ++jj)
#pragma unroll
        for (int t = 0; t < 4; ++t) {
          acc[2 * jj][t] = fma4(sp4(-st[jj].x), cs[t], acc[2 * jj][t]) * st[jj].y;
          acc[2 * jj + 1][t] = fma4(sp4(-st[jj].z), cs[t], acc[2 * jj + 1][t]) * st[jj].w;
        }
    }
#pragma unroll
    for (int ab = 0; ab < 2; ++ab) { er[ab] = acc[0][ab]; ei[ab] = acc[0][2 + ab]; }
#pragma unroll
    for (int j = 1; j < 8; ++j)
#pragma unroll
      for (int ab = 0; ab < 2; ++ab) cmac(er[ab], ei[ab], lr[ab], li[ab], acc[j][ab], acc[j][2 + ab]);
#pragma unroll
    for (int ab = 0; ab < 2; ++ab) { sf4(pe + ab * 16, er[ab]); sf4(pe + 256 + ab * 16, ei[ab]); }
    SB0();
    BAR();
#pragma unroll
    for (int ab = 0; ab < 2; ++ab) {
      const f32x4 kr = lf4(pk + 512 * 4 + ab * 16), ki = lf4(pk + 512 * 4 + 256 + ab * 16);
#pragma unroll
      for (int j = 0; j < 8; ++j) {
        const f32x4 ur = acc[j][ab], ui = acc[j][2 + ab];
        acc[j][ab] = fma4(-ki, ui, kr * ur); acc[j][2 + ab] = fma4(-ki, ur, -kr * ui);
        PIN4(acc[j][ab]); PIN4(acc[j][2 + ab]);
      }
      li[ab] = -li[ab];
    }
    const __amdgpu_buffer_rsrc_t rh = mkrs(p.ws + WS_H + (unsigned)(4 * id.R + 2 * G) * (NKB * 4096), 2 * NKB * 4096);
    unsigned vo[4];
#pragma unroll
    for (int z = 0; z < 4; ++z) { vo[z] = (54 + 2 * id.g + blk + 20 * (z & 1) + NKB * (z >> 1)) * 4096 + lane * 16; PIN4(vo[z]); }
    SB0();
    BAR();
#pragma unroll
    for (int ab = 0; ab < 2; ++ab) { er[ab] = lf4(pe + ab * 16); ei[ab] = lf4(pe + 256 + ab * 16); }
#pragma unroll
    for (int j = 0; j < 8; ++j) {
#pragma unroll
      for (int ab = 0; ab < 2; ++ab) cmac(er[ab], ei[ab], lr[ab], li[ab], acc[j][ab], acc[j][2 + ab]);
      st16c(v4u{pk2(er[0].x, er[0].y), pk2(er[0].z, er[0].w), pk2(er[1].x, er[1].y), pk2(er[1].z, er[1].w)}, rh, vo[2 * (j >> 2)] + (j & 3) * 1024);
      st16c(v4u{pk2(ei[0].x, ei[0].y), pk2(ei[0].z, ei[0].w), pk2(ei[1].x, ei[1].y), pk2(ei[1].z, ei[1].w)}, rh, vo[2 * (j >> 2) + 1] + (j & 3) * 1024);
    }
  }
  SB0();
  return tag;
}

#define HBAR() do { SB0(); BAR(); SB0(); } while (0)
struct HState { f32x4 acs[5]; unsigned pl, pr, pi; };
template <bool SSM>
DI unsigned helper_wave(const Par& p, const Ids& id, const int h, const int lane, HState& hs) {
  constexpr int NWP = SSM ? 4 : 5, NP = 8 + NWP;
  constexpr unsigned WSTEP = SSM ? 128u : 64u * 3456u;
  const int c = lane & 15, q = lane >> 4;
  const __amdgpu_buffer_rsrc_t rctl = mkrs(p.ws, WS_CTL_BYTES);
  const __amdgpu_buffer_rsrc_t rx = mkrs(p.x, L_ * D_ * 2);
  const __amdgpu_buffer_rsrc_t rw = mkrs(SSM ? ((h >> 1) ? p.bim : p.bre) : ((h >> 1) ? p.wu : p.wg), SSM ? P_ * D_ * 2 : D_ * F_ * 2);
  const unsigned v0 = (8 * (lane >> 3)) * 1280 + (((lane & 7) ^ (lane >> 4)) * 16), v1 = v0 ^ 64;
  unsigned st = 0;
  const unsigned sx = (256 * id.R + 128 * (h >> 1) + 4 * (h & 1)) * 1280;
  auto issue_x = [&](unsigned s, unsigned sb) {
    dma_x(MkSeq<8>::T{}, rx, LB + sb + h * 8192, v0, v1, s);
  };
  unsigned vw[NWP];
  if constexpr (SSM) {
    vw[0] = (8 * (lane >> 5) + ((lane >> 3) & 3)) * 1280 + (((lane & 7) ^ (lane >> 4)) * 16); vw[1] = vw[0] ^ 64;
  } else {
#pragma unroll
    for (int i = 0; i < 5; ++i) { const int lc = 64 * (5 * (h & 1) + i) + lane, pos = lc / 10; vw[i] = pos * 3456 + (lc - pos * 10) * 16; }
  }
  const unsigned sw = SSM ? (64 * id.g + 32 * (h & 1)) * 1280 : 160 * id.g;
  const unsigned dw = LB + 32768 + (h >> 1) * (SSM ? 8192 : UPO) + (h & 1) * NWP * 1024;
  auto issue_w = [&](unsigned s, unsigned sb) {
    if constexpr (SSM) dma_b(MkSeq<4>::T{}, rw, sb + dw, vw, s); else dma_g(MkSeq<5>::T{}, rw, sb + dw, vw, s);
  };
  asm volatile("s_nop 4");
  issue_w(sw, 0);
  unsigned sb = STG;
  auto step = [&]() {
    ++st;
    issue_x(sx + st * 128, sb); issue_w(sw + st * WSTEP, sb);
    sb = sb == 2 * STG ? 0 : sb + STG;
  };
  step();
  if (h == 3 && lane < 8) *(LAS unsigned*)LP(L_CNT + lane * 4) = 0u;
  f32x4 acs[NWP] = {};
  v4u fc[NWP];
  const unsigned ca = SSM ? 32768 + (h >> 1) * 8192 + c * 128 + (((4 * (h & 1) + q) ^ (c >> 1)) * 16)
                          : 32768 + (h >> 1) * UPO + (32 * (h & 1) + 4 * q + (c >> 2)) * 160 + (c & 3) * 8;
  const v4u ones = {0x3F803F80u, 0x3F803F80u, 0x3F803F80u, 0x3F803F80u};
  auto cs_rd = [&](unsigned s) {
    const lp a = LP(LB + s + ca);
#pragma unroll
    for (int t = 0; t < NWP; ++t) { if constexpr (SSM) fc[t] = l128(a + t * 2048); else fc[t] = cat(ltr(a + t * 32), ltr(a + t * 32 + 2560)); }
    SB0();
  };
  auto cs_mm = [&]() {
    SB0();
    __builtin_amdgcn_s_setprio(2);
#pragma unroll
    for (int t = 0; t < NWP; ++t) acs[t] = mfma16(fc[t], ones, acs[t]);
    __builtin_amdgcn_s_setprio(0);
    SB0();
  };
  constexpr int JL = 1;
  const int tsk = (id.g * 8 + id.R) * 4 + h, kb = tsk / 10, dq = tsk - 10 * kb;
  v4u f[4] = {}, sel[2], sv = {}, fl = {};
  unsigned fvo, fso[4], tch = 0;
  const __amdgpu_buffer_rsrc_t rf = mkrs(SSM ? ((tsk & 1) ? p.cim : p.cre) : p.wd, SSM ? D_ * P_ * 2 : F_ * D_ * 2);
  const __amdgpu_buffer_rsrc_t r2 = mkrs(p.ws + WS_W2, tsk < 540 ? WS_W2_BYTES : 0);
  if constexpr (SSM) fvo = lane < 40 ? ((tsk >> 1) * 40 + lane) * 128 : OOB;
  else {
    fvo = (32 * kb + 16 * ((lane >> 3) & 1) + 4 * (q & 1) + (q >> 1)) * 1280 + dq * 128 + (lane & 7) * 16;
#pragma unroll
    for (int i = 0; i < 4; ++i) {
      const int dt = 4 * dq + 2 * (q & 1) + (i >> 1), ct = dt / 5;
      fso[i] = ((ct * 54 + kb) * 5 + dt - 5 * ct) * 1024 + (16 * (2 * (q >> 1) + (c >> 3)) + 8 * (i & 1) + (c & 7)) * 16;
      PIN4(fso[i]);
    }
#pragma unroll
    for (int hf = 0; hf < 2; ++hf) {
#pragma unroll
      for (int e = 0; e < 4; ++e) sel[hf][e] = (8 * q + 2 * e == 16 * hf + c ? 0x3F80u : 0u) | (8 * q + 2 * e + 1 == 16 * hf + c ? 0x3F800000u : 0u);
      PIN4(sel[hf]);
    }
  }
  PIN4(fvo);
  unsigned epoch, pl = 0, pr = 0, pi = 0, tag = 0;
  unsigned so = WS_STATS + (256 * id.R + 64 * h + lane) * 16, flo = h == 3 ? WS_FLW + lane * 16 : OOB, fwo = (h == 3 && lane == 0) ? WS_FLW + blockIdx.x * 4 : OOB;
  PIN4(so); PIN4(flo); PIN4(fwo);
  asm volatile("buffer_load_dword %0, off, %1, 0 sc1" : "=v"(epoch) : "s"(rctl));
  if constexpr (SSM) {
    const unsigned po = (64 * id.g + lane) * 2;
    asm volatile("buffer_load_ushort %0, %3, %4, 0 offen\n\tbuffer_load_ushort %1, %3, %5, 0 offen\n\tbuffer_load_ushort %2, %3, %6, 0 offen"
                 : "=&v"(pl), "=&v"(pr), "=&v"(pi) : "v"(po), "s"(mkrs(p.ldt, P_ * 2)), "s"(mkrs(p.lre, P_ * 2)), "s"(mkrs(p.lim, P_ * 2)));
  }
  WAITV(NP + (SSM ? 4 : 1)); BAR();
#pragma unroll
  for (int j = 0; j < 8; ++j) {
    cs_rd((j % 3) * STG);
    if (j == JL + 4) { tag = mktag(epoch); st4c(tag, rctl, fwo); }
    if (j == 6) asm volatile("buffer_load_dwordx4 %0, %1, %2, 0 offen sc1" : "=v"(sv) : "v"(so), "s"(rctl));
    if (!SSM && j == 7) asm volatile("buffer_load_dwordx4 %0, %1, %2, 0 offen sc1" : "=v"(fl) : "v"(flo), "s"(rctl));
    SB0();
    step();
    if (SSM && j == 3) asm volatile("buffer_load_dword %0, %1, %2, 0 offen" : "=v"(tch) : "v"(fvo), "s"(rf));
    if (!SSM && j == JL)
      asm volatile("buffer_load_dwordx4 %0, %4, %5, 0 offen\n\tbuffer_load_dwordx4 %1, %4, %5, 0 offen offset:2560\n\tbuffer_load_dwordx4 %2, %4, %5, %6 offen\n\tbuffer_load_dwordx4 %3, %4, %5, %6 offen offset:2560"
                   : "=&v"(f[0]), "=&v"(f[1]), "=&v"(f[2]), "=&v"(f[3]) : "v"(fvo), "s"(rf), "s"(8 * 1280));
    cs_mm();
    if (!SSM && j == JL + 2) {
      f32x4 d[8];
      __builtin_amdgcn_s_setprio(2);
#pragma unroll
      for (int z = 0; z < 8; ++z) d[z] = mfma16(f[z >> 1], sel[z & 1], f32x4{0.f, 0.f, 0.f, 0.f});
#pragma unroll
      for (int i = 0; i < 4; ++i) st16c(v4u{pk2(d[0][i], d[1][i]), pk2(d[2][i], d[3][i]), pk2(d[4][i], d[5][i]), pk2(d[6][i], d[7][i])}, r2, fso[i]);
      __builtin_amdgcn_s_setprio(0);
      SB0();
    }
    if (!SSM && (j == JL || j == JL + 2)) WAITV(NP + 4);
    else if (SSM && j == 3) WAITV(NP + 1);
    else if (j == JL + 1) asm volatile("s_waitcnt vmcnt(%8)" : "+v"(f[0]), "+v"(f[1]), "+v"(f[2]), "+v"(f[3]), "+v"(epoch), "+v"(pl), "+v"(pr), "+v"(pi) : "i"(NP) : "memory");
    else if (j == 4) asm volatile("s_waitcnt vmcnt(%1)" : "+v"(tch) : "i"(NP) : "memory");
    else if (j == 6) asm volatile("s_waitcnt vmcnt(%1)" : "+v"(sv) : "i"(NP) : "memory");
    else if (j == 7) asm volatile("s_waitcnt vmcnt(%1)" : "+v"(fl) : "i"(NP) : "memory");
    else if (j == JL + 4) WAITV(NP + 1);
    else WAITV(NP);
    if (j == 6) {
      v2u o = {sv.x, sv.z};
      if (!__all((sv.y == tag) & (sv.w == tag))) {
        _Pragma("unroll 1") for (int spin = 0; spin < SPIN; ++spin) { FENCE();
          const v4u s2 = ld16c(rctl, so, 0);
          o = v2u{s2.x, s2.z};
          if (__all((s2.y == tag) & (s2.w == tag))) break;
          __builtin_amdgcn_s_sleep(4);
        }
      }
      *(LAS v2u*)LP(L_STATS + (64 * h + lane) * 8) = o;
    }
    if (!SSM && j == 7 && h == 3) {
      if (!__all((fl.x == tag) & (fl.y == tag) & (fl.z == tag) & (fl.w == tag))) {
        _Pragma("unroll 1") for (int spin = 0; spin < SPIN; ++spin) { FENCE();
          const v4u f2 = ld16c(rctl, flo, 0);
          if (__all((f2.x == tag) & (f2.y == tag) & (f2.z == tag) & (f2.w == tag))) break;
          __builtin_amdgcn_s_sleep(4);
        }
      }
      st4c(tag, rctl, (lane == 0 && blockIdx.x == 255) ? WS_EPOCH : OOB);
    }
    BAR();
  }
#pragma unroll
  for (int t = 0; t < 5; ++t) hs.acs[t] = acs[t < NWP ? t : 0];
  hs.pl = pl; hs.pr = pr; hs.pi = pi;
  return tag;
}
template <bool SSM>
DI void helper_tail(const Par& p, const Ids& id, const int h, const int lane, const unsigned tag, HState& hs) {
  constexpr int NWP = SSM ? 4 : 5;
  const int c = lane & 15, q = lane >> 4;
  const __amdgpu_buffer_rsrc_t rctl = mkrs(p.ws, WS_CTL_BYTES);
  f32x4* acs = hs.acs;
  const unsigned pl = hs.pl, pr = hs.pr, pi = hs.pi;
  v4u fc[NWP];
  const unsigned ca = SSM ? 32768 + (h >> 1) * 8192 + c * 128 + (((4 * (h & 1) + q) ^ (c >> 1)) * 16)
                          : 32768 + (h >> 1) * UPO + (32 * (h & 1) + 4 * q + (c >> 2)) * 160 + (c & 3) * 8;
  const v4u ones = {0x3F803F80u, 0x3F803F80u, 0x3F803F80u, 0x3F803F80u};
  auto cs_rd = [&](unsigned s) {
    const lp a = LP(LB + s + ca);
#pragma unroll
    for (int t = 0; t < NWP; ++t) { if constexpr (SSM) fc[t] = l128(a + t * 2048); else fc[t] = cat(ltr(a + t * 32), ltr(a + t * 32 + 2560)); }
    SB0();
  };
  auto cs_mm = [&]() {
    SB0();
    __builtin_amdgcn_s_setprio(2);
#pragma unroll
    for (int t = 0; t < NWP; ++t) acs[t] = mfma16(fc[t], ones, acs[t]);
    __builtin_amdgcn_s_setprio(0);
    SB0();
  };
  cs_rd(2 * STG);
  float br = 0.f, bi = 0.f;
  if (SSM && h == 2) {
    const float dt = rbf(expf(bf2f(pl))), ex = expf(dt * bf2f(pr));
    float sn, cn;
    sincosf(dt * bf2f(pi), &sn, &cn);
    br = ex * cn; bi = ex * sn;
  }
  cs_mm();
  WAITV(SSM ? 0 : 15); BAR();
  cs_rd(0);
  if (SSM && h == 2) {
    const float lr = bf2f(pr), li = bf2f(pi), nr = br - 1.f, ni = bi, den = lr * lr + li * li;
    LAS float* cst = (LAS float*)LP(L_CST);
    cst[4 * 128 + lane] = 2.f * (nr * lr + ni * li) / den; cst[4 * 128 + 64 + lane] = 2.f * (ni * lr - nr * li) / den;
    cst[lane] = br; cst[64 + lane] = bi;
#pragma unroll
    for (int s = 1; s <= 8; ++s) {
      const float a = br * br - bi * bi, b = 2.f * br * bi;
      br = a; bi = b;
      const int k = s == 3 ? 1 : s == 6 ? 2 : s == 8 ? 3 : 0;
      if (k) { cst[k * 128 + lane] = br; cst[k * 128 + 64 + lane] = bi; }
    }
  }
  cs_mm();
  if (c == 0) {
#pragma unroll
    for (int t = 0; t < NWP; ++t) {
      const int col = SSM ? 32 * (t >> 1) + 8 * q + 4 * (t & 1) : 16 * t + 4 * q;
      sf4(LP(L_CS + ((h & 1) * 160 + (h >> 1) * 80 + col) * 4), acs[t]);
    }
  }
  sig(L_CSF + h * 4, 1);
}

DI void carries(const Par& p, const Ids& id, const int h, const int lane, const unsigned tag, WRing& wr) {
  const __amdgpu_buffer_rsrc_t rctl = mkrs(p.ws, WS_CTL_BYTES);
  const int c = lane & 15, q = lane >> 4;
  {
    const int ch = 16 * h + c;
    lwait<2>(L_CSF + 8, 0, 1);
    LAS float* cst = (LAS float*)LP(L_CST);
    const float l8r = cst[128 + ch], l8i = cst[192 + ch], l64r = cst[256 + ch], l64i = cst[320 + ch], lwr = cst[384 + ch], lwi = cst[448 + ch];
    const float kr = cst[512 + ch], ki = cst[576 + ch];
    float w0r = 1.f, w0i = 0.f, w1r = 1.f, w1i = 0.f, qr = kr, qi = ki;
#pragma unroll
    for (int s = 0; s < 6; ++s) {
      float a = w0r, b = w0i; cm1(a, b, lwr, lwi, 0.f, 0.f);
      if (s < id.R - 1 - q) { w0r = a; w0i = b; }
      if (s < id.R - 5 - q) { w1r = a; w1i = b; }
    }
#pragma unroll
    for (int s = 0; s < 3; ++s) { float a = qr, b = qi; cm1(a, b, l64r, l64i, 0.f, 0.f); if (s < q) { qr = a; qi = b; } }
    const __amdgpu_buffer_rsrc_t rag = mkrs(p.ws + WS_AGG + id.R * (8 * 640 * 16), id.R * 640 * 16);
    LAS float* pe = (LAS float*)LP(L_E + 8 * q * ESTR + ch * 4);
    asm volatile("" : "+v"(w0r), "+v"(w0i), "+v"(w1r), "+v"(w1i), "+v"(qr), "+v"(qi));
    unsigned ao[2], po = (q * 640 + 64 * id.g + ch) * 16;
#pragma unroll
    for (int z = 0; z < 2; ++z) ao[z] = q + 4 * z > id.R ? WS_AGG + (((q + 4 * z) * 8 + id.R) * 640 + 64 * id.g + ch) * 16 : OOB;
    asm volatile("" : "+v"(ao[0]), "+v"(ao[1]), "+v"(po));
    __builtin_amdgcn_s_setprio(3);
    HBAR();
    float er[8], ei[8];
#pragma unroll
    for (int s = 0; s < 8; ++s) { er[s] = pe[s * (ESTR / 4)]; ei[s] = pe[s * (ESTR / 4) + 64]; }
    float a = er[0], b = ei[0];
#pragma unroll
    for (int s = 1; s < 8; ++s) cm1(a, b, l8r, l8i, er[s], ei[s]);
    float ta[4], tb[4];
    gather4(a, ta); gather4(b, tb);
    float pa = 0.f, pb = 0.f;
    a = ta[0]; b = tb[0];
#pragma unroll
    for (int s = 1; s < 4; ++s) {
      if (q == s) { pa = a; pb = b; }
      cm1(a, b, l64r, l64i, ta[s], tb[s]);
    }
#pragma unroll
    for (int z = 0; z < 2; ++z)
      st16c(v4u{__float_as_uint(a), tag, __float_as_uint(b), tag}, rctl, ao[z]);
    SB0();
    wr.dma01();
    v4u p0 = ld16c(rag, po, 0), p1 = ld16c(rag, po, 4 * 640 * 16);
    SB0();
    cm1(pa, pb, kr, ki, 0.f, 0.f);
#pragma unroll
    for (int s = 0; s < 7; ++s) cm1(er[s], ei[s], kr, ki, 0.f, 0.f);
    _Pragma("unroll 1") for (int spin = 0; spin < SPIN; ++spin) { FENCE();
      const bool ok = ((q >= id.R) | ((p0.y == tag) & (p0.w == tag))) & ((q + 4 >= id.R) | ((p1.y == tag) & (p1.w == tag)));
      if (__all(ok)) break;
      p0 = ld16c(rag, po, 0); p1 = ld16c(rag, po, 4 * 640 * 16);
    }
    float ca = __uint_as_float(p1.x), cb = __uint_as_float(p1.z);
    cm1(ca, cb, w1r, w1i, 0.f, 0.f);
    { float x = __uint_as_float(p0.x), y = __uint_as_float(p0.z); cm1(x, y, w0r, w0i, ca, cb); ca = x; cb = y; }
    gather4(ca, ta); gather4(cb, tb);
    ca = (ta[0] + ta[1]) + (ta[2] + ta[3]); cb = (tb[0] + tb[1]) + (tb[2] + tb[3]);
    cm1(ca, cb, qr, qi, pa, pb);
#pragma unroll
    for (int s = 0; s < 8; ++s) {
      pe[s * (ESTR / 4)] = ca; pe[s * (ESTR / 4) + 64] = -cb;
      if (s < 7) cm1(ca, cb, l8r, l8i, er[s], ei[s]);
    }
    __builtin_amdgcn_s_setprio(0);
    HBAR();
  }
}

__global__ __launch_bounds__(512, 2) void fused(Par p) {
  const int bid = blockIdx.x, xcd = bid & 7, idx = bid >> 3, a = xcd >> 2, b = xcd & 3, s = idx >> 2;
  const int wv = rfl(threadIdx.x >> 6), lane = threadIdx.x & 63;
  Ids id;
  const bool ssm = idx < 10;
  const int part = s == 2 ? 1 : 0;
  id.R = 4 * a + (idx & 1) + 2 * ((part ? b : idx >> 1) & 1);
  const int gl = (9 - idx) >> 31;
  id.g = ((2 - 3 * gl) * b + s + 3 * gl) * (1 - part) + (8 - 12 * gl + (b >> 1)) * part;
  id.cg = id.g - 10 * gl;
  asm volatile("" ::"s"(p.x), "s"(p.ws), "s"(p.wg), "s"(p.wd), "s"(id.cg), "s"(id.R));
  RowIn rin;
  if (wv >= 4) {
    const int h = wv - 4;
    const unsigned v0 = (8 * (lane >> 3)) * 1280 + (((lane & 7) ^ (lane >> 4)) * 16);
    asm volatile("s_nop 4");
    dma_x(MkSeq<8>::T{}, mkrs(p.x, L_ * D_ * 2), LB + h * 8192, v0, v0 ^ 64, (256 * id.R + 128 * (h >> 1) + 4 * (h & 1)) * 1280);
  }
  unsigned cpf = 0;
  if (wv == 3) {
    unsigned long long pc; unsigned len;
    asm volatile("s_getpc_b64 %0\n.Lpf_here:\n\ts_mov_b32 %1, .Lpf_end-.Lpf_here" : "=s"(pc), "=s"(len));
    cpf = __builtin_amdgcn_raw_buffer_load_b32(mkrs((const void*)pc, len), lane < 13 ? (idx * 13 + lane) * 128 : OOB, 0, 0);
  }
  id.rt = 16 * a + 8 * (b >> 1) + (idx >> 2);
  id.ct = 4 * (b & 1) + (idx & 3);
  const __amdgpu_buffer_rsrc_t rctl = mkrs(p.ws, WS_CTL_BYTES);
  const __amdgpu_buffer_rsrc_t rw = mkrs(p.ws + WS_W2 + (unsigned)id.ct * (54 * 5120), 54 * 5120);
  const unsigned vl = lane * 16;
  const unsigned fo = WS_FLH + (id.rt * 8 + id.ct) * 128 + (lane & 31) * 4;
  WRing wr;
  wr.rw = rw; wr.vl = vl; wr.wv = wv;
  wr.rcr = mkrs(p.cre + id.ct * 80 * P_, 80 * P_ * 2); wr.rci = mkrs(p.cim + id.ct * 80 * P_, 80 * P_ * 2);
  wr.vc = (lane >> 2) * 1280 + (((lane & 3) ^ cswz(lane >> 4)) * 16);
  unsigned tag;
  if (wv < 4) {
    row_load(p, id, wv, lane, rin);
    if (wv == 2) cpf = ld4c(rctl, lane < 32 ? fo : lane < 40 ? WS_FLW + (lane - 32) * 128 : OOB);
    tag = ssm ? compute_wave<true>(p, id, wv, lane, rin) : compute_wave<false>(p, id, wv, lane, rin);
    WAITV(0);
    unsigned old = 0;
    if (lane == 0) old = __hip_atomic_fetch_add((LAS unsigned*)LP(L_CNT), 1u, __ATOMIC_RELAXED, __HIP_MEMORY_SCOPE_WORKGROUP);
    st4c(tag, rctl, (lane < 32 && rfl(old) == 3) ? WS_FLH + (id.R * 32 + lane) * 128 + id.cg * 4 : OOB);
    asm volatile("" ::"v"(cpf));
#pragma unroll
    for (int it = 0; it < DW; ++it) wr.load(it);
  } else {
    HState hs;
    tag = ssm ? helper_wave<true>(p, id, wv - 4, lane, hs) : helper_wave<false>(p, id, wv - 4, lane, hs);
    if (ssm) {
      helper_tail<true>(p, id, wv - 4, lane, tag, hs);
      _Pragma("unroll 1") for (int spin = 0; spin < SPIN; ++spin) { FENCE();
        const v4u f2 = ld16c(rctl, WS_FLW + lane * 16, 0);
        if (__all((f2.x == tag) & (f2.y == tag) & (f2.z == tag) & (f2.w == tag))) break;
        __builtin_amdgcn_s_sleep(4);
      }
    }
    wr.load(0); wr.load(1); wr.load(2);
    if (!ssm) helper_tail<false>(p, id, wv - 4, lane, tag, hs);
#pragma unroll
    for (int it = 3; it < DW; ++it) wr.load(it);
    if (ssm) carries(p, id, wv - 4, lane, tag, wr); else wr.dma01();
    lwait<2>(L_CNT, 0, 4);
    if (wv == 4) {
      _Pragma("unroll 1") for (int spin = 0; spin < SPIN; ++spin) { FENCE();
        if (__all((ld4c(rctl, fo) == tag) | ((lane & 31) < 10))) break;
        __builtin_amdgcn_s_sleep(2);
      }
      sig(L_GO, 1);
    }
    wr.dma2();
  }

  const __amdgpu_buffer_rsrc_t rh = mkrs(p.ws + WS_H + (unsigned)id.rt * (NKB * 4096), NKB * 4096);
  v4u hb[NIT][4];
  auto loadh1 = [&](int it, int j) {
    const unsigned so = it < 6 ? (8 * it + wv) * 4096 : it == 6 ? (wv < 6 ? (48 + wv) * 4096 : OOB) : (8 * it - 2 + wv) * 4096;
    hb[it][j] = ld16c(rh, vl + j * 1024, so);
  };
  lwait<1>(L_GO, 0, 1);
  SB0();
#pragma unroll
  for (int z = 0; z < 4 * DH; ++z) { loadh1(z >> 2, z & 3); SB0(); }
  f32x4 acc[4][5];
#pragma unroll
  for (int j = 0; j < 4; ++j)
#pragma unroll
    for (int i = 0; i < 5; ++i) acc[j][i] = f32x4{0.f, 0.f, 0.f, 0.f};
  const int tid = wv * 64 + lane;
  v2u xv[3], dv[3]; v4u sv[3]; int oo[3], ra[3];
  unsigned sfl = 0;
  const unsigned cr = wv * 5120 + (lane & 15) * 64 + (((lane >> 4) ^ cswz((lane >> 2) & 3)) * 16);
  const lp pw = LP(L_W + cr), pw2 = LP(cr);
  v4u wl[5][5];
  SB0();
  auto epi_load = [&](int r) {
    const int sl = min(tid + 512 * r, 1279), row = sl / 20, qd = sl - 20 * row;
    const int tok = 128 * (id.rt >> 1) + 8 * (row >> 2) + 4 * (id.rt & 1) + (row & 3), d = 80 * id.ct + 4 * qd;
    oo[r] = tok * D_ + d;
    ra[r] = (row & 3) * 40960 + (qd >> 2) * 1024 + (qd & 3) * 256 + (((row >> 2) + qd) & 15) * 16;
    xv[r] = *(const v2u*)(p.x + oo[r]); dv[r] = *(const v2u*)(p.dsk + d);
    sv[r] = ld16c(rctl, WS_STATS + tok * 16, 0);
  };
  auto round = [&](auto IT) __attribute__((always_inline)) {
    constexpr int it = IT.value;
    if (it + DH == NG) {
      if (__builtin_expect(!__all(sfl == tag), 0)) {
        _Pragma("unroll 1") for (int spin = 0; spin < SPIN; ++spin) { FENCE();
          if (__all(ld4c(rctl, fo) == tag)) break;
          __builtin_amdgcn_s_sleep(2);
        }
      }
      FENCE();
    }
    if constexpr (it + 1 >= NG && it + 1 < NIT) {
      constexpr int nr = it + 1 - NG;
      if (nr == 4) WAITV(9);
#pragma unroll
      for (int i = 0; i < 5; ++i) wl[nr][i] = l128(nr == 2 ? pw2 + 40960 + i * 1024 : nr == 3 ? pw2 + i * 1024 : pw + (nr & 1) * 40960 + i * 1024);
      SB0();
    }
#pragma unroll
    for (int j = 0; j < 4; ++j) {
#pragma unroll
      for (int i = 0; i < 5; ++i) {
        acc[j][i] = mfma16(it >= NG ? wl[it >= NG ? it - NG : 0][i] : wr.wb[it % DW][i], hb[it][j], acc[j][i]);
        if (j == 3 && it + DW < NG) { SB0(); wr.load1(it + DW, i); SB0(); }
        if (i == 2) {
          SB0();
          if (it == 3 && j == 0) wr.template refill<0, 0>();
          if (it == 3 && j == 1) wr.template refill<0, 1>();
          if (it == 3 && j == 2) wr.template refill<0, 2>();
          if (it == 4 && j == 0) wr.template refill<0, 3>();
          if (it == 4 && j == 2) wr.template refill<0, 4>();
          if (it == 2 && j == 1) sfl = ld4c(rctl, fo);
          if (it == 5 && j == 1) epi_load(0);
          if (it == 6 && j == 1) epi_load(1);
          if (it == 9 && j == 1) epi_load(2);
          if (it == 7 && j == 1) wr.template refill<1, 0>();
          if (it == 7 && j == 2) wr.template refill<1, 1>();
          if (it == 7 && j == 3) wr.template refill<1, 2>();
          if (it == 8 && j == 0) wr.template refill<1, 3>();
          if (it == 8 && j == 2) wr.template refill<1, 4>();
          SB0();
        }
      }
      SB0();
      if constexpr (it + DH < NIT) loadh1(it + DH, j);
      SB0();
    }
    if (it == 2) BAR();
    SB0();
  };
  for_seq(MkSeq<NIT>::T{}, round);
  asm volatile("" ::"v"(sv[0]), "v"(sv[1]), "v"(sv[2]), "v"(xv[2]), "v"(dv[2]));
#pragma unroll
  for (int i = 0; i < 5; ++i) {
    const lp rb = LP(wv * 5120 + i * 1024 + (lane >> 4) * 256 + (((lane & 15) + 4 * i + (lane >> 4)) & 15) * 16);
    sf4(rb, acc[0][i]); sf4(rb + 40960, acc[1][i]);
    const lp rc = rb + 81920;
    sf4(rc, acc[2][i]); sf4(rc + 40960, acc[3][i]);
  }
  BAR();
  asm volatile(".Lpf_end:");
#pragma unroll
  for (int r = 0; r < 3; ++r) {
    if (r < 2 || wv < 4) {
      const lp q0 = LP(ra[r]);
      f32x4 sum = ((lf4(q0) + lf4(q0 + 5120)) + (lf4(q0 + 10240) + lf4(q0 + 15360))) + ((lf4(q0 + 20480) + lf4(q0 + 25600)) + (lf4(q0 + 30720) + lf4(q0 + 35840)));
      const f32x4 xf = {bflo(xv[r].x), bfhi(xv[r].x), bflo(xv[r].y), bfhi(xv[r].y)};
      const f32x4 df = {bflo(dv[r].x), bfhi(dv[r].x), bflo(dv[r].y), bfhi(dv[r].y)};
      sum += xf + (xf - __uint_as_float(sv[r].x)) * __uint_as_float(sv[r].z) * df;
      __builtin_amdgcn_raw_buffer_store_b128(__builtin_bit_cast(v4u, sum), mkrs(p.out, L_ * D_ * 4), oo[r] * 4, 0, 16);
    }
  }
}

extern "C" void kernel_launch(void* const* d_in, const int* in_sizes, int n_in, void* d_out, int out_size, void* d_ws, size_t ws_size, hipStream_t stream) {
  Par p;
  p.x = (const u16*)d_in[0]; p.lre = (const u16*)d_in[1]; p.lim = (const u16*)d_in[2];
  p.bre = (const u16*)d_in[3]; p.bim = (const u16*)d_in[4]; p.cre = (const u16*)d_in[5]; p.cim = (const u16*)d_in[6];
  p.ldt = (const u16*)d_in[7]; p.dsk = (const u16*)d_in[8];
  p.wg = (const u16*)d_in[10]; p.wu = (const u16*)d_in[11]; p.wd = (const u16*)d_in[12];
  p.ws = (unsigned char*)d_ws; p.out = (float*)d_out;
  (void)hipFuncSetAttribute((const void*)fused, hipFuncAttributeMaxDynamicSharedMemorySize, LDS1);
  fused<<<256, 512, LDS1, stream>>>(p);
}
```

```cpp
#include <hip/hip_runtime.h>
#include <stdint.h>

typedef unsigned short u16;
typedef unsigned v4u __attribute__((ext_vector_type(4)));
typedef unsigned v2u __attribute__((ext_vector_type(2)));
typedef float f32x4 __attribute__((ext_vector_type(4)));
typedef float f32x2 __attribute__((ext_vector_type(2)));
typedef float f32x16 __attribute__((ext_vector_type(16)));
typedef short bf16x8 __attribute__((ext_vector_type(8)));
typedef short s16x4 __attribute__((ext_vector_type(4)));
typedef __bf16 bf2 __attribute__((ext_vector_type(2)));
#define LAS __attribute__((address_space(3)))
#define DI __device__ __forceinline__
typedef LAS unsigned char* lp;

constexpr int L_ = 2048, D_ = 640, P_ = 640, F_ = 1728;
constexpr int NKB = 94;
constexpr unsigned WS_EPOCH = 0;
constexpr unsigned WS_FLW = 2048;
constexpr unsigned WS_FLH = 4096;
constexpr unsigned WS_AGG = WS_FLH + 32768;
constexpr unsigned WS_STATS = WS_AGG + 64 * 640 * 16;
constexpr unsigned WS_CTL_BYTES = WS_STATS + 32768;
constexpr unsigned WS_W2 = 1u << 20;
constexpr unsigned WS_W2_BYTES = 8 * 54 * 5120;
constexpr unsigned WS_H = 8u << 20;

constexpr int UPO = 10368;
constexpr int STG = 32768 + 20480 + 128;
constexpr int LDS1 = 163840;
constexpr int L_STATS = 0;
constexpr int L_CS = L_STATS + 2048;
constexpr int L_CNT = L_CS + 1280, L_GO = L_CNT + 4, L_CSF = L_CNT + 16;
constexpr int LB = LDS1 - 3 * STG;
constexpr int L_CST = LB + 49152;
constexpr int L_W = 81920, L_W2 = 40960;
constexpr int L_E = LB + STG, ESTR = 528;
constexpr int SPIN = 1 << 18;
constexpr unsigned OOB = 0x40000000u;

DI float bf2f(unsigned b) { return __uint_as_float(b << 16); }
DI float bflo(unsigned w) { return __uint_as_float(w << 16); }
DI float bfhi(unsigned w) { return __uint_as_float(w & 0xffff0000u); }
DI unsigned pk2(float a, float b) { f32x2 v = {a, b}; return __builtin_bit_cast(unsigned, __builtin_convertvector(v, bf2)); }
DI float rbf(float a) { return bflo(pk2(a, a) & 0xffffu); }
DI f32x4 mfma16(v4u a, v4u b, f32x4 c) { return __builtin_amdgcn_mfma_f32_16x16x32_bf16(__builtin_bit_cast(bf16x8, a), __builtin_bit_cast(bf16x8, b), c, 0, 0, 0); }
DI f32x16 mfma32(v4u a, v4u b, f32x16 c) { return __builtin_amdgcn_mfma_f32_32x32x16_bf16(__builtin_bit_cast(bf16x8, a), __builtin_bit_cast(bf16x8, b), c, 0, 0, 0); }
DI unsigned rfl(unsigned v) { return __builtin_amdgcn_readfirstlane(v); }
DI __amdgpu_buffer_rsrc_t mkrs(const void* p, unsigned bytes) { return __builtin_amdgcn_make_buffer_rsrc((void*)p, 0, bytes, 0x00020000); }
template <int LI, int SI> DI void glds(__amdgpu_buffer_rsrc_t rs, unsigned lb, unsigned voff, unsigned sb) {
  unsigned t;
  asm volatile("s_add_u32 m0, %1, %5\n\ts_add_u32 %0, %4, %6\n\tbuffer_load_dwordx4 %2, %3, %0 offen lds" : "=&s"(t) : "s"(lb), "v"(voff), "s"(rs), "s"(sb), "i"(LI), "i"(SI) : "scc");
}
template <int... I> struct Seq {};
template <int N, int... I> struct MkSeq : MkSeq<N - 1, N - 1, I...> {};
template <int... I> struct MkSeq<0, I...> { typedef Seq<I...> T; };
template <int V> struct IC { static constexpr int value = V; };
template <int... I, class F> DI void for_seq(Seq<I...>, F& f) { (f(IC<I>{}), ...); }
template <int... I> DI void dma_x(Seq<I...>, __amdgpu_buffer_rsrc_t rs, unsigned lb, unsigned v0, unsigned v1, unsigned sb) { (glds<I * 1024, (64 * (I & 1) + (I >> 1)) * 1280>(rs, lb, (I & 1) ? v1 : v0, sb), ...); }
template <int... I> DI void dma_b(Seq<I...>, __amdgpu_buffer_rsrc_t rs, unsigned lb, const unsigned* v, unsigned sb) { (glds<I * 1024, (16 * (I & 1) + 4 * (I >> 1)) * 1280>(rs, lb, v[I & 1], sb), ...); }
template <int... I> DI void dma_g(Seq<I...>, __amdgpu_buffer_rsrc_t rs, unsigned lb, const unsigned* v, unsigned sb) { (glds<I * 1024, 0>(rs, lb, v[I], sb), ...); }
DI lp LP(unsigned a) { return (lp)(uintptr_t)a; }
DI v4u l128(lp a) { return *(LAS v4u*)a; }
DI f32x4 lf4(lp a) { return *(LAS f32x4*)a; }
DI void sf4(lp a, f32x4 v) { *(LAS f32x4*)a = v; }
DI s16x4 ltr(lp a) { return __builtin_amdgcn_ds_read_tr16_b64_v4i16((LAS s16x4*)a); }
DI v4u cat(s16x4 lo, s16x4 hi) { return __builtin_bit_cast(v4u, __builtin_shufflevector(lo, hi, 0, 1, 2, 3, 4, 5, 6, 7)); }
DI v4u ld16(__amdgpu_buffer_rsrc_t r, unsigned vo, unsigned so) { return __builtin_amdgcn_raw_buffer_load_b128(r, vo, so, 0); }
DI v4u ld16c(__amdgpu_buffer_rsrc_t r, unsigned vo, unsigned so) { return __builtin_amdgcn_raw_buffer_load_b128(r, vo, so, 16); }
DI unsigned ld4c(__amdgpu_buffer_rsrc_t r, unsigned vo) { return __builtin_amdgcn_raw_buffer_load_b32(r, vo, 0, 16); }
DI void st16c(v4u v, __amdgpu_buffer_rsrc_t r, unsigned vo) { __builtin_amdgcn_raw_buffer_store_b128(v, r, vo, 0, 16); }
DI void st4c(unsigned v, __amdgpu_buffer_rsrc_t r, unsigned vo) { __builtin_amdgcn_raw_buffer_store_b32(v, r, vo, 0, 16); }
#define BAR() asm volatile("s_waitcnt lgkmcnt(0)\n\ts_barrier" ::: "memory")
#define SB0() __builtin_amdgcn_sched_barrier(0)
#define WAITV(n) asm volatile("s_waitcnt vmcnt(%0)" ::"i"(n) : "memory")
#define FENCE() asm volatile("" ::: "memory")
template <int C> DI float dppf(float v) { return __int_as_float(__builtin_amdgcn_update_dpp(0, __float_as_int(v), C, 0xf, 0xf, true)); }
DI float sum8(float v) {
  v += dppf<0xB1>(v); v += dppf<0x4E>(v); v += dppf<0x141>(v);
  return v;
}
DI void gather4(float x, float (&t)[4]) {
  const auto a = __builtin_amdgcn_permlane16_swap(__float_as_uint(x), __float_as_uint(x), false, false);
  const auto e = __builtin_amdgcn_permlane32_swap(a[0], a[0], false, false), o = __builtin_amdgcn_permlane32_swap(a[1], a[1], false, false);
  t[0] = __uint_as_float(e[0]); t[2] = __uint_as_float(e[1]); t[1] = __uint_as_float(o[0]); t[3] = __uint_as_float(o[1]);
}
DI void cm1(float& a, float& b, float lr, float li, float ur, float ui) {
  const float na = fmaf(lr, a, fmaf(-li, b, ur)), nb = fmaf(lr, b, fmaf(li, a, ui));
  a = na; b = nb;
}
DI f32x4 fma4(f32x4 a, f32x4 b, f32x4 c) { return __builtin_elementwise_fma(a, b, c); }
DI f32x4 sp4(float a) { return f32x4{a, a, a, a}; }
DI void cmac(f32x4& a, f32x4& b, f32x4 lr, f32x4 li, f32x4 ur, f32x4 ui) {
  const f32x4 na = fma4(lr, a, fma4(-li, b, ur)), nb = fma4(lr, b, fma4(li, a, ui));
  a = na; b = nb;
}

struct Par {
  const u16 *x, *lre, *lim, *bre, *bim, *cre, *cim, *ldt, *dsk, *wg, *wu, *wd;
  unsigned char* ws;
  float* out;
};
struct Ids { int R, g, cg, rt, ct; };


constexpr int DW = 5, DH = 2, NIT = 12, NG = 7;
#define PIN4(x) asm volatile("" : "+v"(x))
DI void sig(unsigned addr, unsigned v) {
  asm volatile("s_mov_b64 exec, 1\n\tds_write_b32 %0, %1\n\ts_mov_b64 exec, -1" ::"v"(addr), "v"(v) : "memory");
}
DI unsigned lrd(unsigned addr) { return *(volatile LAS unsigned*)LP(addr); }
template <int SL = 1> DI void lwait(unsigned addr, unsigned v, unsigned need) {
  if (__builtin_expect(!__all(v >= need), 0)) {
    _Pragma("unroll 1") for (int spin = 0; spin < 64 * SPIN; ++spin) { FENCE();
      if (__all(lrd(addr) >= need)) break;
      __builtin_amdgcn_s_sleep(SL);
    }
  }
  FENCE();
}

template <int... I> DI void dma_c2(Seq<I...>, __amdgpu_buffer_rsrc_t rs, unsigned lb, unsigned v, unsigned sb) { (glds<(I & 1) * 5120 + (I >> 1) * 1024, (I & 1) * 64 + (I >> 1) * 16 * 1280>(rs, lb, v, sb), ...); }
DI unsigned cswz(unsigned g) { return ((g << 1) & 2) ^ ((g >> 1) * 3); }
struct WRing {
  v4u wb[DW][5];
  __amdgpu_buffer_rsrc_t rw, rcr, rci;
  unsigned vl, vc, wv;
  DI void load(int it) {
#pragma unroll
    for (int i = 0; i < 5; ++i) { wb[it % DW][i] = ld16c(rw, vl + (i & 3) * 1024, (8 * it + wv) * 5120 + (i >> 2) * 4096); SB0(); }
  }
  DI void dma01() {
    asm volatile("s_nop 4");
    dma_c2(MkSeq<10>::T{}, rcr, L_W + (wv - 4) * 20480, vc, (wv - 4) * 256);
    dma_c2(MkSeq<10>::T{}, rcr, L_W + (wv - 4) * 20480 + 10240, vc, (wv - 4) * 256 + 128);
  }
  DI void dma2() {
    asm volatile("s_nop 4");
    dma_c2(MkSeq<10>::T{}, wv < 6 ? rcr : rci, L_W2 + (wv - 4) * 10240, vc, wv < 6 ? 1024 + (wv - 4) * 128 : (wv - 6) * 128);
  }
  template <int R, int I> DI void refill() {
    glds<R * L_W + I * 1024, (4 + 8 * R) * 64 + I * 16 * 1280>(rci, wv * 5120, vc, wv * 64);
  }
  DI void load1(int it, int i) { wb[it % DW][i] = ld16c(rw, vl + (i & 3) * 1024, (8 * it + wv) * 5120 + (i >> 2) * 4096); }
};

DI float wsum(float v) {
  v = sum8(v); v += dppf<0x128>(v);
  float t[4]; gather4(v, t);
  return (t[0] + t[1]) + (t[2] + t[3]);
}
DI unsigned mktag(unsigned epoch) { return rfl(epoch) * 0x9E3779B1u + 0x7F4A7C15u; }
struct RowIn { v4u xa[2]; unsigned xb[2], epoch; };
DI void row_load(const Par& p, const Ids& id, const int w, const int lane, RowIn& in) {
  const int row = 256 * id.R + 8 * id.cg + 2 * w;
#pragma unroll
  for (int z = 0; z < 2; ++z) { in.xa[z] = *(const v4u*)(p.x + (row + z) * D_ + lane * 8); in.xb[z] = *(const unsigned*)(p.x + (row + z) * D_ + 512 + lane * 2); }
  in.epoch = ld4c(mkrs(p.ws, WS_CTL_BYTES), WS_EPOCH);
  SB0();
}
DI unsigned row_stats(const Par& p, const Ids& id, const int w, const int lane, const RowIn& in) {
  const __amdgpu_buffer_rsrc_t rctl = mkrs(p.ws, WS_CTL_BYTES);
  const int row = 256 * id.R + 8 * id.cg + 2 * w;
  const v4u* xa = in.xa; const unsigned* xb = in.xb; const unsigned epoch = in.epoch;
  float v[2][10], mean[2], s2[2];
#pragma unroll
  for (int z = 0; z < 2; ++z) {
    const float t[10] = {bflo(xa[z].x), bfhi(xa[z].x), bflo(xa[z].y), bfhi(xa[z].y), bflo(xa[z].z), bfhi(xa[z].z), bflo(xa[z].w), bfhi(xa[z].w), bflo(xb[z]), bfhi(xb[z])};
#pragma unroll
    for (int e = 0; e < 10; ++e) v[z][e] = t[e];
    mean[z] = wsum(((t[0] + t[1]) + (t[2] + t[3])) + ((t[4] + t[5]) + (t[6] + t[7])) + (t[8] + t[9])) * (1.f / D_);
  }
#pragma unroll
  for (int z = 0; z < 2; ++z) {
    float a = 0.f, b = 0.f;
#pragma unroll
    for (int e = 0; e < 5; ++e) { a = fmaf(v[z][e] - mean[z], v[z][e] - mean[z], a); b = fmaf(v[z][5 + e] - mean[z], v[z][5 + e] - mean[z], b); }
    s2[z] = wsum(a + b);
  }
  const unsigned tag = mktag(epoch);
  const float m = rbf((lane & 1) ? mean[1] : mean[0]), rs = 1.f / rbf(sqrtf(rbf(rbf(((lane & 1) ? s2[1] : s2[0]) * (1.f / D_)) + 1e-5f)));
  st16c(v4u{__float_as_uint(m), tag, __float_as_uint(rs), tag}, rctl, lane < 2 ? WS_STATS + (row + lane) * 16 : OOB);
  return tag;
}

template <bool SSM>
DI unsigned compute_wave(const Par& p, const Ids& id, const int w, const int lane, const RowIn& rin) {
  unsigned tag, cf = 0;
  if constexpr (!SSM) {
    const int r = lane & 31, hh = lane >> 5;
    const unsigned xa = (64 * w + r) * 128 + ((hh ^ ((r >> 1) & 7)) * 16);
    const unsigned wa = 32768 + ((lane >> 4) & 1) * UPO + (8 * hh + ((lane & 15) >> 2)) * 160 + (lane & 3) * 8;
    f32x16 acc[5][2] = {};
#pragma unroll
    for (int e = 0; e < 5; ++e) { PIN4(acc[e][0]); PIN4(acc[e][1]); }
    v4u fx[2][2]; s16x4 fl[4], fh[4];
    lp px[4], pw;
    auto setp = [&](unsigned sb) {
#pragma unroll
      for (int s = 0; s < 4; ++s) px[s] = LP(LB + sb + (xa ^ (32 * s)));
      pw = LP(LB + sb + wa);
    };
    auto woff = [&](int n) { return (n / 5) * 2560 + (n % 5) * 32; };
    setp(0);
    SB0();
    tag = row_stats(p, id, w, lane, rin);
    SB0();
    BAR();
    fx[0][0] = l128(px[0]); fx[0][1] = l128(px[0] + 4096);
#pragma unroll
    for (int n = 0; n < 3; ++n) { fl[n] = ltr(pw + woff(n)); fh[n] = ltr(pw + woff(n) + 640); }
    unsigned sb = 0;
#pragma unroll 1
    for (int i = 0; i < 10; ++i) {
#pragma unroll
      for (int n = 0; n < 20; ++n) {
        const int ks = n / 5, e = n % 5, nn = (n + 3) % 20;
        if (n == 17) { if (i < 9) BAR(); sb = sb == 2 * STG ? 0 : sb + STG; setp(sb); SB0(); }
        const v4u wf = cat(fl[n & 3], fh[n & 3]);
        acc[e][0] = mfma32(wf, fx[ks & 1][0], acc[e][0]); SB0();
        fl[(n + 3) & 3] = ltr(pw + woff(nn));
        fh[(n + 3) & 3] = ltr(pw + woff(nn) + 640); SB0();
        acc[e][1] = mfma32(wf, fx[ks & 1][1], acc[e][1]); SB0();
        if (ks < 3 && e == 1) { fx[(ks + 1) & 1][0] = l128(px[ks + 1]); fx[(ks + 1) & 1][1] = l128(px[ks + 1] + 4096); }
        if (n == 17) { fx[0][0] = l128(px[0]); fx[0][1] = l128(px[0] + 4096); }
        if (n == 18) cf = lrd(L_CSF + (lane & 3) * 4);
        SB0();
      }
    }
    lwait(L_CSF + (lane & 3) * 4, cf, 1);
    const __amdgpu_buffer_rsrc_t rh = mkrs(p.ws + WS_H + (unsigned)(4 * id.R + w) * (NKB * 4096), 54 * 4096);
    const unsigned so0 = (r >> 4) * 1024 + (hh * 16 + (r & 15)) * 16;
    float mu[2], r2[2];
#pragma unroll
    for (int u = 0; u < 2; ++u) {
      const int F = 4 * w + 2 * u + (r >> 4);
      const f32x2 s = *(LAS f32x2*)LP(L_STATS + (128 * (F >> 3) + 8 * (r & 15) + (F & 7)) * 8);
      mu[u] = s.x; r2[u] = s.y * s.y;
    }
    const lp pc = LP(L_CS + hh * 16);
#pragma unroll
    for (int e = 0; e < 5; ++e) {
      const f32x4 g0 = lf4(pc + e * 64) + lf4(pc + e * 64 + 640), g1 = lf4(pc + e * 64 + 32) + lf4(pc + e * 64 + 672);
      const f32x4 u0 = lf4(pc + e * 64 + 320) + lf4(pc + e * 64 + 960), u1 = lf4(pc + e * 64 + 352) + lf4(pc + e * 64 + 992);
      const int T = 5 * id.g + e;
      const unsigned so = so0 + (T >> 1) * 4096 + (T & 1) * 512;
#pragma unroll
      for (int u = 0; u < 2; ++u) {
        const f32x16 a = acc[e][u];
        const f32x4 nm = sp4(-mu[u]);
        const f32x4 h0 = fma4(nm, g0, f32x4{a[0], a[1], a[2], a[3]}) * fma4(nm, u0, f32x4{a[8], a[9], a[10], a[11]}) * r2[u];
        const f32x4 h1 = fma4(nm, g1, f32x4{a[4], a[5], a[6], a[7]}) * fma4(nm, u1, f32x4{a[12], a[13], a[14], a[15]}) * r2[u];
        st16c(v4u{pk2(h0.x, h0.y), pk2(h0.z, h0.w), pk2(h1.x, h1.y), pk2(h1.z, h1.w)}, rh, so + u * 2048);
      }
    }
  } else {
    const int c = lane & 15, q = lane >> 4, G = w >> 1, blk = w & 1;
    const unsigned xa = (128 * G + c) * 128 + ((q ^ (c >> 1)) * 16);
    const unsigned wa = 32768 + (32 * blk + c) * 128 + ((q ^ (c >> 1)) * 16);
    f32x4 acc[8][4] = {};
#pragma unroll
    for (int j = 0; j < 8; ++j)
#pragma unroll
      for (int t = 0; t < 4; ++t) PIN4(acc[j][t]);
    v4u fw[2][4], fx[4];
    lp px[2], pw[2];
    auto setp = [&](unsigned sb) { px[0] = LP(LB + sb + xa); px[1] = LP(LB + sb + (xa ^ 64)); pw[0] = LP(LB + sb + wa); pw[1] = LP(LB + sb + (wa ^ 64)); };
    auto woff = [&](int t) { return (t >> 1) * 8192 + (t & 1) * 2048; };
    setp(0);
    SB0();
    tag = row_stats(p, id, w, lane, rin);
    SB0();
    BAR();
#pragma unroll
    for (int t = 0; t < 4; ++t) fw[0][t] = l128(pw[0] + woff(t));
#pragma unroll
    for (int n = 0; n < 3; ++n) fx[n] = l128(px[0] + n * 2048);
    unsigned sb = 0;
#pragma unroll 1
    for (int i = 0; i < 10; ++i) {
#pragma unroll
      for (int n = 0; n < 16; ++n) {
        const int kk = n >> 3, j = n & 7, nn = (n + 3) & 15;
        if (n == 13) { if (i < 9) BAR(); sb = sb == 2 * STG ? 0 : sb + STG; setp(sb); SB0(); }
        const v4u xf = fx[n & 3];
        acc[j][0] = mfma16(fw[kk][0], xf, acc[j][0]); SB0();
        fx[(n + 3) & 3] = l128(px[nn >> 3] + (nn & 7) * 2048); SB0();
        acc[j][1] = mfma16(fw[kk][1], xf, acc[j][1]); SB0();
        if (n >= 2 && n < 6) fw[1][n - 2] = l128(pw[1] + woff(n - 2));
        if (n == 13) fw[0][0] = l128(pw[0] + woff(0));
        if (n == 14) fw[0][2] = l128(pw[0] + woff(2));
        SB0();
        acc[j][2] = mfma16(fw[kk][2], xf, acc[j][2]); SB0();
        if (n == 13) fw[0][1] = l128(pw[0] + woff(1));
        if (n == 14) fw[0][3] = l128(pw[0] + woff(3));
        SB0();
        acc[j][3] = mfma16(fw[kk][3], xf, acc[j][3]); SB0();
        if (n == 14) { cf = lrd(L_CSF + (lane & 3) * 4); SB0(); }
      }
    }
    lwait(L_CSF + (lane & 3) * 4, cf, 1);
    const lp pe = LP(L_E + (16 * G + c) * ESTR + (32 * blk + 8 * q) * 4);
    const lp pk = LP(L_CST + (32 * blk + 8 * q) * 4);
    f32x4 lr[2], li[2], er[2], ei[2];
    {
      const lp ps = LP(L_STATS + (128 * G + 8 * c) * 8), pc = LP(L_CS + (32 * blk + 8 * q) * 4);
      f32x4 cs[4], st[4];
#pragma unroll
      for (int t = 0; t < 4; ++t) { cs[t] = lf4(pc + (t >> 1) * 320 + (t & 1) * 16) + lf4(pc + 640 + (t >> 1) * 320 + (t & 1) * 16); st[t] = lf4(ps + t * 16); }
#pragma unroll
      for (int ab = 0; ab < 2; ++ab) { lr[ab] = lf4(pk + ab * 16); li[ab] = lf4(pk + 256 + ab * 16); }
#pragma unroll
      for (int jj = 0; jj < 4; ++jj)
#pragma unroll
        for (int t = 0; t < 4; ++t) {
          acc[2 * jj][t] = fma4(sp4(-st[jj].x), cs[t], acc[2 * jj][t]) * st[jj].y;
          acc[2 * jj + 1][t] = fma4(sp4(-st[jj].z), cs[t], acc[2 * jj + 1][t]) * st[jj].w;
        }
    }
#pragma unroll
    for (int ab = 0; ab < 2; ++ab) { er[ab] = acc[0][ab]; ei[ab] = acc[0][2 + ab]; }
#pragma unroll
    for (int j = 1; j < 8; ++j)
#pragma unroll
      for (int ab = 0; ab < 2; ++ab) cmac(er[ab], ei[ab], lr[ab], li[ab], acc[j][ab], acc[j][2 + ab]);
#pragma unroll
    for (int ab = 0; ab < 2; ++ab) { sf4(pe + ab * 16, er[ab]); sf4(pe + 256 + ab * 16, ei[ab]); }
    SB0();
    BAR();
#pragma unroll
    for (int ab = 0; ab < 2; ++ab) {
      const f32x4 kr = lf4(pk + 512 * 4 + ab * 16), ki = lf4(pk + 512 * 4 + 256 + ab * 16);
#pragma unroll
      for (int j = 0; j < 8; ++j) {
        const f32x4 ur = acc[j][ab], ui = acc[j][2 + ab];
        acc[j][ab] = fma4(-ki, ui, kr * ur); acc[j][2 + ab] = fma4(-ki, ur, -kr * ui);
        PIN4(acc[j][ab]); PIN4(acc[j][2 + ab]);
      }
      li[ab] = -li[ab];
    }
    const __amdgpu_buffer_rsrc_t rh = mkrs(p.ws + WS_H + (unsigned)(4 * id.R + 2 * G) * (NKB * 4096), 2 * NKB * 4096);
    unsigned vo[4];
#pragma unroll
    for (int z = 0; z < 4; ++z) { vo[z] = (54 + 2 * id.g + blk + 20 * (z & 1) + NKB * (z >> 1)) * 4096 + lane * 16; PIN4(vo[z]); }
    SB0();
    BAR();
#pragma unroll
    for (int ab = 0; ab < 2; ++ab) { er[ab] = lf4(pe + ab * 16); ei[ab] = lf4(pe + 256 + ab * 16); }
#pragma unroll
    for (int j = 0; j < 8; ++j) {
#pragma unroll
      for (int ab = 0; ab < 2; ++ab) cmac(er[ab], ei[ab], lr[ab], li[ab], acc[j][ab], acc[j][2 + ab]);
      st16c(v4u{pk2(er[0].x, er[0].y), pk2(er[0].z, er[0].w), pk2(er[1].x, er[1].y), pk2(er[1].z, er[1].w)}, rh, vo[2 * (j >> 2)] + (j & 3) * 1024);
      st16c(v4u{pk2(ei[0].x, ei[0].y), pk2(ei[0].z, ei[0].w), pk2(ei[1].x, ei[1].y), pk2(ei[1].z, ei[1].w)}, rh, vo[2 * (j >> 2) + 1] + (j & 3) * 1024);
    }
  }
  SB0();
  return tag;
}

#define HBAR() do { SB0(); BAR(); SB0(); } while (0)
struct HState { f32x4 acs[5]; unsigned pl, pr, pi; };
template <bool SSM>
DI unsigned helper_wave(const Par& p, const Ids& id, const int h, const int lane, HState& hs) {
  constexpr int NWP = SSM ? 4 : 5, NP = 8 + NWP;
  constexpr unsigned WSTEP = SSM ? 128u : 64u * 3456u;
  const int c = lane & 15, q = lane >> 4;
  const __amdgpu_buffer_rsrc_t rctl = mkrs(p.ws, WS_CTL_BYTES);
  const __amdgpu_buffer_rsrc_t rx = mkrs(p.x, L_ * D_ * 2);
  const __amdgpu_buffer_rsrc_t rw = mkrs(SSM ? ((h >> 1) ? p.bim : p.bre) : ((h >> 1) ? p.wu : p.wg), SSM ? P_ * D_ * 2 : D_ * F_ * 2);
  const unsigned v0 = (8 * (lane >> 3)) * 1280 + (((lane & 7) ^ (lane >> 4)) * 16), v1 = v0 ^ 64;
  unsigned st = 0;
  const unsigned sx = (256 * id.R + 128 * (h >> 1) + 4 * (h & 1)) * 1280;
  auto issue_x = [&](unsigned s, unsigned sb) {
    dma_x(MkSeq<8>::T{}, rx, LB + sb + h * 8192, v0, v1, s);
  };
  unsigned vw[NWP];
  if constexpr (SSM) {
    vw[0] = (8 * (lane >> 5) + ((lane >> 3) & 3)) * 1280 + (((lane & 7) ^ (lane >> 4)) * 16); vw[1] = vw[0] ^ 64;
  } else {
#pragma unroll
    for (int i = 0; i < 5; ++i) { const int lc = 64 * (5 * (h & 1) + i) + lane, pos = lc / 10; vw[i] = pos * 3456 + (lc - pos * 10) * 16; }
  }
  const unsigned sw = SSM ? (64 * id.g + 32 * (h & 1)) * 1280 : 160 * id.g;
  const unsigned dw = LB + 32768 + (h >> 1) * (SSM ? 8192 : UPO) + (h & 1) * NWP * 1024;
  auto issue_w = [&](unsigned s, unsigned sb) {
    if constexpr (SSM) dma_b(MkSeq<4>::T{}, rw, sb + dw, vw, s); else dma_g(MkSeq<5>::T{}, rw, sb + dw, vw, s);
  };
  asm volatile("s_nop 4");
  issue_w(sw, 0);
  unsigned sb = STG;
  auto step = [&]() {
    ++st;
    issue_x(sx + st * 128, sb); issue_w(sw + st * WSTEP, sb);
    sb = sb == 2 * STG ? 0 : sb + STG;
  };
  step();
  if (h == 3 && lane < 8) *(LAS unsigned*)LP(L_CNT + lane * 4) = 0u;
  f32x4 acs[NWP] = {};
  v4u fc[NWP];
  const unsigned ca = SSM ? 32768 + (h >> 1) * 8192 + c * 128 + (((4 * (h & 1) + q) ^ (c >> 1)) * 16)
                          : 32768 + (h >> 1) * UPO + (32 * (h & 1) + 4 * q + (c >> 2)) * 160 + (c & 3) * 8;
  const v4u ones = {0x3F803F80u, 0x3F803F80u, 0x3F803F80u, 0x3F803F80u};
  auto cs_rd = [&](unsigned s) {
    const lp a = LP(LB + s + ca);
#pragma unroll
    for (int t = 0; t < NWP; ++t) { if constexpr (SSM) fc[t] = l128(a + t * 2048); else fc[t] = cat(ltr(a + t * 32), ltr(a + t * 32 + 2560)); }
    SB0();
  };
  auto cs_mm = [&]() {
    SB0();
    __builtin_amdgcn_s_setprio(2);
#pragma unroll
    for (int t = 0; t < NWP; ++t) acs[t] = mfma16(fc[t], ones, acs[t]);
    __builtin_amdgcn_s_setprio(0);
    SB0();
  };
  constexpr int JL = 1;
  const int tsk = (id.g * 8 + id.R) * 4 + h, kb = tsk / 10, dq = tsk - 10 * kb;
  v4u f[4] = {}, sel[2], sv = {}, fl = {};
  unsigned fvo, fso[4], tch = 0;
  const __amdgpu_buffer_rsrc_t rf = mkrs(SSM ? ((tsk & 1) ? p.cim : p.cre) : p.wd, SSM ? D_ * P_ * 2 : F_ * D_ * 2);
  const __amdgpu_buffer_rsrc_t r2 = mkrs(p.ws + WS_W2, tsk < 540 ? WS_W2_BYTES : 0);
  if constexpr (SSM) fvo = lane < 40 ? ((tsk >> 1) * 40 + lane) * 128 : OOB;
  else {
    fvo = (32 * kb + 16 * ((lane >> 3) & 1) + 4 * (q & 1) + (q >> 1)) * 1280 + dq * 128 + (lane & 7) * 16;
#pragma unroll
    for (int i = 0; i < 4; ++i) {
      const int dt = 4 * dq + 2 * (q & 1) + (i >> 1), ct = dt / 5;
      fso[i] = ((ct * 54 + kb) * 5 + dt - 5 * ct) * 1024 + (16 * (2 * (q >> 1) + (c >> 3)) + 8 * (i & 1) + (c & 7)) * 16;
      PIN4(fso[i]);
    }
#pragma unroll
    for (int hf = 0; hf < 2; ++hf) {
#pragma unroll
      for (int e = 0; e < 4; ++e) sel[hf][e] = (8 * q + 2 * e == 16 * hf + c ? 0x3F80u : 0u) | (8 * q + 2 * e + 1 == 16 * hf + c ? 0x3F800000u : 0u);
      PIN4(sel[hf]);
    }
  }
  PIN4(fvo);
  unsigned epoch, pl = 0, pr = 0, pi = 0, tag = 0;
  unsigned so = WS_STATS + (256 * id.R + 64 * h + lane) * 16, flo = h == 3 ? WS_FLW + lane * 16 : OOB, fwo = (h == 3 && lane == 0) ? WS_FLW + blockIdx.x * 4 : OOB;
  PIN4(so); PIN4(flo); PIN4(fwo);
  asm volatile("buffer_load_dword %0, off, %1, 0 sc1" : "=v"(epoch) : "s"(rctl));
  if constexpr (SSM) {
    const unsigned po = (64 * id.g + lane) * 2;
    asm volatile("buffer_load_ushort %0, %3, %4, 0 offen\n\tbuffer_load_ushort %1, %3, %5, 0 offen\n\tbuffer_load_ushort %2, %3, %6, 0 offen"
                 : "=&v"(pl), "=&v"(pr), "=&v"(pi) : "v"(po), "s"(mkrs(p.ldt, P_ * 2)), "s"(mkrs(p.lre, P_ * 2)), "s"(mkrs(p.lim, P_ * 2)));
  }
  WAITV(NP + (SSM ? 4 : 1)); BAR();
#pragma unroll
  for (int j = 0; j < 8; ++j) {
    cs_rd((j % 3) * STG);
    if (j == JL + 4) { tag = mktag(epoch); st4c(tag, rctl, fwo); }
    if (j == 6) asm volatile("buffer_load_dwordx4 %0, %1, %2, 0 offen sc1" : "=v"(sv) : "v"(so), "s"(rctl));
    if (!SSM && j == 7) asm volatile("buffer_load_dwordx4 %0, %1, %2, 0 offen sc1" : "=v"(fl) : "v"(flo), "s"(rctl));
    SB0();
    step();
    if (SSM && j == 3) asm volatile("buffer_load_dword %0, %1, %2, 0 offen" : "=v"(tch) : "v"(fvo), "s"(rf));
    if (!SSM && j == JL)
      asm volatile("buffer_load_dwordx4 %0, %4, %5, 0 offen\n\tbuffer_load_dwordx4 %1, %4, %5, 0 offen offset:2560\n\tbuffer_load_dwordx4 %2, %4, %5, %6 offen\n\tbuffer_load_dwordx4 %3, %4, %5, %6 offen offset:2560"
                   : "=&v"(f[0]), "=&v"(f[1]), "=&v"(f[2]), "=&v"(f[3]) : "v"(fvo), "s"(rf), "s"(8 * 1280));
    cs_mm();
    if (!SSM && j == JL + 2) {
      f32x4 d[8];
      __builtin_amdgcn_s_setprio(2);
#pragma unroll
      for (int z = 0; z < 8; ++z) d[z] = mfma16(f[z >> 1], sel[z & 1], f32x4{0.f, 0.f, 0.f, 0.f});
#pragma unroll
      for (int i = 0; i < 4; ++i) st16c(v4u{pk2(d[0][i], d[1][i]), pk2(d[2][i], d[3][i]), pk2(d[4][i], d[5][i]), pk2(d[6][i], d[7][i])}, r2, fso[i]);
      __builtin_amdgcn_s_setprio(0);
      SB0();
    }
    if (!SSM && (j == JL || j == JL + 2)) WAITV(NP + 4);
    else if (SSM && j == 3) WAITV(NP + 1);
    else if (j == JL + 1) asm volatile("s_waitcnt vmcnt(%8)" : "+v"(f[0]), "+v"(f[1]), "+v"(f[2]), "+v"(f[3]), "+v"(epoch), "+v"(pl), "+v"(pr), "+v"(pi) : "i"(NP) : "memory");
    else if (j == 4) asm volatile("s_waitcnt vmcnt(%1)" : "+v"(tch) : "i"(NP) : "memory");
    else if (j == 6) asm volatile("s_waitcnt vmcnt(%1)" : "+v"(sv) : "i"(NP) : "memory");
    else if (j == 7) asm volatile("s_waitcnt vmcnt(%1)" : "+v"(fl) : "i"(NP) : "memory");
    else if (j == JL + 4) WAITV(NP + 1);
    else WAITV(NP);
    if (j == 6) {
      v2u o = {sv.x, sv.z};
      if (!__all((sv.y == tag) & (sv.w == tag))) {
        _Pragma("unroll 1") for (int spin = 0; spin < SPIN; ++spin) { FENCE();
          const v4u s2 = ld16c(rctl, so, 0);
          o = v2u{s2.x, s2.z};
          if (__all((s2.y == tag) & (s2.w == tag))) break;
          __builtin_amdgcn_s_sleep(4);
        }
      }
      *(LAS v2u*)LP(L_STATS + (64 * h + lane) * 8) = o;
    }
    if (!SSM && j == 7 && h == 3) {
      if (!__all((fl.x == tag) & (fl.y == tag) & (fl.z == tag) & (fl.w == tag))) {
        _Pragma("unroll 1") for (int spin = 0; spin < SPIN; ++spin) { FENCE();
          const v4u f2 = ld16c(rctl, flo, 0);
          if (__all((f2.x == tag) & (f2.y == tag) & (f2.z == tag) & (f2.w == tag))) break;
          __builtin_amdgcn_s_sleep(4);
        }
      }
      st4c(tag, rctl, (lane == 0 && blockIdx.x == 255) ? WS_EPOCH : OOB);
    }
    BAR();
  }
#pragma unroll
  for (int t = 0; t < 5; ++t) hs.acs[t] = acs[t < NWP ? t : 0];
  hs.pl = pl; hs.pr = pr; hs.pi = pi;
  return tag;
}
template <bool SSM>
DI void helper_tail(const Par& p, const Ids& id, const int h, const int lane, const unsigned tag, HState& hs) {
  constexpr int NWP = SSM ? 4 : 5;
  const int c = lane & 15, q = lane >> 4;
  const __amdgpu_buffer_rsrc_t rctl = mkrs(p.ws, WS_CTL_BYTES);
  f32x4* acs = hs.acs;
  const unsigned pl = hs.pl, pr = hs.pr, pi = hs.pi;
  v4u fc[NWP];
  const unsigned ca = SSM ? 32768 + (h >> 1) * 8192 + c * 128 + (((4 * (h & 1) + q) ^ (c >> 1)) * 16)
                          : 32768 + (h >> 1) * UPO + (32 * (h & 1) + 4 * q + (c >> 2)) * 160 + (c & 3) * 8;
  const v4u ones = {0x3F803F80u, 0x3F803F80u, 0x3F803F80u, 0x3F803F80u};
  auto cs_rd = [&](unsigned s) {
    const lp a = LP(LB + s + ca);
#pragma unroll
    for (int t = 0; t < NWP; ++t) { if constexpr (SSM) fc[t] = l128(a + t * 2048); else fc[t] = cat(ltr(a + t * 32), ltr(a + t * 32 + 2560)); }
    SB0();
  };
  auto cs_mm = [&]() {
    SB0();
    __builtin_amdgcn_s_setprio(2);
#pragma unroll
    for (int t = 0; t < NWP; ++t) acs[t] = mfma16(fc[t], ones, acs[t]);
    __builtin_amdgcn_s_setprio(0);
    SB0();
  };
  cs_rd(2 * STG);
  float br = 0.f, bi = 0.f;
  if (SSM && h == 2) {
    const float dt = rbf(expf(bf2f(pl))), ex = expf(dt * bf2f(pr));
    float sn, cn;
    sincosf(dt * bf2f(pi), &sn, &cn);
    br = ex * cn; bi = ex * sn;
  }
  cs_mm();
  WAITV(SSM ? 0 : 15); BAR();
  cs_rd(0);
  if (SSM && h == 2) {
    const float lr = bf2f(pr), li = bf2f(pi), nr = br - 1.f, ni = bi, den = lr * lr + li * li;
    LAS float* cst = (LAS float*)LP(L_CST);
    cst[4 * 128 + lane] = 2.f * (nr * lr + ni * li) / den; cst[4 * 128 + 64 + lane] = 2.f * (ni * lr - nr * li) / den;
    cst[lane] = br; cst[64 + lane] = bi;
#pragma unroll
    for (int s = 1; s <= 8; ++s) {
      const float a = br * br - bi * bi, b = 2.f * br * bi;
      br = a; bi = b;
      const int k = s == 3 ? 1 : s == 6 ? 2 : s == 8 ? 3 : 0;
      if (k) { cst[k * 128 + lane] = br; cst[k * 128 + 64 + lane] = bi; }
    }
  }
  cs_mm();
  if (c == 0) {
#pragma unroll
    for (int t = 0; t < NWP; ++t) {
      const int col = SSM ? 32 * (t >> 1) + 8 * q + 4 * (t & 1) : 16 * t + 4 * q;
      sf4(LP(L_CS + ((h & 1) * 160 + (h >> 1) * 80 + col) * 4), acs[t]);
    }
  }
  sig(L_CSF + h * 4, 1);
}

DI void carries(const Par& p, const Ids& id, const int h, const int lane, const unsigned tag, WRing& wr) {
  const __amdgpu_buffer_rsrc_t rctl = mkrs(p.ws, WS_CTL_BYTES);
  const int c = lane & 15, q = lane >> 4;
  {
    const int ch = 16 * h + c;
    lwait<2>(L_CSF + 8, 0, 1);
    LAS float* cst = (LAS float*)LP(L_CST);
    const float l8r = cst[128 + ch], l8i = cst[192 + ch], l64r = cst[256 + ch], l64i = cst[320 + ch], lwr = cst[384 + ch], lwi = cst[448 + ch];
    const float kr = cst[512 + ch], ki = cst[576 + ch];
    float w0r = 1.f, w0i = 0.f, w1r = 1.f, w1i = 0.f, qr = kr, qi = ki;
#pragma unroll
    for (int s = 0; s < 6; ++s) {
      float a = w0r, b = w0i; cm1(a, b, lwr, lwi, 0.f, 0.f);
      if (s < id.R - 1 - q) { w0r = a; w0i = b; }
      if (s < id.R - 5 - q) { w1r = a; w1i = b; }
    }
#pragma unroll
    for (int s = 0; s < 3; ++s) { float a = qr, b = qi; cm1(a, b, l64r, l64i, 0.f, 0.f); if (s < q) { qr = a; qi = b; } }
    const __amdgpu_buffer_rsrc_t rag = mkrs(p.ws + WS_AGG + id.R * (8 * 640 * 16), id.R * 640 * 16);
    LAS float* pe = (LAS float*)LP(L_E + 8 * q * ESTR + ch * 4);
    asm volatile("" : "+v"(w0r), "+v"(w0i), "+v"(w1r), "+v"(w1i), "+v"(qr), "+v"(qi));
    unsigned ao[2], po = (q * 640 + 64 * id.g + ch) * 16;
#pragma unroll
    for (int z = 0; z < 2; ++z) ao[z] = q + 4 * z > id.R ? WS_AGG + (((q + 4 * z) * 8 + id.R) * 640 + 64 * id.g + ch) * 16 : OOB;
    asm volatile("" : "+v"(ao[0]), "+v"(ao[1]), "+v"(po));
    __builtin_amdgcn_s_setprio(3);
    HBAR();
    float er[8], ei[8];
#pragma unroll
    for (int s = 0; s < 8; ++s) { er[s] = pe[s * (ESTR / 4)]; ei[s] = pe[s * (ESTR / 4) + 64]; }
    float a = er[0], b = ei[0];
#pragma unroll
    for (int s = 1; s < 8; ++s) cm1(a, b, l8r, l8i, er[s], ei[s]);
    float ta[4], tb[4];
    gather4(a, ta); gather4(b, tb);
    float pa = 0.f, pb = 0.f;
    a = ta[0]; b = tb[0];
#pragma unroll
    for (int s = 1; s < 4; ++s) {
      if (q == s) { pa = a; pb = b; }
      cm1(a, b, l64r, l64i, ta[s], tb[s]);
    }
#pragma unroll
    for (int z = 0; z < 2; ++z)
      st16c(v4u{__float_as_uint(a), tag, __float_as_uint(b), tag}, rctl, ao[z]);
    SB0();
    wr.dma01();
    v4u p0 = ld16c(rag, po, 0), p1 = ld16c(rag, po, 4 * 640 * 16);
    SB0();
    cm1(pa, pb, kr, ki, 0.f, 0.f);
#pragma unroll
    for (int s = 0; s < 7; ++s) cm1(er[s], ei[s], kr, ki, 0.f, 0.f);
    _Pragma("unroll 1") for (int spin = 0; spin < SPIN; ++spin) { FENCE();
      const bool ok = ((q >= id.R) | ((p0.y == tag) & (p0.w == tag))) & ((q + 4 >= id.R) | ((p1.y == tag) & (p1.w == tag)));
      if (__all(ok)) break;
      p0 = ld16c(rag, po, 0); p1 = ld16c(rag, po, 4 * 640 * 16);
    }
    float ca = __uint_as_float(p1.x), cb = __uint_as_float(p1.z);
    cm1(ca, cb, w1r, w1i, 0.f, 0.f);
    { float x = __uint_as_float(p0.x), y = __uint_as_float(p0.z); cm1(x, y, w0r, w0i, ca, cb); ca = x; cb = y; }
    gather4(ca, ta); gather4(cb, tb);
    ca = (ta[0] + ta[1]) + (ta[2] + ta[3]); cb = (tb[0] + tb[1]) + (tb[2] + tb[3]);
    cm1(ca, cb, qr, qi, pa, pb);
#pragma unroll
    for (int s = 0; s < 8; ++s) {
      pe[s * (ESTR / 4)] = ca; pe[s * (ESTR / 4) + 64] = -cb;
      if (s < 7) cm1(ca, cb, l8r, l8i, er[s], ei[s]);
    }
    __builtin_amdgcn_s_setprio(0);
    HBAR();
  }
}

__global__ __launch_bounds__(512, 2) void fused(Par p) {
  const int bid = blockIdx.x, xcd = bid & 7, idx = bid >> 3, a = xcd >> 2, b = xcd & 3, s = idx >> 2;
  const int wv = rfl(threadIdx.x >> 6), lane = threadIdx.x & 63;
  Ids id;
  const bool ssm = idx < 10;
  const int part = s == 2 ? 1 : 0;
  id.R = 4 * a + (idx & 1) + 2 * ((part ? b : idx >> 1) & 1);
  const int gl = (9 - idx) >> 31;
  id.g = ((2 - 3 * gl) * b + s + 3 * gl) * (1 - part) + (8 - 12 * gl + (b >> 1)) * part;
  id.cg = id.g - 10 * gl;
  asm volatile("" ::"s"(p.x), "s"(p.ws), "s"(p.wg), "s"(p.wd), "s"(id.cg), "s"(id.R));
  RowIn rin;
  if (wv >= 4) {
    const int h = wv - 4;
    const unsigned v0 = (8 * (lane >> 3)) * 1280 + (((lane & 7) ^ (lane >> 4)) * 16);
    asm volatile("s_nop 4");
    dma_x(MkSeq<8>::T{}, mkrs(p.x, L_ * D_ * 2), LB + h * 8192, v0, v0 ^ 64, (256 * id.R + 128 * (h >> 1) + 4 * (h & 1)) * 1280);
  }
  unsigned cpf = 0;
  if (wv == 3) {
    unsigned long long pc; unsigned len;
    asm volatile("s_getpc_b64 %0\n.Lpf_here:\n\ts_mov_b32 %1, .Lpf_end-.Lpf_here" : "=s"(pc), "=s"(len));
    cpf = __builtin_amdgcn_raw_buffer_load_b32(mkrs((const void*)pc, len), lane < 13 ? (idx * 13 + lane) * 128 : OOB, 0, 0);
  }
  id.rt = 16 * a + 8 * (b >> 1) + (idx >> 2);
  id.ct = 4 * (b & 1) + (idx & 3);
  const __amdgpu_buffer_rsrc_t rctl = mkrs(p.ws, WS_CTL_BYTES);
  const __amdgpu_buffer_rsrc_t rw = mkrs(p.ws + WS_W2 + (unsigned)id.ct * (54 * 5120), 54 * 5120);
  const unsigned vl = lane * 16;
  const unsigned fo = WS_FLH + (id.rt * 8 + id.ct) * 128 + (lane & 31) * 4;
  WRing wr;
  wr.rw = rw; wr.vl = vl; wr.wv = wv;
  wr.rcr = mkrs(p.cre + id.ct * 80 * P_, 80 * P_ * 2); wr.rci = mkrs(p.cim + id.ct * 80 * P_, 80 * P_ * 2);
  wr.vc = (lane >> 2) * 1280 + (((lane & 3) ^ cswz(lane >> 4)) * 16);
  unsigned tag;
  if (wv < 4) {
    row_load(p, id, wv, lane, rin);
    if (wv == 2) cpf = ld4c(rctl, lane < 32 ? fo : lane < 40 ? WS_FLW + (lane - 32) * 128 : OOB);
    tag = ssm ? compute_wave<true>(p, id, wv, lane, rin) : compute_wave<false>(p, id, wv, lane, rin);
    WAITV(0);
    unsigned old = 0;
    if (lane == 0) old = __hip_atomic_fetch_add((LAS unsigned*)LP(L_CNT), 1u, __ATOMIC_RELAXED, __HIP_MEMORY_SCOPE_WORKGROUP);
    st4c(tag, rctl, (lane < 32 && rfl(old) == 3) ? WS_FLH + (id.R * 32 + lane) * 128 + id.cg * 4 : OOB);
    asm volatile("" ::"v"(cpf));
#pragma unroll
    for (int it = 0; it < DW; ++it) wr.load(it);
  } else {
    HState hs;
    tag = ssm ? helper_wave<true>(p, id, wv - 4, lane, hs) : helper_wave<false>(p, id, wv - 4, lane, hs);
    if (ssm) {
      helper_tail<true>(p, id, wv - 4, lane, tag, hs);
      _Pragma("unroll 1") for (int spin = 0; spin < SPIN; ++spin) { FENCE();
        const v4u f2 = ld16c(rctl, WS_FLW + lane * 16, 0);
        if (__all((f2.x == tag) & (f2.y == tag) & (f2.z == tag) & (f2.w == tag))) break;
        __builtin_amdgcn_s_sleep(4);
      }
    }
    wr.load(0); wr.load(1); wr.load(2);
    if (!ssm) helper_tail<false>(p, id, wv - 4, lane, tag, hs);
#pragma unroll
    for (int it = 3; it < DW; ++it) wr.load(it);
    if (ssm) carries(p, id, wv - 4, lane, tag, wr); else wr.dma01();
    lwait<2>(L_CNT, 0, 4);
    if (wv == 4) {
      _Pragma("unroll 1") for (int spin = 0; spin < SPIN; ++spin) { FENCE();
        if (__all((ld4c(rctl, fo) == tag) | ((lane & 31) < 10))) break;
        __builtin_amdgcn_s_sleep(2);
      }
      sig(L_GO, 1);
    }
    wr.dma2();
  }

  const __amdgpu_buffer_rsrc_t rh = mkrs(p.ws + WS_H + (unsigned)id.rt * (NKB * 4096), NKB * 4096);
  v4u hb[NIT][4];
  auto loadh1 = [&](int it, int j) {
    const unsigned so = it < 6 ? (8 * it + wv) * 4096 : it == 6 ? (wv < 6 ? (48 + wv) * 4096 : OOB) : (8 * it - 2 + wv) * 4096;
    hb[it][j] = ld16c(rh, vl + j * 1024, so);
  };
  lwait<1>(L_GO, 0, 1);
  SB0();
#pragma unroll
  for (int z = 0; z < 4 * DH; ++z) { loadh1(z >> 2, z & 3); SB0(); }
  f32x4 acc[4][5];
#pragma unroll
  for (int j = 0; j < 4; ++j)
#pragma unroll
    for (int i = 0; i < 5; ++i) acc[j][i] = f32x4{0.f, 0.f, 0.f, 0.f};
  const int tid = wv * 64 + lane;
  v2u xv[3], dv[3]; v4u sv[3]; int oo[3], ra[3];
  unsigned sfl = 0;
  const unsigned cr = wv * 5120 + (lane & 15) * 64 + (((lane >> 4) ^ cswz((lane >> 2) & 3)) * 16);
  const lp pw = LP(L_W + cr), pw2 = LP(cr);
  v4u wl[5][5];
  SB0();
  auto epi_load = [&](int r) {
    const int sl = min(tid + 512 * r, 1279), row = sl / 20, qd = sl - 20 * row;
    const int tok = 128 * (id.rt >> 1) + 8 * (row >> 2) + 4 * (id.rt & 1) + (row & 3), d = 80 * id.ct + 4 * qd;
    oo[r] = tok * D_ + d;
    ra[r] = (row & 3) * 40960 + (qd >> 2) * 1024 + (qd & 3) * 256 + (((row >> 2) + qd) & 15) * 16;
    xv[r] = *(const v2u*)(p.x + oo[r]); dv[r] = *(const v2u*)(p.dsk + d);
    sv[r] = ld16c(rctl, WS_STATS + tok * 16, 0);
  };
  auto slab = [&](int j) {
#pragma unroll
    for (int i = 0; i < 5; ++i) sf4(LP(j * 40960 + wv * 5120 + i * 1024 + (lane >> 4) * 256 + (((lane & 15) + 4 * i + (lane >> 4)) & 15) * 16), acc[j][i]);
  };
  auto round = [&](auto IT) __attribute__((always_inline)) {
    constexpr int it = IT.value;
    if (it + DH == NG) {
      if (__builtin_expect(!__all(sfl == tag), 0)) {
        _Pragma("unroll 1") for (int spin = 0; spin < SPIN; ++spin) { FENCE();
          if (__all(ld4c(rctl, fo) == tag)) break;
          __builtin_amdgcn_s_sleep(2);
        }
      }
      FENCE();
    }
    if constexpr (it + 1 >= NG && it + 1 < NIT) {
      constexpr int nr = it + 1 - NG;
      if (nr == 4) WAITV(6);
#pragma unroll
      for (int i = 0; i < 5; ++i) wl[nr][i] = l128(nr == 2 ? pw2 + 40960 + i * 1024 : nr == 3 ? pw2 + i * 1024 : pw + (nr & 1) * 40960 + i * 1024);
      SB0();
    }
#pragma unroll
    for (int j = 0; j < 4; ++j) {
#pragma unroll
      for (int i = 0; i < 5; ++i) {
        acc[j][i] = mfma16(it >= NG ? wl[it >= NG ? it - NG : 0][i] : wr.wb[it % DW][i], hb[it][j], acc[j][i]);
        if (j == 3 && it + DW < NG) { SB0(); wr.load1(it + DW, i); SB0(); }
        if (i == 2) {
          SB0();
          if (it == 3 && j == 0) wr.template refill<0, 0>();
          if (it == 3 && j == 1) wr.template refill<0, 1>();
          if (it == 3 && j == 2) wr.template refill<0, 2>();
          if (it == 4 && j == 0) wr.template refill<0, 3>();
          if (it == 4 && j == 2) wr.template refill<0, 4>();
          if (it == 2 && j == 1) sfl = ld4c(rctl, fo);
          if (it == 5 && j == 1) epi_load(0);
          if (it == 6 && j == 1) epi_load(1);
          if (it == 8 && j == 1) epi_load(2);
          if (it == 7 && j == 1) wr.template refill<1, 0>();
          if (it == 7 && j == 2) wr.template refill<1, 1>();
          if (it == 7 && j == 3) wr.template refill<1, 2>();
          if (it == 8 && j == 0) wr.template refill<1, 3>();
          if (it == 8 && j == 2) wr.template refill<1, 4>();
          SB0();
        }
      }
      SB0();
      if constexpr (it + DH < NIT) loadh1(it + DH, j);
      if (it == NIT - 1 && j > 0) slab(j - 1);
      SB0();
    }
    if (it == 2) BAR();
    SB0();
  };
  for_seq(MkSeq<NIT>::T{}, round);
  asm volatile("" ::"v"(sv[0]), "v"(sv[1]), "v"(sv[2]), "v"(xv[2]), "v"(dv[2]));
  slab(3);
  BAR();
  asm volatile(".Lpf_end:");
#pragma unroll
  for (int r = 0; r < 3; ++r) {
    if (r < 2 || wv < 4) {
      const lp q0 = LP(ra[r]);
      f32x4 sum = ((lf4(q0) + lf4(q0 + 5120)) + (lf4(q0 + 10240) + lf4(q0 + 15360))) + ((lf4(q0 + 20480) + lf4(q0 + 25600)) + (lf4(q0 + 30720) + lf4(q0 + 35840)));
      const f32x4 xf = {bflo(xv[r].x), bfhi(xv[r].x), bflo(xv[r].y), bfhi(xv[r].y)};
      const f32x4 df = {bflo(dv[r].x), bfhi(dv[r].x), bflo(dv[r].y), bfhi(dv[r].y)};
      sum += xf + (xf - __uint_as_float(sv[r].x)) * __uint_as_float(sv[r].z) * df;
      __builtin_amdgcn_raw_buffer_store_b128(__builtin_bit_cast(v4u, sum), mkrs(p.out, L_ * D_ * 4), oo[r] * 4, 0, 16);
    }
  }
}

extern "C" void kernel_launch(void* const* d_in, const int* in_sizes, int n_in, void* d_out, int out_size, void* d_ws, size_t ws_size, hipStream_t stream) {
  Par p;
  p.x = (const u16*)d_in[0]; p.lre = (const u16*)d_in[1]; p.lim = (const u16*)d_in[2];
  p.bre = (const u16*)d_in[3]; p.bim = (const u16*)d_in[4]; p.cre = (const u16*)d_in[5]; p.cim = (const u16*)d_in[6];
  p.ldt = (const u16*)d_in[7]; p.dsk = (const u16*)d_in[8];
  p.wg = (const u16*)d_in[10]; p.wu = (const u16*)d_in[11]; p.wd = (const u16*)d_in[12];
  p.ws = (unsigned char*)d_ws; p.out = (float*)d_out;
  (void)hipFuncSetAttribute((const void*)fused, hipFuncAttributeMaxDynamicSharedMemorySize, LDS1);
  fused<<<256, 512, LDS1, stream>>>(p);
}
```
